```python
import torch

def _softmax(x, axis=-1):
    return torch.softmax(x, dim=axis)

def _gelu(x):
    return torch.nn.functional.gelu(x, approximate='tanh')

def _silu(x):
    return torch.nn.functional.silu(x)

def _sigmoid(x):
    return torch.sigmoid(x)

def _layernorm(x, axis=-1, eps=1e-05):
    m = x.mean(dim=axis, keepdim=True)
    v = x.var(dim=axis, keepdim=True, unbiased=False)
    return (x - m) / torch.sqrt(v + eps)

def _scatter_add(tgt, idx, src, axis=0):
    return tgt.clone().index_add_(axis, idx, src)

def _segment_sum(src, seg, n):
    out = torch.zeros((n,) + src.shape[1:], dtype=src.dtype)
    return out.index_add_(0, seg, src)

def _pad(x, pw):
    flat = tuple((v for pair in reversed(pw) for v in pair))
    return torch.nn.functional.pad(x, flat)
kernel_name = 'concat_heads__linear_kernel_hedgehog__memory_aug_recurrent_mem_tr__prompt'

def setup_inputs():

    _w = lambda a: (lambda t: t.to(torch.bfloat16) if t.is_floating_point() else t)(torch.as_tensor(a))

    def _A_setup():
        B, S, D, H = (1, 2048, 448, 16)
        Dh = D // H
        return {'x': _w((lambda *s: torch.randn(*s, dtype=torch.bfloat16) * 0.4)(B, S, D)), 'wqkv': _w((lambda *s: torch.randn(*s, dtype=torch.bfloat16) * 0.4)(D, 3 * D)), 'w_phi': _w((lambda *s: torch.randn(*s, dtype=torch.bfloat16) * 0.4)(H, Dh, Dh) * Dh ** (-0.5)), 'wo': _w((lambda *s: torch.randn(*s, dtype=torch.bfloat16) * 0.05)(D, D))}
    _A_in = _A_setup()

    def _B_setup():
        B, S, Dm = (1, 2048, 448)
        H, Dh = (16, 256)
        M, seg = (8, 32)
        seg = min(seg, S)
        S = S // seg * seg
        return {'x': _w((lambda *s: torch.randn(*s, dtype=torch.bfloat16) * 0.05)(B, S, Dm)), 'mem0': _w((lambda *s: torch.randn(*s, dtype=torch.bfloat16) * 0.8)(B, M, Dm)), 'Wq': _w((lambda *s: torch.randn(*s, dtype=torch.bfloat16) * 1.6)(Dm, H, Dh) * Dm ** (-0.5)), 'Wk': _w((lambda *s: torch.randn(*s, dtype=torch.bfloat16) * 1.6)(Dm, H, Dh) * Dm ** (-0.5)), 'Wv': _w((lambda *s: torch.randn(*s, dtype=torch.bfloat16) * 0.8)(Dm, H, Dh) * Dm ** (-0.5)), 'Wo': _w((lambda *s: torch.randn(*s, dtype=torch.bfloat16) * 0.8)(H, Dh, Dm) * (H * Dh) ** (-0.5)), 'seg_len': seg}
    _B_in = _B_setup()
    return {'A_x': _A_in['x'], 'A_wqkv': _A_in['wqkv'], 'A_w_phi': _A_in['w_phi'], 'A_wo': _A_in['wo'], 'B_x': _B_in['x'], 'B_mem0': _B_in['mem0'], 'B_Wq': _B_in['Wq'], 'B_Wk': _B_in['Wk'], 'B_Wv': _B_in['Wv'], 'B_Wo': _B_in['Wo'], 'B_seg_len': _B_in['seg_len']}

def reference(A_x, A_wqkv, A_w_phi, A_wo, B_x, B_mem0, B_Wq, B_Wk, B_Wv, B_Wo, B_seg_len):

    def _A_ref(x, wqkv, w_phi, wo):
        B, S, D = x.shape
        H = 16
        Dh = D // H
        qkv = (lambda x, *d: x.permute(*d))((x @ wqkv).reshape(B, S, 3, H, Dh), 2, 0, 3, 1, 4)
        q, k, v = (qkv[0], qkv[1], qkv[2])
        pq = torch.einsum('bhsd,hdf->bhsf', q, w_phi)
        pk = torch.einsum('bhsd,hdf->bhsf', k, w_phi)
        phi_q = torch.concatenate([_softmax(pq, axis=-1), _softmax(-pq, axis=-1)], axis=-1)
        phi_k = torch.concatenate([_softmax(pk, axis=-1), _softmax(-pk, axis=-1)], axis=-1)
        kv = torch.einsum('bhsf,bhsd->bhfd', phi_k, v)
        z = torch.einsum('bhsf->bhf', phi_k)
        num = torch.einsum('bhqf,bhfd->bhqd', phi_q, kv)
        den = torch.einsum('bhqf,bhf->bhq', phi_q, z)[..., None] + 1e-06
        o = num / den
        return (lambda x, *d: x.permute(*d))(o, 0, 2, 1, 3).reshape(B, S, D) @ wo

    def _B_ref(x, mem0, Wq, Wk, Wv, Wo, seg_len):
        B, S, Dm = x.shape
        H, Dh = (Wq.shape[1], Wq.shape[2])
        M = mem0.shape[1]
        scale = Dh ** (-0.5)
        n_seg = S // seg_len
        L = M + seg_len
        idx = torch.arange(L)
        mask = (idx[:, None] < M) | (idx[:, None] >= idx[None, :])
        mem = mem0
        outs = []
        for s in range(n_seg):
            xs = x[:, s * seg_len:(s + 1) * seg_len, :].clone()
            h = torch.concatenate([mem, xs], axis=1)
            q = torch.einsum('btd,dhe->bhte', h, Wq)
            k = torch.einsum('btd,dhe->bhte', h, Wk)
            v = torch.einsum('btd,dhe->bhte', h, Wv)
            scores = torch.einsum('bhqe,bhke->bhqk', q, k) * scale
            scores = torch.where(mask[None, None], scores, -1000000000.0)
            p = _softmax(scores, axis=-1)
            o = torch.einsum('bhqk,bhke->bhqe', p, v)
            o = torch.einsum('bhte,hed->btd', o, Wo)
            mem = o[:, :M, :].clone()
            outs.append(o[:, M:, :])
        return torch.concatenate(outs, axis=1)
    a = _A_ref(A_x, A_wqkv, A_w_phi, A_wo)
    b = _B_ref(B_x, B_mem0, B_Wq, B_Wk, B_Wv, B_Wo, B_seg_len)
    return torch.concatenate([a, b], axis=-1)
```

```cpp
#include <hip/hip_runtime.h>
#include <hip/hip_bf16.h>
#include <stdint.h>

typedef unsigned short u16;
typedef unsigned v4u __attribute__((ext_vector_type(4)));
typedef unsigned v2u __attribute__((ext_vector_type(2)));
typedef float f4 __attribute__((ext_vector_type(4)));
typedef float f2 __attribute__((ext_vector_type(2)));
typedef short s8 __attribute__((ext_vector_type(8)));
typedef __bf16 bf2 __attribute__((ext_vector_type(2)));
#define DI __device__ __forceinline__

constexpr int D = 448, H = 16, DH = 256, MEM = 8, SEG = 32, NSEG = 64, S_TOK = 2048;
constexpr int NKS = 14;
constexpr int OUTW = 896;
constexpr unsigned SENT = 0xFFFFFFFFu;
constexpr float LOG2E = 1.4426950408889634f;

constexpr size_t MB = 1024 * 1024;
constexpr int KCOPY = 2;
constexpr size_t COMM_OFF = 0;
constexpr size_t COMM_BYTES = 8 * MB;
constexpr size_t CTRL_OFF = COMM_BYTES;
constexpr size_t AS_OFF = CTRL_OFF + 65536;
constexpr size_t AS_BYTES = (size_t)16 * 14 * 2 * 14 * 1024;
constexpr size_t BS_OFF = AS_OFF + AS_BYTES;
constexpr size_t BS_BYTES = (size_t)56 * 8 * 14 * 1024;
constexpr size_t XF_OFF = BS_OFF + BS_BYTES;
constexpr size_t XF_BYTES = (size_t)64 * 28 * 1024;
constexpr size_t G_OFF = XF_OFF + XF_BYTES;
constexpr size_t G_BYTES = (size_t)64 * 16 * 28 * 1024;
constexpr size_t U_OFF = G_OFF + G_BYTES;
constexpr size_t U_BYTES = (size_t)64 * 56 * 8 * 1024;
constexpr size_t GM_OFF = U_OFF + U_BYTES;
constexpr size_t GM_BYTES = (size_t)64 * 16 * 14 * 512;
constexpr size_t VP_OFF = GM_OFF + GM_BYTES;
constexpr size_t VP_BYTES = (size_t)64 * 56 * 4 * 512;
constexpr size_t PA_OFF = VP_OFF + VP_BYTES;
constexpr size_t WS_END = PA_OFF + 32 * MB;

DI unsigned pk2(float a, float b) { f2 v = {a, b}; return __builtin_bit_cast(unsigned, __builtin_convertvector(v, bf2)); }
DI float asf(unsigned x) { return __builtin_bit_cast(float, x); }
DI unsigned asu(float x) { return __builtin_bit_cast(unsigned, x); }
DI float bflo(unsigned x) { return __builtin_bit_cast(float, x << 16); }
DI float bfhi(unsigned x) { return __builtin_bit_cast(float, x & 0xffff0000u); }
DI float bf2f(u16 x) { return __builtin_bit_cast(float, (unsigned)x << 16); }
DI f4 mfma(v4u a, v4u b, f4 c) { return __builtin_amdgcn_mfma_f32_16x16x32_bf16(__builtin_bit_cast(s8, a), __builtin_bit_cast(s8, b), c, 0, 0, 0); }
DI v4u pack8(f4 a, f4 b) { v4u r; r.x = pk2(a[0], a[1]); r.y = pk2(a[2], a[3]); r.z = pk2(b[0], b[1]); r.w = pk2(b[2], b[3]); return r; }
DI v4u nosent(v4u v) { v.x = min(v.x, 0xFFFFFFFEu); v.y = min(v.y, 0xFFFFFFFEu); v.z = min(v.z, 0xFFFFFFFEu); v.w = min(v.w, 0xFFFFFFFEu); return v; }
DI int perm32(int t, int rho) { return 8 * (rho >> 2) + 4 * t + (rho & 3); }
DI __amdgpu_buffer_rsrc_t mkrs(void* p, unsigned bytes) { return __builtin_amdgcn_make_buffer_rsrc(p, 0, bytes, 0x00020000); }
DI v4u ld_sc1(__amdgpu_buffer_rsrc_t rs, unsigned voff, unsigned soff) { return __builtin_amdgcn_raw_buffer_load_b128(rs, voff, soff, 16); }
DI v2u ld2_sc1(__amdgpu_buffer_rsrc_t rs, unsigned voff, unsigned soff) { return __builtin_amdgcn_raw_buffer_load_b64(rs, voff, soff, 16); }
DI void st_sc1(v4u v, __amdgpu_buffer_rsrc_t rs, unsigned voff, unsigned soff) { __builtin_amdgcn_raw_buffer_store_b128(v, rs, voff, soff, 16); }
DI void st2_sc1(v2u v, __amdgpu_buffer_rsrc_t rs, unsigned voff, unsigned soff) { __builtin_amdgcn_raw_buffer_store_b64(v, rs, voff, soff, 16); }
DI unsigned long long mtime() { unsigned long long t; asm volatile("s_memtime %0\n\ts_waitcnt lgkmcnt(0)" : "=s"(t) :: "memory"); return t; }

DI unsigned comm_step_base(int step, int copy) { return __builtin_amdgcn_readfirstlane((unsigned)step * 131072u + (unsigned)copy * 65536u); }
DI unsigned comm_off(int chunk0, unsigned o, unsigned pol) { return (unsigned)(chunk0 + (o >> 13)) * 16384u + pol * 8192u + (o & 8191u); }

__global__ void __launch_bounds__(256) k_init(unsigned char* ws, const u16* __restrict__ bx, const u16* __restrict__ mem0) {
    const int tid = threadIdx.x, b = blockIdx.x;
    __amdgpu_buffer_rsrc_t rs = mkrs(ws, 0x7fffffff);
    const int gt = b * 256 + tid, GN = gridDim.x * 256;
    if ((b & 7) == 0 && b < 32 && tid < 64) {
        const int page = b >> 3;
        unsigned base = (unsigned)page * 2097152u + 3u * 16384u + 4096u;
        unsigned best[2] = {0xffffffffu, 0xffffffffu};
        for (int r = 0; r < 4; ++r)
            for (int p = 0; p < 2; ++p) {
                v4u z = {SENT, SENT, SENT, SENT};
                unsigned off = base + p * 8192u + (r & 1) * 128u;
                unsigned long long t0 = mtime();
                if (tid < 8) st_sc1(z, rs, off + tid * 16, 0);
                asm volatile("s_waitcnt vmcnt(0)" ::: "memory");
                unsigned long long t1 = mtime();
                unsigned dt = (unsigned)(t1 - t0);
                if (r > 0) best[p] = min(best[p], dt);
            }
        if (tid == 0) ((unsigned*)(ws + CTRL_OFF))[page] = best[1] < best[0] ? 1u : 0u;
    }
    for (unsigned i = gt; i < COMM_BYTES / 16; i += GN) {
        unsigned off = i * 16;
        unsigned within = off & 131071u, step = off >> 17;
        unsigned chunk = (within >> 14) & 3, o = within & 8191u;
        if (chunk == 0 && o >= 7168) continue;
        if (chunk == 3 && o >= 2048) continue;
        v4u v = {SENT, SENT, SENT, SENT};
        if (step == 0 && chunk == 0) {
            int j = o >> 7, irow = (o >> 4) & 7;
            v = *(const v4u*)(mem0 + irow * D + j * 8);
            v = nosent(v);
        }
        st_sc1(v, rs, off, 0);
    }
    for (unsigned i = gt; i < (GM_BYTES + VP_BYTES) / 16; i += GN) {
        v4u v = {SENT, SENT, SENT, SENT};
        st_sc1(v, rs, (unsigned)GM_OFF + i * 16, 0);
    }
    for (unsigned i = gt; i < XF_BYTES / 16; i += GN) {
        int lane = i & 63; int f = i >> 6; int ks = f % 14; int st = f / 14; int tau = st & 1, seg = st >> 1;
        int tok = seg * 32 + perm32(tau, lane & 15);
        v4u v = *(const v4u*)(bx + (size_t)tok * D + ks * 32 + (lane >> 4) * 8);
        *(v4u*)(ws + XF_OFF + (size_t)i * 16) = v;
    }
}

__global__ void __launch_bounds__(448) k_ab(unsigned char* ws, const u16* __restrict__ Wq, const u16* __restrict__ Wk, const u16* __restrict__ Wv, const u16* __restrict__ Wo) {
    extern __shared__ __attribute__((aligned(16))) unsigned char lds[];
    const int tid = threadIdx.x, lane = tid & 63, w = __builtin_amdgcn_readfirstlane(tid >> 6);
    const int wg = blockIdx.x;
    const int mat = wg >> 7, h = (wg >> 3) & 15, rh = (wg >> 2) & 1, q = wg & 3;
    const int rho = lane & 15, g = lane >> 4;
    const int rb = 7 * rh + w;
    const u16* Wr = mat ? Wv : Wk;
    v4u a[2][8];
#pragma unroll
    for (int t = 0; t < 2; ++t) {
        const int row = 32 * rb + perm32(t, rho);
#pragma unroll
        for (int es = 0; es < 8; ++es) a[t][es] = *(const v4u*)(Wr + ((size_t)row * H + h) * DH + es * 32 + g * 8);
    }
    if (mat == 0) {
        const int ct = 7 * q + w; const int d = 32 * (ct >> 1) + perm32(ct & 1, rho);
#pragma unroll
        for (int es = 0; es < 8; ++es) *(v4u*)(lds + (w * 8 + es) * 1024 + lane * 16) = *(const v4u*)(Wq + ((size_t)d * H + h) * DH + es * 32 + g * 8);
    } else {
        for (int idx = tid; idx < 256 * 14; idx += 448) {
            int e = idx / 14, c8 = idx % 14;
            v4u v = *(const v4u*)(Wo + ((size_t)h * DH + e) * D + 112 * q + c8 * 8);
            const u16* pv = (const u16*)&v;
#pragma unroll
            for (int k = 0; k < 8; ++k) {
                int cl = c8 * 8 + k;
                *(u16*)(lds + ((cl >> 4) * 8 + (e >> 5)) * 1024 + ((cl & 15) + 16 * ((e & 31) >> 3)) * 16 + (e & 7) * 2) = pv[k];
            }
        }
    }
    __syncthreads();
    f4 acc[2][7];
#pragma unroll
    for (int t = 0; t < 2; ++t)
#pragma unroll
        for (int c = 0; c < 7; ++c) acc[t][c] = f4{0, 0, 0, 0};
#pragma unroll
    for (int es = 0; es < 8; ++es)
#pragma unroll
        for (int c = 0; c < 7; ++c) {
            v4u bfr = *(const v4u*)(lds + (c * 8 + es) * 1024 + lane * 16);
            acc[0][c] = mfma(a[0][es], bfr, acc[0][c]);
            acc[1][c] = mfma(a[1][es], bfr, acc[1][c]);
        }
    if (mat == 0) {
        const float sc = LOG2E * 0.0625f;
#pragma unroll
        for (int c = 0; c < 7; ++c) {
            const int ct = 7 * q + c;
            v4u o = pack8(acc[0][c] * sc, acc[1][c] * sc);
            *(v4u*)(ws + AS_OFF + ((size_t)((h * 14 + (ct >> 1)) * 2 + (ct & 1)) * 14 + rb) * 1024 + lane * 16) = o;
        }
    } else {
#pragma unroll
        for (int c = 0; c < 7; ++c) {
            const int ct = 7 * q + c;
            const int j = 2 * ct + (rho >> 3);
            v4u o = pack8(acc[0][c], acc[1][c]);
            *(v4u*)(ws + BS_OFF + ((size_t)(j * 8 + (h >> 1)) * 14 + rb) * 1024 + (((h & 1) * 8 + (rho & 7)) + 16 * g) * 16) = o;
        }
    }
}

__global__ void __launch_bounds__(256, 1) k_gu(unsigned char* ws) {
    extern __shared__ __attribute__((aligned(16))) unsigned char lds[];
    const int tid = threadIdx.x, lane = tid & 63, w = __builtin_amdgcn_readfirstlane(tid >> 6);
    const int gw = blockIdx.x;
    const bool isG = gw < 56;
    v4u wt[2][14];
    const unsigned char* wsrc = isG ? ws + AS_OFF + (size_t)(4 * gw + w) * 28 * 1024 : ws + BS_OFF + ((size_t)(gw - 56) * 8 + 2 * w) * 14 * 1024;
#pragma unroll
    for (int t = 0; t < 2; ++t)
#pragma unroll
        for (int ks = 0; ks < 14; ++ks) wt[t][ks] = *(const v4u*)(wsrc + (t * 14 + ks) * 1024 + lane * 16);
    __amdgpu_buffer_rsrc_t rs = mkrs(ws, 0x7fffffff);
    v4u stg[7];
#pragma unroll
    for (int i = 0; i < 7; ++i) stg[i] = *(const v4u*)(ws + XF_OFF + (i * 256 + tid) * 16);
#pragma unroll
    for (int i = 0; i < 7; ++i) *(v4u*)(lds + (i * 256 + tid) * 16) = stg[i];
    __syncthreads();
    for (int seg = 0; seg < NSEG; ++seg) {
        const unsigned char* cur = lds + (seg & 1) * 28672;
        if (seg + 1 < NSEG) {
#pragma unroll
            for (int i = 0; i < 7; ++i) stg[i] = *(const v4u*)(ws + XF_OFF + (size_t)(seg + 1) * 28672 + (i * 256 + tid) * 16);
        }
        f4 acc[2][2] = {{f4{0,0,0,0}, f4{0,0,0,0}}, {f4{0,0,0,0}, f4{0,0,0,0}}};
        if (isG) {
#pragma unroll
            for (int ks = 0; ks < 14; ++ks) {
                v4u x0 = *(const v4u*)(cur + ks * 1024 + lane * 16), x1 = *(const v4u*)(cur + (14 + ks) * 1024 + lane * 16);
                acc[0][0] = mfma(wt[0][ks], x0, acc[0][0]); acc[1][0] = mfma(wt[1][ks], x0, acc[1][0]);
                acc[0][1] = mfma(wt[0][ks], x1, acc[0][1]); acc[1][1] = mfma(wt[1][ks], x1, acc[1][1]);
            }
            const int bb = 4 * gw + w, h = bb / 14, b = bb % 14;
#pragma unroll
            for (int tau = 0; tau < 2; ++tau)
                st_sc1(pack8(acc[0][tau], acc[1][tau]), rs, (unsigned)(G_OFF + ((size_t)((seg * 16 + h) * 2 + tau) * 14 + b) * 1024) + lane * 16, 0);
        } else {
#pragma unroll
            for (int ks = 0; ks < 14; ++ks) {
                v4u x0 = *(const v4u*)(cur + ks * 1024 + lane * 16), x1 = *(const v4u*)(cur + (14 + ks) * 1024 + lane * 16);
                acc[0][0] = mfma(x0, wt[0][ks], acc[0][0]); acc[1][0] = mfma(x1, wt[0][ks], acc[1][0]);
                acc[0][1] = mfma(x0, wt[1][ks], acc[0][1]); acc[1][1] = mfma(x1, wt[1][ks], acc[1][1]);
            }
            const int j = gw - 56;
#pragma unroll
            for (int u = 0; u < 2; ++u)
                st_sc1(pack8(acc[0][u], acc[1][u]), rs, (unsigned)(U_OFF + ((size_t)(seg * 56 + j) * 8 + 2 * w + u) * 1024) + lane * 16, 0);
        }
        if (seg + 1 < NSEG) {
            unsigned char* nxt = lds + ((seg + 1) & 1) * 28672;
#pragma unroll
            for (int i = 0; i < 7; ++i) *(v4u*)(nxt + (i * 256 + tid) * 16) = stg[i];
        }
        __syncthreads();
    }
}

DI void delay64(int n) { for (int i = 0; i < n; ++i) __builtin_amdgcn_s_sleep(1); }
DI unsigned umax3(unsigned a, unsigned b, unsigned c) { return max(max(a, b), c); }
DI unsigned vmax4(unsigned t, v4u v) { return umax3(umax3(t, v.x, v.y), v.z, v.w); }
DI float dpp_xor8(float v) {
    return __builtin_bit_cast(float, __builtin_amdgcn_update_dpp(0, __builtin_bit_cast(int, v), 0x128, 0xf, 0xf, false));
}
DI unsigned dpp_xor8u(unsigned v) { return (unsigned)__builtin_amdgcn_update_dpp(0, (int)v, 0x128, 0xf, 0xf, false); }
constexpr unsigned SPIN_MAX = 200000;

DI void gather_m(__amdgpu_buffer_rsrc_t rs, int step, unsigned pol, unsigned char* ldsm, int tid) {
    const unsigned o0 = tid * 16, o1 = (256 + tid) * 16;
    const bool has1 = o1 < 7168;
    const unsigned v0 = comm_off(0, o0, pol), v1 = comm_off(0, has1 ? o1 : o0, pol);
    int copy = 0;
    for (unsigned spin = 0;; ++spin) {
        asm volatile("" ::: "memory");
        const unsigned sb = comm_step_base(step, copy);
        v4u a = ld_sc1(rs, v0, sb), b = ld_sc1(rs, v1, sb);
        unsigned t = vmax4(vmax4(0, a), b);
        if (__all(t != SENT) || spin > SPIN_MAX) {
            *(v4u*)(ldsm + o0) = a;
            if (has1) *(v4u*)(ldsm + o1) = b;
            break;
        }
        copy = KCOPY - 1;
    }
}

struct RecParams { int dS, dV, dVm; };

__global__ void __launch_bounds__(256, 1) k_rec(unsigned char* ws, int dS, int dV, int dVm) {
    extern __shared__ __attribute__((aligned(16))) unsigned char lds[];
    const int bidx = blockIdx.x;
    if ((bidx & 7) >= 4) return;
    const int role = (bidx >> 3) * 4 + (bidx & 3);
    if (role >= 120) return;
    const int tid = threadIdx.x, lane = tid & 63, w = __builtin_amdgcn_readfirstlane(tid >> 6);
    const int rho = lane & 15, g = lane >> 4;
    __amdgpu_buffer_rsrc_t rs = mkrs(ws, 0x7fffffff);
    unsigned char* ldsm = lds;
    float* red = (float*)(lds + 8192);
    const unsigned* polp = (const unsigned*)(ws + CTRL_OFF);
    const unsigned polmask = polp[0] | (polp[1] << 1) | (polp[2] << 2) | (polp[3] << 3);
    const unsigned mfrag_off = g * 128 + (rho & 7) * 16;

    if (role < 64) {
        const int h = role >> 2, q = role & 3;
        const bool is_mm = (q < 3) || (w < 2);
        const bool is_mx = (q == 3) && (w == 2);
        const int b = 4 * q + w;
        v4u wt[2][14];
        if (is_mm) {
            const unsigned char* src = ws + AS_OFF + (size_t)(h * 14 + b) * 28 * 1024;
#pragma unroll
            for (int t = 0; t < 2; ++t)
#pragma unroll
                for (int ks = 0; ks < 14; ++ks) wt[t][ks] = *(const v4u*)(src + (t * 14 + ks) * 1024 + lane * 16);
        } else if (is_mx) {
#pragma unroll
            for (int t = 0; t < 2; ++t)
#pragma unroll
                for (int ks = 0; ks < 14; ++ks) wt[t][ks] = ld_sc1(rs, (unsigned)(G_OFF + ((size_t)h * 28 + t * 14 + ks) * 1024) + lane * 16, 0);
        } else {
#pragma unroll
            for (int t = 0; t < 2; ++t)
#pragma unroll
                for (int ks = 0; ks < 14; ++ks) wt[t][ks] = v4u{0, 0, 0, 0};
        }
        for (int step = 0; step < NSEG; ++step) {
            const unsigned pol = (polmask >> (step >> 4)) & 1;
            if (step > 0) delay64(dS);
            gather_m(rs, step, pol, ldsm, tid);
            __syncthreads();
            f4 a0 = {0, 0, 0, 0}, a1 = {0, 0, 0, 0};
            v4u mb;
#pragma unroll
            for (int ks = 0; ks < 14; ++ks) {
                v4u mf = *(const v4u*)(ldsm + ks * 512 + mfrag_off);
                a0 = mfma(wt[0][ks], mf, a0);
                a1 = mfma(wt[1][ks], mf, a1);
            }
            const bool last = (step == NSEG - 1);
            if (is_mm) {
                mb = *(const v4u*)(ldsm + b * 512 + mfrag_off);
                v4u gm = pack8(a0, a1);
                f4 sp = mfma(mb, gm, f4{0, 0, 0, 0});
                if (g < 2 && rho < 8) {
#pragma unroll
                    for (int r = 0; r < 4; ++r) red[w * 64 + (4 * g + r) * 8 + rho] = sp[r];
                }
                __syncthreads();
                if (w == 0 && !last) {
                    const int nw = (q < 3) ? 4 : 2;
                    if (lane < 8 * KCOPY) {
                        const int i = lane & 7, cp = lane >> 3;
                        float v[8];
#pragma unroll
                        for (int j = 0; j < 8; ++j) v[j] = red[i * 8 + j] + red[64 + i * 8 + j];
                        if (nw == 4) {
#pragma unroll
                            for (int j = 0; j < 8; ++j) v[j] += red[128 + i * 8 + j] + red[192 + i * 8 + j];
                        }
                        v4u o; o.x = pk2(v[0], v[1]); o.y = pk2(v[2], v[3]); o.z = pk2(v[4], v[5]); o.w = pk2(v[6], v[7]);
                        st_sc1(nosent(o), rs, comm_off(1, h * 1152 + q * 128 + i * 16, pol) + cp * 65536u, comm_step_base(step, 0));
                    }
                }
                if (rho < 8) st_sc1(nosent(gm), rs, (unsigned)(GM_OFF + ((size_t)(step * 16 + h) * 14 + b) * 512) + g * 128 + rho * 16, 0);
            } else if (is_mx) {
                if (!last) {
                float mx = fmaxf(fmaxf(fmaxf(a0[0], a0[1]), fmaxf(a0[2], a0[3])), fmaxf(fmaxf(a1[0], a1[1]), fmaxf(a1[2], a1[3])));
                mx = fmaxf(mx, __shfl_xor(mx, 16));
                mx = fmaxf(mx, __shfl_xor(mx, 32));
                f4 e0, e1; float sum = 0.f;
#pragma unroll
                for (int r = 0; r < 4; ++r) { e0[r] = __builtin_amdgcn_exp2f(a0[r] - mx); e1[r] = __builtin_amdgcn_exp2f(a1[r] - mx); sum += e0[r] + e1[r]; }
                sum += __shfl_xor(sum, 16);
                sum += __shfl_xor(sum, 32);
                v4u eo = nosent(pack8(e0, e1));
                const int cp = rho >> 3, i = rho & 7;
                st_sc1(eo, rs, comm_off(1, h * 1152 + 512 + g * 128 + i * 16, pol) + cp * 65536u, comm_step_base(step, 0));
                if (g == 0) {
                    v2u so; so.x = min(__builtin_bit_cast(unsigned, mx), 0xFFFFFFFEu); so.y = min(__builtin_bit_cast(unsigned, sum), 0xFFFFFFFEu);
                    st2_sc1(so, rs, comm_off(1, h * 1152 + 1024 + i * 8, pol) + cp * 65536u, comm_step_base(step, 0));
                }
#pragma unroll
                for (int t = 0; t < 2; ++t)
#pragma unroll
                    for (int ks = 0; ks < 14; ++ks) wt[t][ks] = ld_sc1(rs, (unsigned)(G_OFF + ((size_t)((step + 1) * 16 + h) * 28 + t * 14 + ks) * 1024) + lane * 16, 0);
                }
                __syncthreads();
            } else {
                __syncthreads();
            }
        }
    } else {
        const int j = role - 64;
        v4u wt[2][14];
        {
            const unsigned char* src = ws + BS_OFF + ((size_t)j * 8 + 2 * w) * 14 * 1024;
#pragma unroll
            for (int t = 0; t < 2; ++t)
#pragma unroll
                for (int ks = 0; ks < 14; ++ks) wt[t][ks] = *(const v4u*)(src + (t * 14 + ks) * 1024 + lane * 16);
        }
        u16* tr = (u16*)(lds + 16384 + w * 1024);
        const int half = (lane >> 3) & 1, i8 = lane & 7;
        for (int step = 0; step < NSEG; ++step) {
            const unsigned pol = (polmask >> (step >> 4)) & 1;
            const bool last = (step == NSEG - 1);
            v4u uf[2];
            if (!last) {
#pragma unroll
                for (int u = 0; u < 2; ++u) uf[u] = ld_sc1(rs, (unsigned)(U_OFF + ((size_t)(step * 56 + j) * 8 + 2 * w + u) * 1024) + lane * 16, 0);
            }
            if (step > 0) delay64(dVm);
            gather_m(rs, step, pol, ldsm, tid);
            __syncthreads();
            f4 a0 = {0, 0, 0, 0}, a1 = {0, 0, 0, 0};
#pragma unroll
            for (int ks = 0; ks < 14; ++ks) {
                v4u mf = *(const v4u*)(ldsm + ks * 512 + mfrag_off);
                a0 = mfma(mf, wt[0][ks], a0);
                a1 = mfma(mf, wt[1][ks], a1);
            }
            if (g < 2) {
                v2u p0; p0.x = pk2(a0[0], a0[1]); p0.y = pk2(a0[2], a0[3]);
                v2u p1; p1.x = pk2(a1[0], a1[1]); p1.y = pk2(a1[2], a1[3]);
                *(v2u*)(tr + ((0 + half) * 8 + i8) * 8 + 4 * g) = p0;
                *(v2u*)(tr + ((2 + half) * 8 + i8) * 8 + 4 * g) = p1;
            }
            __builtin_amdgcn_fence(__ATOMIC_RELEASE, "wavefront");
            __builtin_amdgcn_wave_barrier();
            v4u vp = *(const v4u*)(tr + (g * 8 + i8) * 8);
            if (rho < 8) st_sc1(nosent(vp), rs, (unsigned)(VP_OFF + ((size_t)(step * 56 + j) * 4 + w) * 512) + g * 128 + rho * 16, 0);
            if (last) break;
            delay64(dV);
            const int hh = 4 * w + g;
            v4u e01, e23; v2u pm[4], stt;
            {
                const unsigned oe0 = comm_off(1, (4 * w + half) * 1152 + 512 + g * 128 + i8 * 16, pol);
                const unsigned oe1 = comm_off(1, (4 * w + 2 + half) * 1152 + 512 + g * 128 + i8 * 16, pol);
                unsigned op[4];
#pragma unroll
                for (int q = 0; q < 4; ++q) op[q] = comm_off(1, hh * 1152 + q * 128 + i8 * 16 + half * 8, pol);
                const unsigned os = comm_off(1, hh * 1152 + 1024 + i8 * 8, pol);
                int copy = 0;
                for (unsigned spin = 0;; ++spin) {
                    asm volatile("" ::: "memory");
                    const unsigned sb = comm_step_base(step, copy);
                    e01 = ld_sc1(rs, oe0, sb); e23 = ld_sc1(rs, oe1, sb);
#pragma unroll
                    for (int q = 0; q < 4; ++q) pm[q] = ld2_sc1(rs, op[q], sb);
                    stt = ld2_sc1(rs, os, sb);
                    unsigned t = vmax4(vmax4(0, e01), e23);
#pragma unroll
                    for (int q = 0; q < 4; ++q) t = umax3(t, pm[q].x, pm[q].y);
                    t = umax3(t, stt.x, stt.y);
                    if (__all(t != SENT) || spin > SPIN_MAX) break;
                    copy = KCOPY - 1;
                }
            }
            f4 x0 = mfma(uf[0], e01, f4{0, 0, 0, 0});
            f4 x1 = mfma(uf[1], e23, f4{0, 0, 0, 0});
            float s0 = (bflo(pm[0].x) + bflo(pm[1].x)) + (bflo(pm[2].x) + bflo(pm[3].x));
            float s1 = (bfhi(pm[0].x) + bfhi(pm[1].x)) + (bfhi(pm[2].x) + bfhi(pm[3].x));
            float s2 = (bflo(pm[0].y) + bflo(pm[1].y)) + (bflo(pm[2].y) + bflo(pm[3].y));
            float s3 = (bfhi(pm[0].y) + bfhi(pm[1].y)) + (bfhi(pm[2].y) + bfhi(pm[3].y));
            const float mloc = asf(stt.x), lsum = asf(stt.y);
            float mx = fmaxf(fmaxf(fmaxf(s0, s1), fmaxf(s2, s3)), mloc);
            mx = fmaxf(mx, dpp_xor8(mx));
            float p0 = __builtin_amdgcn_exp2f(s0 - mx), p1 = __builtin_amdgcn_exp2f(s1 - mx), p2 = __builtin_amdgcn_exp2f(s2 - mx), p3 = __builtin_amdgcn_exp2f(s3 - mx);
            float f = __builtin_amdgcn_exp2f(mloc - mx);
            float z = (p0 + p1) + (p2 + p3);
            z += dpp_xor8(z);
            z += f * lsum;
            float rz = __builtin_amdgcn_rcpf(z);
            float fx = f * rz;
            unsigned q0 = pk2(p0 * rz, p1 * rz), q1 = pk2(p2 * rz, p3 * rz);
            unsigned q2 = dpp_xor8u(q0), q3 = dpp_xor8u(q1);
            v4u pb = {q0, q1, q2, q3};
            f4 cm = mfma(vp, pb, f4{0, 0, 0, 0});
            float fx0 = __builtin_bit_cast(float, __builtin_amdgcn_ds_bpermute((16 * (0 + half) + i8) * 4, __builtin_bit_cast(int, fx)));
            float fx1 = __builtin_bit_cast(float, __builtin_amdgcn_ds_bpermute((16 * (2 + half) + i8) * 4, __builtin_bit_cast(int, fx)));
            f4 cx = x0 * fx0 + x1 * fx1;
            if (half == 0 && g < 2) { f4 t = cx + cm; *(f4*)(red + ((w * 2 + 0) * 8 + i8) * 8 + 4 * g) = t; }
            if (half == 1 && g >= 2) { *(f4*)(red + ((w * 2 + 1) * 8 + i8) * 8 + 4 * (g - 2)) = cx; }
            __syncthreads();
            if (w == 0 && lane < 8 * KCOPY) {
                const int i = lane & 7, cp = lane >> 3;
                f4 lo = {0, 0, 0, 0}, hi = {0, 0, 0, 0};
#pragma unroll
                for (int p = 0; p < 8; ++p) { lo += *(const f4*)(red + (p * 8 + i) * 8); hi += *(const f4*)(red + (p * 8 + i) * 8 + 4); }
                const unsigned pol2 = (polmask >> ((step + 1) >> 4)) & 1;
                st_sc1(nosent(pack8(lo, hi)), rs, comm_off(0, j * 128 + i * 16, pol2) + cp * 65536u, comm_step_base(step + 1, 0));
            }
        }
    }
}

__global__ void __launch_bounds__(256, 1) k_p2(unsigned char* ws, u16* __restrict__ out) {
    extern __shared__ __attribute__((aligned(16))) unsigned char lds[];
    const int seg = blockIdx.x;
    const int tid = threadIdx.x, lane = tid & 63, w = __builtin_amdgcn_readfirstlane(tid >> 6);
    const int rho = lane & 15, g = lane >> 4;
    __amdgpu_buffer_rsrc_t rs = mkrs(ws, 0x7fffffff);
    unsigned char* xl = lds + 40960;
#pragma unroll
    for (int i = 0; i < 7; ++i) *(v4u*)(xl + (i * 256 + tid) * 16) = *(const v4u*)(ws + XF_OFF + (size_t)seg * 28672 + (i * 256 + tid) * 16);
    __syncthreads();
    f4 sm[2][2];
#pragma unroll
    for (int a = 0; a < 2; ++a)
#pragma unroll
        for (int b2 = 0; b2 < 2; ++b2) sm[a][b2] = f4{0, 0, 0, 0};
#pragma unroll
    for (int ks = 0; ks < 14; ++ks) {
        v4u x0 = *(const v4u*)(xl + ks * 1024 + lane * 16), x1 = *(const v4u*)(xl + (14 + ks) * 1024 + lane * 16);
#pragma unroll
        for (int tau = 0; tau < 2; ++tau) {
            v4u gm = ld_sc1(rs, (unsigned)(GM_OFF + ((size_t)(seg * 16 + 4 * w + (rho >> 2)) * 14 + ks) * 512) + g * 128 + (4 * tau + (rho & 3)) * 16, 0);
            sm[tau][0] = mfma(gm, x0, sm[tau][0]);
            sm[tau][1] = mfma(gm, x1, sm[tau][1]);
        }
    }
    float pmx[2][8];
    for (int hq = 0; hq < 4; ++hq) {
        const int h = 4 * w + hq;
        f4 sx[2][2];
#pragma unroll
        for (int a = 0; a < 2; ++a)
#pragma unroll
            for (int b2 = 0; b2 < 2; ++b2) sx[a][b2] = f4{0, 0, 0, 0};
#pragma unroll
        for (int ks = 0; ks < 14; ++ks) {
            v4u x0 = *(const v4u*)(xl + ks * 1024 + lane * 16), x1 = *(const v4u*)(xl + (14 + ks) * 1024 + lane * 16);
#pragma unroll
            for (int tau = 0; tau < 2; ++tau) {
                v4u gf = ld_sc1(rs, (unsigned)(G_OFF + ((size_t)((seg * 16 + h) * 2 + tau) * 14 + ks) * 1024) + lane * 16, 0);
                sx[tau][0] = mfma(gf, x0, sx[tau][0]);
                sx[tau][1] = mfma(gf, x1, sx[tau][1]);
            }
        }
#pragma unroll
        for (int tp = 0; tp < 2; ++tp) {
            const int tq = perm32(tp, rho);
            float v[8];
#pragma unroll
            for (int tau = 0; tau < 2; ++tau)
#pragma unroll
                for (int r = 0; r < 4; ++r) { const int key = 8 * g + 4 * tau + r; v[4 * tau + r] = (key <= tq) ? sx[tau][tp][r] : -3.0e38f; }
            float mx = v[0];
#pragma unroll
            for (int k = 1; k < 8; ++k) mx = fmaxf(mx, v[k]);
            const bool own = (g == hq);
            if (own) {
#pragma unroll
                for (int tau = 0; tau < 2; ++tau)
#pragma unroll
                    for (int r = 0; r < 4; ++r) mx = fmaxf(mx, sm[tau][tp][r]);
            }
            mx = fmaxf(mx, __shfl_xor(mx, 16));
            mx = fmaxf(mx, __shfl_xor(mx, 32));
            float sum = 0.f, em[8];
#pragma unroll
            for (int k = 0; k < 8; ++k) { v[k] = __builtin_amdgcn_exp2f(v[k] - mx); sum += v[k]; }
#pragma unroll
            for (int tau = 0; tau < 2; ++tau)
#pragma unroll
                for (int r = 0; r < 4; ++r) { em[4 * tau + r] = own ? __builtin_amdgcn_exp2f(sm[tau][tp][r] - mx) : 0.f; sum += em[4 * tau + r]; }
            sum += __shfl_xor(sum, 16);
            sum += __shfl_xor(sum, 32);
            const float rz = 1.0f / sum;
            v4u pf; pf.x = pk2(v[0] * rz, v[1] * rz); pf.y = pk2(v[2] * rz, v[3] * rz); pf.z = pk2(v[4] * rz, v[5] * rz); pf.w = pk2(v[6] * rz, v[7] * rz);
            *(v4u*)(lds + (h * 2 + tp) * 1024 + lane * 16) = pf;
            if (own) {
#pragma unroll
                for (int k = 0; k < 8; ++k) pmx[tp][k] = em[k] * rz;
            }
        }
    }
#pragma unroll
    for (int tp = 0; tp < 2; ++tp) {
        v4u pf; pf.x = pk2(pmx[tp][0], pmx[tp][1]); pf.y = pk2(pmx[tp][2], pmx[tp][3]); pf.z = pk2(pmx[tp][4], pmx[tp][5]); pf.w = pk2(pmx[tp][6], pmx[tp][7]);
        *(v4u*)(lds + ((16 + w) * 2 + tp) * 1024 + lane * 16) = pf;
    }
    __syncthreads();
    for (int cb = w; cb < 14; cb += 4) {
        f4 acc[2][2];
#pragma unroll
        for (int a = 0; a < 2; ++a)
#pragma unroll
            for (int b2 = 0; b2 < 2; ++b2) acc[a][b2] = f4{0, 0, 0, 0};
#pragma unroll 4
        for (int h = 0; h < 16; ++h) {
            v4u p0 = *(const v4u*)(lds + (h * 2) * 1024 + lane * 16), p1 = *(const v4u*)(lds + (h * 2 + 1) * 1024 + lane * 16);
#pragma unroll
            for (int t = 0; t < 2; ++t) {
                const int c = 32 * cb + perm32(t, rho); const int j = c >> 3, c8 = c & 7;
                v4u uf = ld_sc1(rs, (unsigned)(U_OFF + ((size_t)(seg * 56 + j) * 8 + (h >> 1)) * 1024) + (((h & 1) * 8 + c8) + 16 * g) * 16, 0);
                acc[t][0] = mfma(uf, p0, acc[t][0]);
                acc[t][1] = mfma(uf, p1, acc[t][1]);
            }
        }
#pragma unroll
        for (int w4 = 0; w4 < 4; ++w4) {
            v4u p0 = *(const v4u*)(lds + ((16 + w4) * 2) * 1024 + lane * 16), p1 = *(const v4u*)(lds + ((16 + w4) * 2 + 1) * 1024 + lane * 16);
#pragma unroll
            for (int t = 0; t < 2; ++t) {
                const int c = 32 * cb + perm32(t, rho); const int j = c >> 3, c8 = c & 7;
                v4u vf = ld_sc1(rs, (unsigned)(VP_OFF + ((size_t)(seg * 56 + j) * 4 + w4) * 512) + g * 128 + c8 * 16, 0);
                acc[t][0] = mfma(vf, p0, acc[t][0]);
                acc[t][1] = mfma(vf, p1, acc[t][1]);
            }
        }
#pragma unroll
        for (int tp = 0; tp < 2; ++tp) {
            const int tok = seg * 32 + perm32(tp, rho);
            *(v4u*)(out + (size_t)tok * OUTW + 448 + 32 * cb + 8 * g) = pack8(acc[0][tp], acc[1][tp]);
        }
    }
}

__global__ void __launch_bounds__(256) k_a_qkv(const u16* __restrict__ x, const u16* __restrict__ wqkv, float* __restrict__ qkv) {
    __shared__ float xs[16][448];
    const int t0 = blockIdx.y * 16, c = blockIdx.x * 256 + threadIdx.x;
    for (int i = threadIdx.x; i < 16 * 448; i += 256) xs[i / 448][i % 448] = bf2f(x[(size_t)(t0 + i / 448) * 448 + i % 448]);
    __syncthreads();
    if (c >= 1344) return;
    float acc[16];
#pragma unroll
    for (int t = 0; t < 16; ++t) acc[t] = 0.f;
    for (int k = 0; k < 448; ++k) {
        float wv = bf2f(wqkv[(size_t)k * 1344 + c]);
#pragma unroll
        for (int t = 0; t < 16; ++t) acc[t] += xs[t][k] * wv;
    }
#pragma unroll
    for (int t = 0; t < 16; ++t) qkv[(size_t)(t0 + t) * 1344 + c] = acc[t];
}
__global__ void __launch_bounds__(256) k_a_phi(const float* __restrict__ qkv, const u16* __restrict__ wphi, float* __restrict__ phi) {
    const int idx = blockIdx.x * 256 + threadIdx.x;
    const int tok = idx >> 5, which = (idx >> 4) & 1, h = idx & 15;
    float qv[28], p[28];
    for (int d = 0; d < 28; ++d) qv[d] = qkv[(size_t)tok * 1344 + which * 448 + h * 28 + d];
    float mx = -3e38f, mn = 3e38f;
    for (int f = 0; f < 28; ++f) { float s = 0.f; for (int d = 0; d < 28; ++d) s += qv[d] * bf2f(wphi[(h * 28 + d) * 28 + f]); p[f] = s; mx = fmaxf(mx, s); mn = fminf(mn, s); }
    float sp = 0.f, sn = 0.f;
    for (int f = 0; f < 28; ++f) { sp += __expf(p[f] - mx); sn += __expf(mn - p[f]); }
    float* o = phi + (size_t)idx * 56;
    for (int f = 0; f < 28; ++f) { o[f] = __expf(p[f] - mx) / sp; o[28 + f] = __expf(mn - p[f]) / sn; }
}
__global__ void __launch_bounds__(256) k_a_kv(const float* __restrict__ qkv, const float* __restrict__ phi, float* __restrict__ kv) {
    const int h = blockIdx.x, f = blockIdx.y;
    __shared__ float redu[8][32];
    const int d = threadIdx.x & 31, part = threadIdx.x >> 5;
    float acc = 0.f;
    for (int t = part; t < 2048; t += 8) {
        float pk = phi[((size_t)t * 32 + 16 + h) * 56 + f];
        float v = d < 28 ? qkv[(size_t)t * 1344 + 896 + h * 28 + d] : 1.0f;
        acc += pk * v;
    }
    redu[part][d] = acc;
    __syncthreads();
    if (part == 0) { float s = 0.f; for (int p = 0; p < 8; ++p) s += redu[p][d]; kv[(h * 56 + f) * 32 + d] = s; }
}
__global__ void __launch_bounds__(448) k_a_out(const float* __restrict__ phi, const float* __restrict__ kv, const u16* __restrict__ wo, u16* __restrict__ out) {
    __shared__ float o[448];
    const int tok = blockIdx.x, c = threadIdx.x, h = c / 28, d = c % 28;
    const float* pq = phi + ((size_t)tok * 32 + h) * 56;
    float num = 0.f, den = 0.f;
    for (int f = 0; f < 56; ++f) { num += pq[f] * kv[(h * 56 + f) * 32 + d]; den += pq[f] * kv[(h * 56 + f) * 32 + 28]; }
    o[c] = num / (den + 1e-6f);
    __syncthreads();
    float acc = 0.f;
    for (int k = 0; k < 448; ++k) acc += o[k] * bf2f(wo[(size_t)k * 448 + c]);
    out[(size_t)tok * OUTW + c] = (u16)(pk2(acc, 0.f) & 0xffff);
}

#define REC_DS 18
#define REC_DV 8
#define REC_DVM 8
int g_dS = REC_DS, g_dV = REC_DV, g_dVm = REC_DVM;

extern "C" void kernel_launch(void* const* d_in, const int* in_sizes, int n_in, void* d_out, int out_size, void* d_ws, size_t ws_size, hipStream_t stream) {
    const u16* A_x = (const u16*)d_in[0]; const u16* A_wqkv = (const u16*)d_in[1]; const u16* A_wphi = (const u16*)d_in[2]; const u16* A_wo = (const u16*)d_in[3];
    const u16* B_x = (const u16*)d_in[4]; const u16* B_mem0 = (const u16*)d_in[5];
    const u16* Wq = (const u16*)d_in[6]; const u16* Wk = (const u16*)d_in[7]; const u16* Wv = (const u16*)d_in[8]; const u16* Wo = (const u16*)d_in[9];
    u16* out = (u16*)d_out;
    unsigned char* ws = (unsigned char*)(((uintptr_t)d_ws + 2 * MB - 1) & ~(uintptr_t)(2 * MB - 1));
    hipFuncSetAttribute((const void*)k_ab, hipFuncAttributeMaxDynamicSharedMemorySize, 57344);
    hipFuncSetAttribute((const void*)k_gu, hipFuncAttributeMaxDynamicSharedMemorySize, 57344);
    hipFuncSetAttribute((const void*)k_rec, hipFuncAttributeMaxDynamicSharedMemorySize, 98304);
    hipFuncSetAttribute((const void*)k_p2, hipFuncAttributeMaxDynamicSharedMemorySize, 69632);
    k_init<<<256, 256, 0, stream>>>(ws, B_x, B_mem0);
    k_ab<<<256, 448, 57344, stream>>>(ws, Wq, Wk, Wv, Wo);
    k_gu<<<112, 256, 57344, stream>>>(ws);
    k_rec<<<256, 256, 98304, stream>>>(ws, g_dS, g_dV, g_dVm);
    k_p2<<<64, 256, 69632, stream>>>(ws, out);
    float* qkv = (float*)(ws + PA_OFF); float* phi = qkv + 2048 * 1344; float* kv = phi + (size_t)2048 * 32 * 56;
    k_a_qkv<<<dim3(6, 128), 256, 0, stream>>>(A_x, A_wqkv, qkv);
    k_a_phi<<<256, 256, 0, stream>>>(qkv, A_wphi, phi);
    k_a_kv<<<dim3(16, 56), 256, 0, stream>>>(qkv, phi, kv);
    k_a_out<<<2048, 448, 0, stream>>>(phi, kv, A_wo, out);
}
```

```cpp
#include <hip/hip_runtime.h>
#include <hip/hip_bf16.h>
#include <stdint.h>

typedef unsigned short u16;
typedef unsigned v4u __attribute__((ext_vector_type(4)));
typedef unsigned v2u __attribute__((ext_vector_type(2)));
typedef float f4 __attribute__((ext_vector_type(4)));
typedef float f2 __attribute__((ext_vector_type(2)));
typedef short s8 __attribute__((ext_vector_type(8)));
typedef __bf16 bf2 __attribute__((ext_vector_type(2)));
#define DI __device__ __forceinline__

constexpr int D = 448, H = 16, DH = 256, MEM = 8, SEG = 32, NSEG = 64, S_TOK = 2048;
constexpr int NKS = 14;
constexpr int OUTW = 896;
constexpr unsigned SENT = 0xFFFFFFFFu;
constexpr float LOG2E = 1.4426950408889634f;

constexpr size_t MB = 1024 * 1024;
constexpr int KCOPY = 4;
constexpr size_t COMM_OFF = 0;
constexpr size_t COMM_BYTES = 2 * MB;
constexpr size_t CTRL_OFF = COMM_BYTES;
constexpr size_t AS_OFF = CTRL_OFF + 262144;
constexpr size_t AS_BYTES = (size_t)16 * 14 * 2 * 14 * 1024;
constexpr size_t BS_OFF = AS_OFF + AS_BYTES;
constexpr size_t BS_BYTES = (size_t)56 * 8 * 14 * 1024;
constexpr size_t XF_OFF = BS_OFF + BS_BYTES;
constexpr size_t XF_BYTES = (size_t)64 * 28 * 1024;
constexpr size_t G_OFF = XF_OFF + XF_BYTES;
constexpr size_t G_BYTES = (size_t)64 * 16 * 28 * 1024;
constexpr size_t U_OFF = G_OFF + G_BYTES;
constexpr size_t U_BYTES = (size_t)64 * 56 * 8 * 1024;
constexpr size_t GM_OFF = U_OFF + U_BYTES;
constexpr size_t GM_BYTES = (size_t)64 * 16 * 14 * 512;
constexpr size_t VP_OFF = GM_OFF + GM_BYTES;
constexpr size_t VP_BYTES = (size_t)64 * 56 * 4 * 512;
constexpr size_t AXF_OFF = VP_OFF + VP_BYTES;
constexpr size_t WQF_OFF = AXF_OFF + XF_BYTES;
constexpr size_t WQF_ONE = (size_t)16 * 2 * 14 * 1024;
constexpr size_t WOF_OFF = WQF_OFF + 3 * WQF_ONE;
constexpr size_t KVP_OFF = WOF_OFF + WQF_ONE;
constexpr size_t KVF_OFF = KVP_OFF + (size_t)16 * 16 * 8192;
constexpr size_t WS_END = KVF_OFF + 65536;
constexpr unsigned C_POL = 0, C_REC = 128, C_DONE = 256, C_A1 = 384, C_KV = 512, C_TICKET = 640, C_GU = 1024;
constexpr unsigned C_DBG = 32768;

DI unsigned pk2(float a, float b) { f2 v = {a, b}; return __builtin_bit_cast(unsigned, __builtin_convertvector(v, bf2)); }
DI float asf(unsigned x) { return __builtin_bit_cast(float, x); }
DI unsigned asu(float x) { return __builtin_bit_cast(unsigned, x); }
DI float bflo(unsigned x) { return __builtin_bit_cast(float, x << 16); }
DI float bfhi(unsigned x) { return __builtin_bit_cast(float, x & 0xffff0000u); }
DI float bf2f(u16 x) { return __builtin_bit_cast(float, (unsigned)x << 16); }
DI f4 mfma(v4u a, v4u b, f4 c) { return __builtin_amdgcn_mfma_f32_16x16x32_bf16(__builtin_bit_cast(s8, a), __builtin_bit_cast(s8, b), c, 0, 0, 0); }
DI v4u pack8(f4 a, f4 b) { v4u r; r.x = pk2(a[0], a[1]); r.y = pk2(a[2], a[3]); r.z = pk2(b[0], b[1]); r.w = pk2(b[2], b[3]); return r; }
DI v4u nosent(v4u v) { v.x = min(v.x, 0xFFFFFFFEu); v.y = min(v.y, 0xFFFFFFFEu); v.z = min(v.z, 0xFFFFFFFEu); v.w = min(v.w, 0xFFFFFFFEu); return v; }
DI int perm32(int t, int rho) { return 8 * (rho >> 2) + 4 * t + (rho & 3); }
DI __amdgpu_buffer_rsrc_t mkrs(void* p, unsigned bytes) { return __builtin_amdgcn_make_buffer_rsrc(p, 0, bytes, 0x00020000); }
DI v4u ld_sc1(__amdgpu_buffer_rsrc_t rs, unsigned voff, unsigned soff) { return __builtin_amdgcn_raw_buffer_load_b128(rs, voff, soff, 16); }
DI v2u ld2_sc1(__amdgpu_buffer_rsrc_t rs, unsigned voff, unsigned soff) { return __builtin_amdgcn_raw_buffer_load_b64(rs, voff, soff, 16); }
DI void st_sc1(v4u v, __amdgpu_buffer_rsrc_t rs, unsigned voff, unsigned soff) { __builtin_amdgcn_raw_buffer_store_b128(v, rs, voff, soff, 16); }
DI void st2_sc1(v2u v, __amdgpu_buffer_rsrc_t rs, unsigned voff, unsigned soff) { __builtin_amdgcn_raw_buffer_store_b64(v, rs, voff, soff, 16); }
DI unsigned long long mtime() { unsigned long long t; asm volatile("s_memtime %0\n\ts_waitcnt lgkmcnt(0)" : "=s"(t) :: "memory"); return t; }

typedef short s4t __attribute__((ext_vector_type(4)));
DI v2u trread(const unsigned char* p) { return __builtin_bit_cast(v2u, __builtin_amdgcn_ds_read_tr16_b64_v4i16((__attribute__((address_space(3))) s4t*)p)); }
DI unsigned comm_step_base(int step, int copy) { return __builtin_amdgcn_readfirstlane((unsigned)(step & 7) * 262144u + (unsigned)copy * 65536u); }
DI unsigned comm_off(int chunk0, unsigned o, unsigned pol) { return (unsigned)(chunk0 + (o >> 13)) * 16384u + pol * 8192u + (o & 8191u); }

__global__ void __launch_bounds__(448) k_pre(unsigned char* ws, const u16* __restrict__ Wq, const u16* __restrict__ Wk, const u16* __restrict__ Wv, const u16* __restrict__ Wo,
                                             const u16* __restrict__ bx, const u16* __restrict__ mem0,
                                             const u16* __restrict__ ax, const u16* __restrict__ wqkv, const u16* __restrict__ wphi, const u16* __restrict__ awo) {
    extern __shared__ __attribute__((aligned(16))) unsigned char lds[];
    const int tid = threadIdx.x, lane = tid & 63, w = __builtin_amdgcn_readfirstlane(tid >> 6);
    const int wg = blockIdx.x;
    const int h = (wg & 7) * 2 + ((wg >> 3) & 1), rest = wg >> 4;
    const int mat = rest >> 3, rh = (rest >> 2) & 1, q = rest & 3;
    const int rho = lane & 15, g = lane >> 4;
    const int rb = 7 * rh + w;
    __amdgpu_buffer_rsrc_t rs = mkrs(ws, 0x7fffffff);
    const u16* Wr = mat ? Wv : Wk;
    v4u a[2][8];
#pragma unroll
    for (int t = 0; t < 2; ++t) {
        const int row = 32 * rb + perm32(t, rho);
#pragma unroll
        for (int es = 0; es < 8; ++es) a[t][es] = *(const v4u*)(Wr + ((size_t)row * H + h) * DH + es * 32 + g * 8);
    }
    constexpr int WST = 240;
    if (mat == 0) {
        const int ct = 7 * q + w; const int d = 32 * (ct >> 1) + perm32(ct & 1, rho);
#pragma unroll
        for (int es = 0; es < 8; ++es) *(v4u*)(lds + (w * 8 + es) * 1024 + lane * 16) = *(const v4u*)(Wq + ((size_t)d * H + h) * DH + es * 32 + g * 8);
    } else {
#pragma unroll
        for (int it = 0; it < 8; ++it) {
            const int idx = it * 448 + tid; const int e = idx / 14, c8 = idx % 14;
            *(v4u*)(lds + e * WST + c8 * 16) = *(const v4u*)(Wo + ((size_t)h * DH + e) * D + 112 * q + c8 * 8);
        }
    }
    const int gt = wg * 448 + tid; constexpr int GN = 256 * 448;
    if (wg == 0 && w == 0) {
        unsigned best[2] = {0xffffffffu, 0xffffffffu};
        for (int r = 0; r < 4; ++r)
            for (int p = 0; p < 2; ++p) {
                v4u z = {SENT, SENT, SENT, SENT};
                unsigned off = 7680u + p * 8192u + (r & 1) * 128u;
                asm volatile("s_waitcnt vmcnt(0)" ::: "memory");
                unsigned long long t0 = mtime();
                if (lane < 8) st_sc1(z, rs, off + lane * 16, 0);
                asm volatile("s_waitcnt vmcnt(0)" ::: "memory");
                unsigned long long t1 = mtime();
                unsigned dt = (unsigned)(t1 - t0);
                if (r > 0) best[p] = min(best[p], dt);
            }
        if (lane == 0) *(unsigned*)(ws + CTRL_OFF + C_POL) = best[1] < best[0] ? 1u : 0u;
    }
    if (wg == 1 && tid >= 1 && tid < 128) __builtin_amdgcn_raw_buffer_store_b32(0u, rs, (unsigned)CTRL_OFF + tid * 128, 0, 16);
    for (unsigned i = gt; i < COMM_BYTES / 16; i += GN) {
        unsigned off = i * 16;
        unsigned within = off & 65535u, slot = off >> 18;
        unsigned chunk = (within >> 14) & 3, o = within & 8191u;
        if (chunk == 0 && o >= 7184) continue;
        v4u v = {SENT, SENT, SENT, SENT};
        if (slot == 0 && chunk == 0 && o < 7168) {
            int j = o >> 7, irow = (o >> 4) & 7;
            v = nosent(*(const v4u*)(mem0 + irow * D + j * 8));
        }
        st_sc1(v, rs, off, 0);
    }
    {
        const int i = gt; const int ln = i & 63; const int f = i >> 6; const int ks = f % 14; const int st = f / 14; const int tau = st & 1, seg = st >> 1;
        const int tok = seg * 32 + perm32(tau, ln & 15);
        *(v4u*)(ws + XF_OFF + (size_t)i * 16) = *(const v4u*)(bx + (size_t)tok * D + ks * 32 + (ln >> 4) * 8);
        *(v4u*)(ws + AXF_OFF + (size_t)i * 16) = *(const v4u*)(ax + (size_t)tok * D + ks * 32 + (ln >> 4) * 8);
    }
    __syncthreads();
    f4 acc[2][7];
#pragma unroll
    for (int t = 0; t < 2; ++t)
#pragma unroll
        for (int c = 0; c < 7; ++c) acc[t][c] = f4{0, 0, 0, 0};
    if (mat == 0) {
#pragma unroll
        for (int es = 0; es < 8; ++es)
#pragma unroll
            for (int c = 0; c < 7; ++c) {
                v4u bfr = *(const v4u*)(lds + (c * 8 + es) * 1024 + lane * 16);
                acc[0][c] = mfma(a[0][es], bfr, acc[0][c]);
                acc[1][c] = mfma(a[1][es], bfr, acc[1][c]);
            }
        const float sc = LOG2E * 0.0625f;
#pragma unroll
        for (int c = 0; c < 7; ++c) {
            const int ct = 7 * q + c;
            v4u o = pack8(acc[0][c] * sc, acc[1][c] * sc);
            *(v4u*)(ws + AS_OFF + ((size_t)((h * 14 + (ct >> 1)) * 2 + (ct & 1)) * 14 + rb) * 1024 + lane * 16) = o;
        }
    } else {
        const unsigned trb = (8 * g + (rho >> 2)) * WST + (lane & 3) * 8;
#pragma unroll
        for (int es = 0; es < 8; ++es)
#pragma unroll
            for (int c = 0; c < 7; ++c) {
                v2u lo = trread(lds + trb + (32 * es) * WST + c * 32), hi = trread(lds + trb + (32 * es + 4) * WST + c * 32);
                v4u bfr = {lo.x, lo.y, hi.x, hi.y};
                acc[0][c] = mfma(a[0][es], bfr, acc[0][c]);
                acc[1][c] = mfma(a[1][es], bfr, acc[1][c]);
            }
#pragma unroll
        for (int c = 0; c < 7; ++c) {
            const int ct = 7 * q + c;
            const int j = 2 * ct + (rho >> 3);
            v4u o = pack8(acc[0][c], acc[1][c]);
            *(v4u*)(ws + BS_OFF + ((size_t)(j * 8 + (h >> 1)) * 14 + rb) * 1024 + (((h & 1) * 8 + (rho & 7)) + 16 * g) * 16) = o;
        }
    }
    const int gwv = wg * 7 + w;
    if (gwv < 448) {
        const int which = gwv / 224, h2 = (gwv / 14) & 15, ks = gwv % 14;
        v4u af[2], bf[2];
#pragma unroll
        for (int t = 0; t < 2; ++t) {
            const u16* p = wqkv + (size_t)(32 * ks + perm32(t, rho)) * 1344 + which * 448 + h2 * 28 + g * 8;
            v2u lo = *(const v2u*)p, hi = {0, 0};
            if (g < 3) hi = *(const v2u*)(p + 4);
            af[t] = v4u{lo.x, lo.y, hi.x, hi.y};
            const int f = perm32(t, rho);
            unsigned e[8];
#pragma unroll
            for (int jj = 0; jj < 8; ++jj) { const int d = 8 * g + jj; e[jj] = (d < 28 && f < 28) ? wphi[(h2 * 28 + d) * 28 + f] : 0u; }
            bf[t] = v4u{e[0] | (e[1] << 16), e[2] | (e[3] << 16), e[4] | (e[5] << 16), e[6] | (e[7] << 16)};
        }
#pragma unroll
        for (int t = 0; t < 2; ++t) {
            f4 d0 = mfma(af[0], bf[t], f4{0, 0, 0, 0}), d1 = mfma(af[1], bf[t], f4{0, 0, 0, 0});
            *(v4u*)(ws + WQF_OFF + which * WQF_ONE + ((size_t)(h2 * 2 + t) * 14 + ks) * 1024 + lane * 16) = pack8(d0 * LOG2E, d1 * LOG2E);
        }
    } else if (gwv < 896) {
        const int fi = gwv - 448; const int h2 = fi / 28, t = (fi / 14) & 1, ks = fi % 14;
        const int d = 16 * t + rho;
        unsigned e[8];
#pragma unroll
        for (int jj = 0; jj < 8; ++jj) e[jj] = d < 28 ? wqkv[(size_t)(32 * ks + 8 * g + jj) * 1344 + 896 + h2 * 28 + d] : 0u;
        *(v4u*)(ws + WQF_OFF + 2 * WQF_ONE + (size_t)fi * 1024 + lane * 16) = v4u{e[0] | (e[1] << 16), e[2] | (e[3] << 16), e[4] | (e[5] << 16), e[6] | (e[7] << 16)};
    } else if (gwv < 1344) {
        const int fi = gwv - 896; const int cb = fi >> 5, t = (fi >> 4) & 1, h2 = fi & 15;
        const int c = 32 * cb + perm32(t, rho);
        unsigned e[8];
#pragma unroll
        for (int jj = 0; jj < 8; ++jj) { const int sl = 8 * g + jj; e[jj] = sl < 28 ? awo[(size_t)(h2 * 28 + sl) * 448 + c] : 0u; }
        *(v4u*)(ws + WOF_OFF + (size_t)fi * 1024 + lane * 16) = v4u{e[0] | (e[1] << 16), e[2] | (e[3] << 16), e[4] | (e[5] << 16), e[6] | (e[7] << 16)};
    }
}

DI unsigned umax3(unsigned a, unsigned b, unsigned c) { return max(max(a, b), c); }
DI unsigned vmax4(unsigned t, v4u v) { return umax3(umax3(t, v.x, v.y), v.z, v.w); }
DI float dpp_xor8(float v) { return asf((unsigned)__builtin_amdgcn_update_dpp(0, (int)asu(v), 0x128, 0xf, 0xf, false)); }
DI unsigned dpp_xor8u(unsigned v) { return (unsigned)__builtin_amdgcn_update_dpp(0, (int)v, 0x128, 0xf, 0xf, false); }
constexpr unsigned SPIN_MAX = 200000;
DI float dpp_x1(float v) { return asf((unsigned)__builtin_amdgcn_update_dpp(0, (int)asu(v), 0xB1, 0xf, 0xf, false)); }
DI float dpp_x2(float v) { return asf((unsigned)__builtin_amdgcn_update_dpp(0, (int)asu(v), 0x4E, 0xf, 0xf, false)); }
DI f4 quadsum(f4 v) {
#pragma unroll
    for (int r = 0; r < 4; ++r) v[r] += dpp_x1(v[r]);
#pragma unroll
    for (int r = 0; r < 4; ++r) v[r] += dpp_x2(v[r]);
    return v;
}
DI float xmax16(float x) { auto r = __builtin_amdgcn_permlane16_swap(asu(x), asu(x), false, false); unsigned a = r[0], b = r[1]; return fmaxf(asf(a), asf(b)); }
DI float xmax32(float x) { auto r = __builtin_amdgcn_permlane32_swap(asu(x), asu(x), false, false); unsigned a = r[0], b = r[1]; return fmaxf(asf(a), asf(b)); }
DI float xsum16(float x) { auto r = __builtin_amdgcn_permlane16_swap(asu(x), asu(x), false, false); unsigned a = r[0], b = r[1]; return asf(a) + asf(b); }
DI float xsum32(float x) { auto r = __builtin_amdgcn_permlane32_swap(asu(x), asu(x), false, false); unsigned a = r[0], b = r[1]; return asf(a) + asf(b); }
#define LOADM(mf) v4u mf[14]; _Pragma("unroll") for (int ks = 0; ks < 14; ++ks) mf[ks] = *(const v4u*)(ldsm + ks * 512 + mfrag_off); __builtin_amdgcn_sched_barrier(0);
DI void bar() { asm volatile("s_waitcnt lgkmcnt(0)" ::: "memory"); __builtin_amdgcn_s_barrier(); asm volatile("" ::: "memory"); }
DI unsigned now32() { return (unsigned)mtime(); }
DI void wait_until(unsigned target) {
    int rem = (int)(target - now32());
    while (rem > 80) { __builtin_amdgcn_s_sleep(1); rem -= 64; }
    while (rem > 0) { asm volatile("s_nop 7" ::: "memory"); rem -= 16; }
}

DI unsigned rt32() { unsigned long long t; asm volatile("s_memrealtime %0\n\ts_waitcnt lgkmcnt(0)" : "=s"(t) :: "memory"); return (unsigned)t; }
DI unsigned approach_rt(unsigned target, int lead) {
    int rem;
    for (;;) {
        rem = (int)(target - rt32());
        if (rem <= lead + 4) break;
        int n = (rem - lead) >> 2;
        while (n-- > 0) __builtin_amdgcn_s_sleep(1);
    }
    return now32() + (unsigned)(rem * 23 - 110);
}
DI void spin_to(unsigned tc) { while ((int)(tc - now32()) > 0) {} }
DI void wait_rt(unsigned target) { spin_to(approach_rt(target, 10)); }
template <int NP, bool CTRL>
DI unsigned gather_m(__amdgpu_buffer_rsrc_t rs, unsigned sb, unsigned char* ldsm, int p0, int lane, int skipline = -1) {
    const unsigned o0 = p0 * 1024 + lane * 16;
    for (unsigned spin = 0;; ++spin) {
        asm volatile("" ::: "memory");
        v4u a[NP]; v2u c = {0, 0};
        bool sk[NP];
#pragma unroll
        for (int k = 0; k < NP; ++k) { sk[k] = (int)((o0 + k * 1024) >> 7) == skipline; a[k] = ld_sc1(rs, (o0 + k * 1024) ^ (sk[k] ? 128u : 0u), sb); }
        if (CTRL) c = ld2_sc1(rs, 7168u, sb);
#pragma unroll
        for (int k = 0; k < NP; ++k) *(v4u*)(ldsm + (sk[k] ? 7424u + (lane & 7) * 16 : o0 + k * 1024)) = a[k];
        if (CTRL && lane == 0) *(v2u*)(ldsm + 7168) = c;
        unsigned t;
        if (NP == 2) t = max(vmax4(0, a[0]), vmax4(0, a[1])); else t = vmax4(0, a[0]);
        if (CTRL) t = umax3(t, c.x, c.y);
        if (__all(t != SENT) || spin > SPIN_MAX) return spin;
        __builtin_amdgcn_s_sleep(1);
    }
}
#define LOADP(mf) v4u mf[7]; _Pragma("unroll") for (int q_ = 0; q_ < 7; ++q_) mf[q_] = *(const v4u*)(ldsm + q_ * 1024 + pfrag_off); __builtin_amdgcn_sched_barrier(0);
DI unsigned reduce8(const float* rd, int lane) {
    const float* q = rd + (lane >> 3) * 100 + (lane & 7) * 12;
    f4 a = *(const f4*)q, b = *(const f4*)(q + 4);
    a += b;
    float v = (a[0] + a[1]) + (a[2] + a[3]);
    float o = dpp_x1(v);
    unsigned r = (lane & 1) ? pk2(o, v) : pk2(v, o);
    return min(r, 0xFFFFFFFEu);
}
#define READCTRL() { SAVER1 v2u c_ = *(const v2u*)(ldsm + 7168); R2 = __builtin_amdgcn_readfirstlane(c_.x); R1n = __builtin_amdgcn_readfirstlane(c_.y); }

#define SAVER1
typedef short s4 __attribute__((ext_vector_type(4)));
DI f4 mfma16(v2u a, v2u b, f4 c) { return __builtin_amdgcn_mfma_f32_16x16x16bf16_1k(__builtin_bit_cast(s4, a), __builtin_bit_cast(s4, b), c, 0, 0, 0); }
DI float vmax(float a, float b) { float r; asm("s_nop 1\n\tv_max_f32 %0, %1, %2" : "=v"(r) : "v"(a), "v"(b)); return r; }
DI float vmax3(float a, float b, float c) { float r; asm("v_max3_f32 %0, %1, %2, %3" : "=v"(r) : "v"(a), "v"(b), "v"(c)); return r; }
DI float vmax3_mfma(float a, float b, float c) { float r; asm("s_nop 3\n\tv_max3_f32 %0, %1, %2, %3" : "=v"(r) : "v"(a), "v"(b), "v"(c)); return r; }
DI float rmax16(float x) { auto r = __builtin_amdgcn_permlane16_swap(asu(x), asu(x), false, false); unsigned a = r[0], b = r[1]; return vmax(asf(a), asf(b)); }
DI float rmax32(float x) { auto r = __builtin_amdgcn_permlane32_swap(asu(x), asu(x), false, false); unsigned a = r[0], b = r[1]; return vmax(asf(a), asf(b)); }
constexpr int HS = 1408;
constexpr int ST_OFF = 16 * HS;
constexpr int ZL_OFF = ST_OFF + 1024;
constexpr int L_M = 0, L_RED = 8192, L_RED2 = 12800, L_GM = 16128, L_VP = 17152, L_UF = 19200;

DI unsigned gather_ctrl(__amdgpu_buffer_rsrc_t rs, unsigned sb, unsigned char* ldsm, int lane) {
    for (unsigned spin = 0;; ++spin) {
        asm volatile("" ::: "memory");
        v2u c = ld2_sc1(rs, 7168u, sb);
        if (lane == 0) *(v2u*)(ldsm + 7168) = c;
        if (__all(max(c.x, c.y) != SENT) || spin > SPIN_MAX) return spin;
        __builtin_amdgcn_s_sleep(1);
    }
}
#define HSLEEP 2
#define SB0 __builtin_amdgcn_sched_barrier(0);

DI unsigned ldflag(__amdgpu_buffer_rsrc_t rs, unsigned off) { return __builtin_amdgcn_raw_buffer_load_b32(rs, (unsigned)CTRL_OFF + off, 0, 16); }
DI unsigned wait_flags(__amdgpu_buffer_rsrc_t rs, unsigned off, unsigned need, int slp) {
    unsigned v;
    for (unsigned spin = 0;; ++spin) {
        asm volatile("" ::: "memory");
        v = ldflag(rs, off);
        if (__all(v >= need) || spin > 4 * SPIN_MAX) break;
        for (int i = 0; i < slp; ++i) __builtin_amdgcn_s_sleep(4);
    }
#pragma unroll
    for (int o = 32; o >= 1; o >>= 1) v = min(v, (unsigned)__shfl_xor((int)v, o));
    return __builtin_amdgcn_readfirstlane(v);
}

DI void rec_role(unsigned char* ws, unsigned char* lds, const int role, int dS, int dV, int dVm) {
    const int tid = threadIdx.x, lane = tid & 63, w = __builtin_amdgcn_readfirstlane(tid >> 6);
    const int rho = lane & 15, g = lane >> 4;
    __amdgpu_buffer_rsrc_t rs = mkrs(ws, 0x7fffffff);
    unsigned char* ldsm = lds + L_M;
    float* red = (float*)(lds + L_RED);
    float* red2 = (float*)(lds + L_RED2);
    const unsigned polo = __builtin_amdgcn_readfirstlane(*(const unsigned*)(ws + CTRL_OFF + C_POL)) * 8192u;
    const unsigned myc = (unsigned)(role & 3) * 65536u + polo;
    unsigned nfail = 0, nfail2 = 0;
    unsigned long long* dbg = (unsigned long long*)(ws + CTRL_OFF + C_DBG);
#define STAMP(cond, idx) if ((cond) && role == 0 && tid == 0) dbg[idx] = __builtin_amdgcn_s_memrealtime();
#define TL(idx)
#define GT(idx, ref)
    const unsigned pfrag_off = (rho >> 3) * 512 + g * 128 + (rho & 7) * 16;
    const unsigned TA = dS, TB = dV, TE = dVm;
    unsigned R2 = 0, R1n = 0;
    const bool is_mx = role >= 112;
    const bool is_v = !is_mx && (((role >> 2) & 1) == 0);
    const int j = (role >> 3) * 4 + (role & 3);
    const int h = role / 7, p = role % 7;
    const int hp = (lane >> 3) & 1, i8 = lane & 7;
    const v4u SENT4 = {SENT, SENT, SENT, SENT};

    if (is_mx) {
        const int hx = role - 112;
        if (w >= 4) {
            const unsigned orst = comm_off(1, hx * HS + 896 + (lane & 31) * 16, 0) + (lane >> 5) * 65536u + polo;
            const unsigned orst2 = comm_off(1, ST_OFF + (hx >> 1) * 128 + (hx & 1) * 64 + (lane & 1) * 16, 0) + ((lane >> 1) & 3) * 65536u + polo;
            for (int step = 0; step < NSEG; ++step) {
                const unsigned sbr = comm_step_base(step, 0) + myc;
                if (step > 0 && w == 7) wait_rt(R1n);
                __builtin_amdgcn_s_barrier();
                if (w < 7) nfail += gather_m<2, false>(rs, sbr, ldsm, 2 * (w - 4), lane) ? 1 : 0;
                else nfail += gather_m<1, true>(rs, sbr, ldsm, 6, lane) ? 1 : 0;
                bar();
                READCTRL()
                bar();
                if (w == 4 && step >= 4) {
                    __builtin_amdgcn_s_sleep(4);
                    const unsigned sbz = comm_step_base(step + 4, 0);
                    st_sc1(SENT4, rs, orst, sbz); st_sc1(SENT4, rs, orst + 131072u, sbz);
                    if (lane < 8) st_sc1(SENT4, rs, orst2, sbz);
                }
            }
        } else {
            __builtin_amdgcn_s_setprio(2);
            const int tau = w & 1, kh = w >> 1;
            v4u wt[8];
            const unsigned gbase = (unsigned)(G_OFF + ((size_t)hx * 2 + tau) * 14 * 1024 + kh * 8192) + lane * 16;
            const unsigned gl6 = kh ? 4096u : 6144u;
#define GLOAD(sb_) { _Pragma("unroll") for (int ks = 0; ks < 6; ++ks) wt[ks] = ld_sc1(rs, gbase + ks * 1024, sb_); \
                     if (kh == 0) { wt[6] = ld_sc1(rs, gbase + 6144, sb_); wt[7] = ld_sc1(rs, gbase + 7168, sb_); } }
            wt[6] = v4u{0, 0, 0, 0}; wt[7] = v4u{0, 0, 0, 0};
            const unsigned gfl = C_GU + 128u * (unsigned)min((14 * hx) / 4 + (lane & 7), (14 * hx + 13) / 4);
            unsigned known = wait_flags(rs, gfl, 1, 1);
            GLOAD(0)
            float* sc = red;
            const int tq = lane & 7, ir = lane >> 3;
            const unsigned oes = comm_off(1, hx * HS + 896 + lane * 8, 0) + w * 65536u + polo;
            const unsigned oss = comm_off(1, ST_OFF + (hx >> 1) * 128 + (hx & 1) * 64 + ir * 4, 0) + w * 65536u + polo;
            for (int step = 0; step < NSEG; ++step) {
                const bool last = (step == NSEG - 1);
                const unsigned sbs = comm_step_base(step, 0);
                __builtin_amdgcn_s_barrier();
                bar();
                TL(1)
                f4 aE = {0, 0, 0, 0}, aO = {0, 0, 0, 0};
                v4u mf[4];
#pragma unroll
                for (int q = 0; q < 3; ++q) mf[q] = *(const v4u*)(ldsm + (4 * kh + q) * 1024 + pfrag_off);
                mf[3] = *(const v4u*)(ldsm + 3 * 1024 + pfrag_off);
                SB0
#pragma unroll
                for (int q = 0; q < 3; ++q) { aE = mfma(wt[2 * q], mf[q], aE); aO = mfma(wt[2 * q + 1], mf[q], aO); }
                if (kh == 0) { aE = mfma(wt[6], mf[3], aE); aO = mfma(wt[7], mf[3], aO); }
                f4 xs = hp ? aO : aE;
                *(f4*)(sc + ((2 * kh + hp) * 8 + i8) * 36 + 16 * tau + 4 * g) = xs;
                bar();
                const float* sp = sc + ir * 36 + 4 * tq;
                f4 a0 = (*(const f4*)(sp) + *(const f4*)(sp + 288)) + (*(const f4*)(sp + 576) + *(const f4*)(sp + 864));
                float mx;
                asm("v_max3_f32 %0, %1, %2, %3\n\tv_max_f32 %0, %0, %4\n\ts_nop 1\n\t"
                    "v_max_f32_dpp %0, %0, %0 quad_perm:[1,0,3,2] row_mask:0xf bank_mask:0xf\n\ts_nop 1\n\t"
                    "v_max_f32_dpp %0, %0, %0 quad_perm:[2,3,0,1] row_mask:0xf bank_mask:0xf\n\ts_nop 1\n\t"
                    "v_max_f32_dpp %0, %0, %0 row_half_mirror row_mask:0xf bank_mask:0xf"
                    : "=&v"(mx) : "v"(a0[0]), "v"(a0[1]), "v"(a0[2]), "v"(a0[3]));
                if (!last) {
                    unsigned e0 = pk2(__builtin_amdgcn_exp2f(a0[0] - mx), __builtin_amdgcn_exp2f(a0[1] - mx));
                    unsigned e1 = pk2(__builtin_amdgcn_exp2f(a0[2] - mx), __builtin_amdgcn_exp2f(a0[3] - mx));
                    e0 = min(e0, 0xFFFFFFFEu); e1 = min(e1, 0xFFFFFFFEu);
                    v2u eo; eo.x = e0; eo.y = e1;
                    st2_sc1(eo, rs, oes, sbs);
                    if (tq == 1) __builtin_amdgcn_raw_buffer_store_b32(min(asu(mx), 0xFFFFFFFEu), rs, oss, sbs, 16);
                    GT(0, R1n)
                    TL(3)
                    if (known < (unsigned)step + 2) known = wait_flags(rs, gfl, step + 2, 1);
                    const unsigned sb = __builtin_amdgcn_readfirstlane((step + 1) * 16 * 28 * 1024);
                    GLOAD(sb)
                }
            }
        }
    } else if (w >= 4) {
        const int wv = w - 4;
        v4u wtv[2][14];
        if (is_v) {
            const unsigned char* src = ws + BS_OFF + ((size_t)j * 8 + 2 * wv) * 14 * 1024;
#pragma unroll
            for (int t = 0; t < 2; ++t)
#pragma unroll
                for (int f = 0; f < 14; ++f) wtv[t][f] = *(const v4u*)(src + (t * 14 + f) * 1024 + lane * 16);
        } else {
#pragma unroll
            for (int t = 0; t < 2; ++t)
#pragma unroll
                for (int f = 0; f < 14; ++f) wtv[t][f] = v4u{0, 0, 0, 0};
        }
        const bool cond = is_v && j == 0;
        const unsigned ubase = (unsigned)(U_OFF + ((size_t)j * 8 + 2 * wv) * 1024) + lane * 16;
        const unsigned ufl = C_GU + 128u * (56 + j);
        unsigned known = 0;
        v4u un0 = {0, 0, 0, 0}, un1 = {0, 0, 0, 0};
        if (is_v) {
            known = wait_flags(rs, ufl, 1, 1);
            un0 = ld_sc1(rs, ubase, 0); un1 = ld_sc1(rs, ubase + 1024, 0);
        }
        if (cond && w == 5) {
            asm volatile("s_waitcnt vmcnt(0)" ::: "memory");
            unsigned t = rt32() + 150u;
            v2u c; c.x = min(t, 0xFFFFFFFEu); c.y = min(t + TB, 0xFFFFFFFEu);
            if (lane < KCOPY) st2_sc1(c, rs, polo + 7168u + lane * 65536u, 0);
            if (lane == 0) *(v2u*)(ldsm + 7168) = c;
        }
        const unsigned opub = polo + j * 128 + (lane >> 1) * 4 + (lane & 1) * 65536u;
        const unsigned orS = comm_off(1, h * HS + p * 128 + (lane & 7) * 16, 0) + ((lane >> 3) & 3) * 65536u + polo;
        const unsigned orM = polo + j * 128 + (lane & 7) * 16 + ((lane >> 3) & 3) * 65536u;
        for (int step = 0; step < NSEG; ++step) {
            const bool last = (step == NSEG - 1);
            const unsigned sbr = comm_step_base(step, 0) + myc;
            const unsigned sbn = comm_step_base(step + 1, 0);
            if (step > 0 && w == 7) wait_rt(R1n);
            bar();
            TL(0)
            const int skl = (is_v && step > 0) ? j : -1;
            if (w < 7) nfail += gather_m<1, false>(rs, sbr, ldsm, w, lane, skl) ? 1 : 0;
            else if (!cond) nfail += gather_ctrl(rs, sbr, ldsm, lane) ? 1 : 0;
            else asm volatile("s_waitcnt vmcnt(0)" ::: "memory");
            bar();
            READCTRL()
            if (is_v) { *(v4u*)(lds + L_UF + wv * 2048 + lane * 16) = un0; *(v4u*)(lds + L_UF + wv * 2048 + 1024 + lane * 16) = un1; }
            bar();
            TL(2)
            if (is_v) {
                __builtin_amdgcn_s_sleep(HSLEEP);
                LOADP(mf)
                f4 e0 = {0, 0, 0, 0}, o0 = {0, 0, 0, 0}, e1 = {0, 0, 0, 0}, o1 = {0, 0, 0, 0};
#pragma unroll
                for (int q = 0; q < 7; ++q) {
                    e0 = mfma(mf[q], wtv[0][2 * q], e0); o0 = mfma(mf[q], wtv[0][2 * q + 1], o0);
                    e1 = mfma(mf[q], wtv[1][2 * q], e1); o1 = mfma(mf[q], wtv[1][2 * q + 1], o1);
                }
                f4 cs;
#pragma unroll
                for (int r = 0; r < 4; ++r) {
                    auto sw = __builtin_amdgcn_permlane32_swap(asu(o0[r]), asu(e1[r]), false, false);
                    unsigned d0 = sw[0], d1 = sw[1];
                    cs[r] = g < 2 ? e0[r] + asf(d1) : o1[r] + asf(d0);
                }
                v2u vp; vp.x = pk2(cs[0], cs[1]); vp.y = pk2(cs[2], cs[3]);
                *(v2u*)(lds + L_VP + wv * 512 + (2 * (g >> 1) + hp) * 128 + i8 * 16 + (g & 1) * 8) = vp;
            }
            TL(3)
            bar();
            if (w == 7) {
                v4u gmv = *(const v4u*)(lds + L_GM + lane * 16);
                st_sc1(nosent(gmv), rs, (unsigned)(GM_OFF + ((size_t)h * 14 + 2 * p) * 512) + lane * 16, __builtin_amdgcn_readfirstlane(step * 16 * 14 * 512));
                if (step >= 4 && lane < 32) st_sc1(SENT4, rs, orS, comm_step_base(step + 4, 0));
                if (is_v && step >= 3) {
                    if (lane < 32) st_sc1(SENT4, rs, orM, comm_step_base(step + 5, 0));
                    if (cond && lane < 4) st_sc1(SENT4, rs, polo + 7168u + lane * 65536u, comm_step_base(step + 5, 0));
                }
            }
            if (is_v) {
                if (w == 6 || w == 5) {
                    v4u v0 = *(const v4u*)(lds + L_VP + (w - 5) * 1024 + lane * 16);
                    st_sc1(nosent(v0), rs, (unsigned)(VP_OFF + (size_t)j * 2048 + (w - 5) * 1024) + lane * 16, __builtin_amdgcn_readfirstlane(step * 56 * 4 * 512));
                }
                if (step + 2 < NSEG) {
                    if (known < (unsigned)step + 2) known = wait_flags(rs, ufl, step + 2, 1);
                    const unsigned sbu = __builtin_amdgcn_readfirstlane((step + 1) * 56 * 8 * 1024);
                    un0 = ld_sc1(rs, ubase, sbu); un1 = ld_sc1(rs, ubase + 1024, sbu);
                }
                if (w == 4) __builtin_amdgcn_s_setprio(3);
                bar();
                TL(4)
                if (!last) {
                    if (w == 4) {
                        unsigned o = reduce8(red2, lane);
                        __builtin_amdgcn_raw_buffer_store_b32(o, rs, opub, sbn, 16);
                        __builtin_amdgcn_raw_buffer_store_b32(o, rs, opub + 131072u, sbn, 16);
                        GT(2, R2)
                        TL(5)
                        if ((lane & 1) == 0) *(unsigned*)(ldsm + j * 128 + (lane >> 1) * 4) = o;
                        __builtin_amdgcn_s_setprio(0);
                    }
                    if (cond && w == 5) {
                        const unsigned tp = rt32();
                        unsigned base = R1n;
                        if ((int)(tp + 4u - R1n) > 0) base = tp + 90u;
                        v2u c; c.x = min(base + TA, 0xFFFFFFFEu); c.y = min(base + TA + TB, 0xFFFFFFFEu);
                        if (lane < KCOPY) st2_sc1(c, rs, polo + 7168u + lane * 65536u, sbn);
                        if (lane == 0) *(v2u*)(ldsm + 7168) = c;
                    }
                    if (cond && w == 6 && lane == 0) __builtin_amdgcn_raw_buffer_store_b32((unsigned)step, rs, (unsigned)CTRL_OFF + C_REC, 0, 16);
                }
            }
        }
        asm volatile("s_waitcnt vmcnt(0)" ::: "memory");
        __builtin_amdgcn_s_barrier();
        if (w == 4 && lane == 0) __hip_atomic_fetch_add((unsigned*)(ws + CTRL_OFF + C_DONE), 1u, __ATOMIC_RELAXED, __HIP_MEMORY_SCOPE_AGENT);
    } else {
        __builtin_amdgcn_s_setprio(2);
        const int b = 2 * p + (w >> 1), t = w & 1;
        v4u wt[14];
        {
            const unsigned char* src = ws + AS_OFF + ((size_t)(h * 14 + b) * 2 + t) * 14 * 1024;
#pragma unroll
            for (int ks = 0; ks < 14; ++ks) wt[ks] = *(const v4u*)(src + ks * 1024 + lane * 16);
        }
        unsigned op[2][2], oe[2], ost;
        v4u pf[2][2], ef[2]; unsigned stt;
#pragma unroll
        for (int a = 0; a < 2; ++a) {
            const int hd = 4 * w + 2 * a + hp;
#pragma unroll
            for (int u = 0; u < 2; ++u) {
                const int q = 4 * u + g;
                const bool z = (q == 7) || (hd == h && q == p);
                op[a][u] = z ? 0x80000000u : comm_off(1, hd * HS + q * 128 + i8 * 16, 0);
            }
            oe[a] = comm_off(1, hd * HS + 896 + i8 * 64 + g * 16, 0);
        }
        const int hl = 4 * w + 2 * (g >> 1) + hp;
        ost = comm_off(1, ST_OFF + (hl >> 1) * 128 + (hl & 1) * 64 + i8 * 4, 0);
        const bool ownhead = (hl == h);
        v4u selLo = {0, 0, 0, 0}, selHi = {0, 0, 0, 0}, oneLo, oneHi;
        {
            const unsigned one = 0x3F80u << (16 * (rho & 1));
            const int wd = (rho & 7) >> 1;
            v4u sv; sv.x = wd == 0 ? one : 0; sv.y = wd == 1 ? one : 0; sv.z = wd == 2 ? one : 0; sv.w = wd == 3 ? one : 0;
            if (rho < 8) selLo = sv; else selHi = sv;
            const unsigned o2 = 0x3F803F80u;
            oneLo = rho < 8 ? v4u{o2, o2, o2, o2} : v4u{0, 0, 0, 0};
            oneHi = rho < 8 ? v4u{0, 0, 0, 0} : v4u{o2, o2, o2, o2};
        }
        const unsigned opub = comm_off(1, h * HS + p * 128 + (lane >> 1) * 4, 0) + (lane & 1) * 65536u + polo;
        const int pubw = ((h >> 2) + 1) & 3;
        for (int step = 0; step < NSEG; ++step) {
            const bool last = (step == NSEG - 1);
            const unsigned sbs = comm_step_base(step, 0);
            const unsigned sbr = sbs + myc;
            STAMP(step == 1, 0); STAMP(step == 63, 1);
            __builtin_amdgcn_s_barrier();
            TL(0)
            const int skl = (is_v && step > 0) ? j : -1;
            nfail += gather_m<1, false>(rs, sbr, ldsm, w, lane, skl) ? 1 : 0;
            bar();
            TL(1)
            f4 aE = {0, 0, 0, 0}, aO = {0, 0, 0, 0};
            LOADP(mf)
            v2u mb = *(const v2u*)(ldsm + b * 512 + g * 128 + i8 * 16 + t * 8);
            v2u cw = *(const v2u*)(ldsm + 7168);
#pragma unroll
            for (int q = 0; q < 7; ++q) { aE = mfma(wt[2 * q], mf[q], aE); aO = mfma(wt[2 * q + 1], mf[q], aO); }
            f4 xs = hp ? aO : aE;
            v2u gh; gh.x = pk2(xs[0], xs[1]); gh.y = pk2(xs[2], xs[3]);
            f4 sp = mfma16(gh, mb, f4{0, 0, 0, 0});
            if (rho < 8) {
                float* rq = red + rho * 100 + (4 * (g & 1)) * 12 + 2 * w + (g >> 1);
                rq[0] = sp[0]; rq[12] = sp[1]; rq[24] = sp[2]; rq[36] = sp[3];
            }
            SB0
            TL(7)
            bar();
            if (w == pubw && !last) {
                unsigned o = reduce8(red, lane);
                __builtin_amdgcn_raw_buffer_store_b32(o, rs, opub, sbs, 16);
                __builtin_amdgcn_raw_buffer_store_b32(o, rs, opub + 131072u, sbs, 16);
                GT(0, R1n)
            }
            TL(2)
            SB0
            R2 = __builtin_amdgcn_readfirstlane(cw.x); R1n = __builtin_amdgcn_readfirstlane(cw.y);
#define GMSTAGE() { f4 fs; _Pragma("unroll") for (int r = 0; r < 4; ++r) fs[r] = xs[r] + dpp_xor8(xs[r]); \
                v2u gm; gm.x = pk2(fs[0], fs[1]); gm.y = pk2(fs[2], fs[3]); \
                if (rho < 8) *(v2u*)(lds + L_GM + (w >> 1) * 512 + g * 128 + rho * 16 + t * 8) = gm; }
            if (!is_v || last) { GMSTAGE() bar(); if (is_v) bar(); continue; }
#define E_LOADS(sb_) { stt = __builtin_amdgcn_raw_buffer_load_b32(rs, ost, sb_, 16); ef[0] = ld_sc1(rs, oe[0], sb_); ef[1] = ld_sc1(rs, oe[1], sb_); SB0 }
#define P_LOADS(sb_) { pf[0][0] = ld_sc1(rs, op[0][0], sb_); SB0 pf[1][0] = ld_sc1(rs, op[1][0], sb_); SB0 pf[0][1] = ld_sc1(rs, op[0][1], sb_); SB0 pf[1][1] = ld_sc1(rs, op[1][1], sb_); SB0 }
            const unsigned tc2 = approach_rt(R2, 10 + (int)TE);
            spin_to(tc2 - TE * 23u);
            E_LOADS(sbr)
            asm volatile("" ::: "memory");
            spin_to(tc2);
            GT(1, R2)
            TL(3)
            P_LOADS(sbr)
            asm volatile("" ::: "memory");
            SB0
            GMSTAGE()
            v4u uf[2];
            uf[0] = *(const v4u*)(lds + L_UF + w * 2048 + lane * 16); uf[1] = *(const v4u*)(lds + L_UF + w * 2048 + 1024 + lane * 16);
            f4 sini = {0, 0, 0, 0};
            if (__builtin_amdgcn_readfirstlane(h >> 2) == w) {
                float ov[4];
#pragma unroll
                for (int r = 0; r < 4; ++r) {
                    const float* q = red + i8 * 100 + (4 * (g & 1) + r) * 12;
                    f4 a = *(const f4*)q, b2 = *(const f4*)(q + 4);
                    a += b2;
                    ov[r] = (a[0] + a[1]) + (a[2] + a[3]);
                }
                unsigned q0 = pk2(ov[0], ov[1]), q1 = pk2(ov[2], ov[3]);
                if (ownhead) { sini[0] = bflo(q0); sini[1] = bfhi(q0); sini[2] = bflo(q1); sini[3] = bfhi(q1); }
            }
            bar();
            v2u vpA, vpB;
            {
                v2u vp = *(const v2u*)(lds + L_VP + w * 512 + (2 * (g >> 1) + hp) * 128 + i8 * 16 + (g & 1) * 8);
                vpA = g < 2 ? vp : v2u{0, 0};
                vpB = g < 2 ? v2u{0, 0} : vp;
            }
            asm volatile("" ::: "memory");
            SB0
            TL(4)
            bool okall;
            auto combine = [&]() __attribute__((always_inline)) {
                f4 ls = mfma(oneLo, ef[0], f4{0, 0, 0, 0});
                ls = mfma(oneHi, ef[1], ls);
                f4 x0 = mfma(uf[0], ef[0], f4{0, 0, 0, 0});
                f4 x1 = mfma(uf[1], ef[1], f4{0, 0, 0, 0});
                unsigned t2 = vmax4(vmax4(stt, ef[0]), ef[1]);
                float lsv = ls[0];
                asm volatile("" : "+v"(t2), "+v"(lsv));
                SB0
                f4 sacc = mfma(selLo, pf[0][0], sini);
                sacc = mfma(selHi, pf[1][0], sacc);
                sacc = mfma(selLo, pf[0][1], sacc);
                sacc = mfma(selHi, pf[1][1], sacc);
                SB0
                unsigned t0 = vmax4(vmax4(t2, pf[0][0]), pf[0][1]), t1 = vmax4(vmax4(0, pf[1][0]), pf[1][1]);
                asm volatile("" : "+v"(t0), "+v"(t1));
                SB0
                const float mloc = asf(stt);
                float mx = vmax3(vmax3_mfma(sacc[0], sacc[1], sacc[2]), sacc[3], mloc);
                mx = rmax16(mx);
                float p0 = __builtin_amdgcn_exp2f(sacc[0] - mx), p1 = __builtin_amdgcn_exp2f(sacc[1] - mx), p2 = __builtin_amdgcn_exp2f(sacc[2] - mx), p3 = __builtin_amdgcn_exp2f(sacc[3] - mx);
                float f = __builtin_amdgcn_exp2f(mloc - mx);
                v2u pb; pb.x = pk2(p0, p1); pb.y = pk2(p2, p3);
                float zq = (p0 + p1) + (p2 + p3);
                SB0
                f4 cmA = mfma16(vpA, pb, f4{0, 0, 0, 0});
                f4 cmB = mfma16(vpB, pb, f4{0, 0, 0, 0});
                SB0
                float z = xsum16(zq);
                auto fs = __builtin_amdgcn_permlane32_swap(asu(f), asu(f), false, false);
                unsigned fa = fs[0], fb = fs[1];
                z += f * lsv;
                float rz = __builtin_amdgcn_rcpf(z);
                okall = __all(max(t0, t1) != SENT);
                f4 ya = x0 * asf(fa) + cmA, yb = x1 * asf(fb) + cmB;
                auto rs2 = __builtin_amdgcn_permlane32_swap(asu(rz), asu(rz), false, false);
                unsigned ra = rs2[0], rb = rs2[1];
                f4 cx = ya * asf(ra) + yb * asf(rb);
                if (hp == (g >> 1)) {
                    float* rq = red2 + i8 * 100 + (4 * (g & 1)) * 12 + 2 * w + hp;
                    rq[0] = cx[0]; rq[12] = cx[1]; rq[24] = cx[2]; rq[36] = cx[3];
                }
            };
            combine();
            if (!okall) {
                nfail2++;
                for (unsigned spin = 0; spin < SPIN_MAX && !okall; ++spin) {
                    __builtin_amdgcn_s_sleep(1);
                    asm volatile("" ::: "memory");
                    E_LOADS(sbr)
                    P_LOADS(sbr)
                    combine();
                }
            }
            TL(6)
            bar();
        }
        __builtin_amdgcn_s_barrier();
    }
    if (lane == 0 && w < 4) dbg[16 + role * 4 + w] = nfail | ((unsigned long long)nfail2 << 32);
}

DI void signal_add(unsigned char* ws, unsigned off) {
    asm volatile("s_waitcnt vmcnt(0)" ::: "memory");
    __syncthreads();
    if (threadIdx.x == 0) __hip_atomic_fetch_add((unsigned*)(ws + CTRL_OFF + off), 1u, __ATOMIC_RELAXED, __HIP_MEMORY_SCOPE_AGENT);
}
DI void wg_wait(__amdgpu_buffer_rsrc_t rs, unsigned off_lane, unsigned need, int slp) {
    if (threadIdx.x < 64) wait_flags(rs, off_lane, need, slp);
    __syncthreads();
}

DI void gu_role(unsigned char* ws, unsigned char* lds, const int gw) {
    const int tid = threadIdx.x, lane = tid & 63, w8 = __builtin_amdgcn_readfirstlane(tid >> 6);
    const int w = w8 & 3, sp = w8 >> 2;
    const bool isG = gw < 56;
    const int nat0 = (((lane & 15) >> 2) & 1) * 14336 + ((4 * ((lane & 15) >> 3) + (lane & 3)) + 16 * (lane >> 4)) * 16;
    v4u stg[7];
#pragma unroll
    for (int i = 0; i < 7; ++i) stg[i] = *(const v4u*)(ws + XF_OFF + (i * 512 + tid) * 16);
    v4u wt[2][14];
    const unsigned char* wsrc = isG ? ws + AS_OFF + (size_t)(4 * gw + w) * 28 * 1024 : ws + BS_OFF + ((size_t)(gw - 56) * 8 + 2 * w) * 14 * 1024;
#pragma unroll
    for (int t = 0; t < 2; ++t)
#pragma unroll
        for (int ks = 0; ks < 14; ++ks) wt[t][ks] = *(const v4u*)(wsrc + (t * 14 + ks) * 1024 + lane * 16);
    __amdgpu_buffer_rsrc_t rs = mkrs(ws, 0x7fffffff);
#pragma unroll
    for (int i = 0; i < 7; ++i) *(v4u*)(lds + (i * 512 + tid) * 16) = stg[i];
    __syncthreads();
    const int bb = 4 * gw + w, hh = bb / 14, bq = bb % 14;
    const unsigned obase = isG ? (unsigned)(G_OFF + ((size_t)(hh * 2) * 14 + bq) * 1024) + lane * 16 : (unsigned)(U_OFF + ((size_t)(gw - 56) * 8 + 2 * w) * 1024) + lane * 16;
    const unsigned ostep = isG ? 14336u : 1024u;
    for (int pr = 0; pr < 32; ++pr) {
        const unsigned char* cur = lds + (pr & 1) * 57344 + sp * 28672;
        if (pr + 1 < 32) {
#pragma unroll
            for (int i = 0; i < 7; ++i) stg[i] = *(const v4u*)(ws + XF_OFF + (size_t)(pr + 1) * 57344 + (i * 512 + tid) * 16);
        }
        f4 acc[2][2] = {{f4{0,0,0,0}, f4{0,0,0,0}}, {f4{0,0,0,0}, f4{0,0,0,0}}};
        if (isG) {
#pragma unroll
            for (int ks = 0; ks < 14; ++ks) {
                v4u x0 = *(const v4u*)(cur + ks * 1024 + nat0), x1 = *(const v4u*)(cur + ks * 1024 + nat0 + 128);
                acc[0][0] = mfma(wt[0][ks], x0, acc[0][0]); acc[1][0] = mfma(wt[1][ks], x0, acc[1][0]);
                acc[0][1] = mfma(wt[0][ks], x1, acc[0][1]); acc[1][1] = mfma(wt[1][ks], x1, acc[1][1]);
            }
        } else {
#pragma unroll
            for (int ks = 0; ks < 14; ++ks) {
                v4u x0 = *(const v4u*)(cur + ks * 1024 + lane * 16), x1 = *(const v4u*)(cur + (14 + ks) * 1024 + lane * 16);
                acc[0][0] = mfma(x0, wt[0][ks], acc[0][0]); acc[1][0] = mfma(x1, wt[0][ks], acc[1][0]);
                acc[0][1] = mfma(x0, wt[1][ks], acc[0][1]); acc[1][1] = mfma(x1, wt[1][ks], acc[1][1]);
            }
        }
        if (pr + 1 < 32) {
            unsigned char* nxt = lds + ((pr + 1) & 1) * 57344;
#pragma unroll
            for (int i = 0; i < 7; ++i) *(v4u*)(nxt + (i * 512 + tid) * 16) = stg[i];
        }
        const unsigned so = __builtin_amdgcn_readfirstlane((2 * pr + sp) * 458752);
        st_sc1(pack8(acc[0][0], acc[1][0]), rs, obase, so);
        st_sc1(pack8(acc[0][1], acc[1][1]), rs, obase + ostep, so);
        const bool drain = (pr < 2) || (pr == 31);
        if (drain) asm volatile("s_waitcnt vmcnt(0)" ::: "memory");
        else asm volatile("s_waitcnt vmcnt(2)" ::: "memory");
        bar();
        if (tid == 0) __builtin_amdgcn_raw_buffer_store_b32((unsigned)(drain ? 2 * pr + 2 : 2 * pr), rs, (unsigned)CTRL_OFF + C_GU + 128u * gw, 0, 16);
    }
}

DI void phi_pair(f4 d0, f4 d1, int g, v4u& pos, v4u& neg) {
    float v[8] = {d0[0], d0[1], d0[2], d0[3], d1[0], d1[1], d1[2], d1[3]};
    const bool tail = (g == 3);
    float mx = fmaxf(fmaxf(v[0], v[1]), fmaxf(v[2], v[3])), mn = fminf(fminf(v[0], v[1]), fminf(v[2], v[3]));
    float mx2 = fmaxf(fmaxf(v[4], v[5]), fmaxf(v[6], v[7])), mn2 = fminf(fminf(v[4], v[5]), fminf(v[6], v[7]));
    mx = tail ? mx : fmaxf(mx, mx2); mn = tail ? mn : fminf(mn, mn2);
    mx = xmax32(xmax16(mx)); mn = -xmax32(xmax16(-mn));
    float ep[8], en[8], sp = 0.f, sn = 0.f;
#pragma unroll
    for (int k = 0; k < 8; ++k) {
        ep[k] = __builtin_amdgcn_exp2f(v[k] - mx); en[k] = __builtin_amdgcn_exp2f(mn - v[k]);
        if (k >= 4) { ep[k] = tail ? 0.f : ep[k]; en[k] = tail ? 0.f : en[k]; }
        sp += ep[k]; sn += en[k];
    }
    sp = xsum32(xsum16(sp)); sn = xsum32(xsum16(sn));
    const float rp = __builtin_amdgcn_rcpf(sp), rn = __builtin_amdgcn_rcpf(sn);
    pos = v4u{pk2(ep[0] * rp, ep[1] * rp), pk2(ep[2] * rp, ep[3] * rp), pk2(ep[4] * rp, ep[5] * rp), pk2(ep[6] * rp, ep[7] * rp)};
    neg = v4u{pk2(en[0] * rn, en[1] * rn), pk2(en[2] * rn, en[3] * rn), pk2(en[4] * rn, en[5] * rn), pk2(en[6] * rn, en[7] * rn)};
}
DI void stage_x64(unsigned char* ws, unsigned char* lds, int blk64) {
    const int tid = threadIdx.x;
    v4u stg[7];
#pragma unroll
    for (int i = 0; i < 7; ++i) stg[i] = *(const v4u*)(ws + AXF_OFF + (size_t)blk64 * 57344 + (i * 512 + tid) * 16);
#pragma unroll
    for (int i = 0; i < 7; ++i) *(v4u*)(lds + (i * 512 + tid) * 16) = stg[i];
}

DI void a1_unit(unsigned char* ws, unsigned char* lds, const int u) {
    const int tid = threadIdx.x, lane = tid & 63, w = __builtin_amdgcn_readfirstlane(tid >> 6);
    const int rho = lane & 15, g = lane >> 4;
    __amdgpu_buffer_rsrc_t rs = mkrs(ws, 0x7fffffff);
    unsigned char* T = lds + 57344 + w * 9216;
    constexpr int TS = 144;
    f4 kv[2][4][2];
#pragma unroll
    for (int a = 0; a < 2; ++a)
#pragma unroll
        for (int b = 0; b < 4; ++b) { kv[a][b][0] = f4{0,0,0,0}; kv[a][b][1] = f4{0,0,0,0}; }
    for (int pass = 0; pass < 2; ++pass) {
        __syncthreads();
        stage_x64(ws, lds, 2 * u + pass);
        __syncthreads();
#pragma unroll
        for (int hi = 0; hi < 2; ++hi) {
            const int h = 2 * w + hi;
            const unsigned char* wk = ws + WQF_OFF + WQF_ONE + (size_t)h * 28672 + lane * 16;
            const unsigned char* wv = ws + WQF_OFF + 2 * WQF_ONE + (size_t)h * 28672 + lane * 16;
            f4 ak[2][4], av[2][4];
#pragma unroll
            for (int t = 0; t < 2; ++t)
#pragma unroll
                for (int q = 0; q < 4; ++q) { ak[t][q] = f4{0,0,0,0}; av[t][q] = f4{0,0,0,0}; }
#pragma unroll 2
            for (int ks = 0; ks < 14; ++ks) {
                v4u k0 = *(const v4u*)(wk + ks * 1024), k1 = *(const v4u*)(wk + (14 + ks) * 1024);
                v4u v0 = *(const v4u*)(wv + ks * 1024), v1 = *(const v4u*)(wv + (14 + ks) * 1024);
#pragma unroll
                for (int q = 0; q < 4; ++q) {
                    v4u x = *(const v4u*)(lds + (q * 14 + ks) * 1024 + lane * 16);
                    ak[0][q] = mfma(k0, x, ak[0][q]); ak[1][q] = mfma(k1, x, ak[1][q]);
                    av[0][q] = mfma(x, v0, av[0][q]); av[1][q] = mfma(x, v1, av[1][q]);
                }
            }
#pragma unroll
            for (int q = 0; q < 4; ++q) {
                v4u pos, neg; phi_pair(ak[0][q], ak[1][q], g, pos, neg);
                const int tok = 32 * (q >> 1) + perm32(q & 1, rho);
                *(v4u*)(T + tok * TS + g * 16) = pos;
                *(v4u*)(T + tok * TS + 64 + g * 16) = neg;
            }
            asm volatile("s_waitcnt lgkmcnt(0)" ::: "memory");
            v4u vf[2][2];
#pragma unroll
            for (int bk = 0; bk < 2; ++bk)
#pragma unroll
                for (int t = 0; t < 2; ++t) {
                    vf[bk][t] = pack8(av[t][2 * bk], av[t][2 * bk + 1]);
                    if (t == 1 && rho == 12) vf[bk][t] = v4u{0x3F803F80u, 0x3F803F80u, 0x3F803F80u, 0x3F803F80u};
                }
            const unsigned trb = (8 * g + (rho >> 2)) * TS + (lane & 3) * 8;
#pragma unroll
            for (int bk = 0; bk < 2; ++bk)
#pragma unroll
                for (int ft = 0; ft < 4; ++ft) {
                    v2u lo = trread(T + trb + (32 * bk) * TS + ft * 32), hi2 = trread(T + trb + (32 * bk + 4) * TS + ft * 32);
                    v4u pa = {lo.x, lo.y, hi2.x, hi2.y};
                    kv[hi][ft][0] = mfma(pa, vf[bk][0], kv[hi][ft][0]);
                    kv[hi][ft][1] = mfma(pa, vf[bk][1], kv[hi][ft][1]);
                }
            asm volatile("s_waitcnt lgkmcnt(0)" ::: "memory");
        }
    }
#pragma unroll
    for (int hi = 0; hi < 2; ++hi)
#pragma unroll
        for (int ft = 0; ft < 4; ++ft)
#pragma unroll
            for (int dt = 0; dt < 2; ++dt)
                st_sc1(__builtin_bit_cast(v4u, kv[hi][ft][dt]), rs, (unsigned)(KVP_OFF + ((size_t)(u * 16 + 2 * w + hi) * 8 + ft * 2 + dt) * 1024) + lane * 16, 0);
}
DI void a15_unit(unsigned char* ws, unsigned char* lds, const int h) {
    const int tid = threadIdx.x, lane = tid & 63, w = tid >> 6;
    __amdgpu_buffer_rsrc_t rs = mkrs(ws, 0x7fffffff);
    f4 s = {0, 0, 0, 0};
    v4u t[16];
#pragma unroll
    for (int u = 0; u < 16; ++u) t[u] = ld_sc1(rs, (unsigned)(KVP_OFF + ((size_t)(u * 16 + h) * 8) * 1024) + tid * 16, 0);
#pragma unroll
    for (int u = 0; u < 16; ++u) s += __builtin_bit_cast(f4, t[u]);
    const int ft = w >> 1, dt = w & 1, rho = lane & 15, g = lane >> 4;
    __syncthreads();
    v2u o; o.x = pk2(s[0], s[1]); o.y = pk2(s[2], s[3]);
    *(v2u*)(lds + (16 * dt + rho) * 128 + (16 * ft + 4 * g) * 2) = o;
    __syncthreads();
    if (tid < 256) {
        const int fr = tid >> 6;
        const int tt = fr >> 1, kb = fr & 1;
        v4u v = *(const v4u*)(lds + perm32(tt, rho) * 128 + (32 * kb + 8 * g) * 2);
        st_sc1(v, rs, (unsigned)(KVF_OFF + (size_t)(h * 4 + fr) * 1024) + lane * 16, 0);
    }
}
DI void a2_unit(unsigned char* ws, unsigned char* lds, const int u, u16* __restrict__ out) {
    int tid = threadIdx.x; asm volatile("" : "+v"(tid));
    const int lane = tid & 63, w = __builtin_amdgcn_readfirstlane(tid >> 6);
    const int rho = lane & 15, g = lane >> 4;
    __amdgpu_buffer_rsrc_t rs = mkrs(ws, 0x7fffffff);
    unsigned char* O = lds + 57344;
    __syncthreads();
    stage_x64(ws, lds, u);
    __syncthreads();
    v4u pos[2][4], neg[2][4];
#pragma unroll
    for (int hi = 0; hi < 2; ++hi) {
        const int h = 2 * w + hi;
        const unsigned char* wq = ws + WQF_OFF + (size_t)h * 28672 + lane * 16;
        f4 aq[2][4];
#pragma unroll
        for (int t = 0; t < 2; ++t)
#pragma unroll
            for (int q = 0; q < 4; ++q) aq[t][q] = f4{0,0,0,0};
#pragma unroll 2
        for (int ks = 0; ks < 14; ++ks) {
            v4u k0 = *(const v4u*)(wq + ks * 1024), k1 = *(const v4u*)(wq + (14 + ks) * 1024);
#pragma unroll
            for (int q = 0; q < 4; ++q) {
                v4u x = *(const v4u*)(lds + (q * 14 + ks) * 1024 + lane * 16);
                aq[0][q] = mfma(k0, x, aq[0][q]); aq[1][q] = mfma(k1, x, aq[1][q]);
            }
        }
#pragma unroll
        for (int q = 0; q < 4; ++q) phi_pair(aq[0][q], aq[1][q], g, pos[hi][q], neg[hi][q]);
    }
    wg_wait(rs, C_KV, 16, 4);
#pragma unroll
    for (int hi = 0; hi < 2; ++hi) {
        const int h = 2 * w + hi;
        v4u kf[4];
#pragma unroll
        for (int fr = 0; fr < 4; ++fr) kf[fr] = ld_sc1(rs, (unsigned)(KVF_OFF + (size_t)(h * 4 + fr) * 1024) + lane * 16, 0);
#pragma unroll
        for (int q = 0; q < 4; ++q) {
            f4 n0 = mfma(kf[0], pos[hi][q], f4{0,0,0,0}); n0 = mfma(kf[1], neg[hi][q], n0);
            f4 n1 = mfma(kf[2], pos[hi][q], f4{0,0,0,0}); n1 = mfma(kf[3], neg[hi][q], n1);
            const float den = asf((unsigned)__builtin_amdgcn_ds_bpermute((48 + rho) * 4, (int)asu(n1[0])));
            const float rd = 1.0f / (den + 1e-6f);
            if (g == 3) n1 = f4{0, 0, 0, 0};
            *(v4u*)(O + (h * 4 + q) * 1024 + lane * 16) = pack8(n0 * rd, n1 * rd);
        }
    }
    __syncthreads();
    for (int cb = w; cb < 14; cb += 8) {
        f4 acc[2][4];
#pragma unroll
        for (int t = 0; t < 2; ++t)
#pragma unroll
            for (int q = 0; q < 4; ++q) acc[t][q] = f4{0,0,0,0};
        const unsigned char* wo = ws + WOF_OFF + (size_t)cb * 32768 + lane * 16;
#pragma unroll 4
        for (int h = 0; h < 16; ++h) {
            v4u w0 = *(const v4u*)(wo + h * 1024), w1 = *(const v4u*)(wo + (16 + h) * 1024);
#pragma unroll
            for (int q = 0; q < 4; ++q) {
                v4u o = *(const v4u*)(O + (h * 4 + q) * 1024 + lane * 16);
                acc[0][q] = mfma(w0, o, acc[0][q]); acc[1][q] = mfma(w1, o, acc[1][q]);
            }
        }
#pragma unroll
        for (int q = 0; q < 4; ++q) {
            const int tok = u * 64 + 32 * (q >> 1) + perm32(q & 1, rho);
            *(v4u*)(out + (size_t)tok * OUTW + 32 * cb + 8 * g) = pack8(acc[0][q], acc[1][q]);
        }
    }
}

DI void p2_unit(unsigned char* ws, unsigned char* lds, const int unit, u16* __restrict__ out) {
    const int seg = unit >> 1, chalf = unit & 1;
    int tid = threadIdx.x; asm volatile("" : "+v"(tid));
    const int lane = tid & 63, w = __builtin_amdgcn_readfirstlane(tid >> 6);
    const int rho = lane & 15, g = lane >> 4;
    __amdgpu_buffer_rsrc_t rs = mkrs(ws, 0x7fffffff);
    unsigned char* xl = lds + 40960;
    __syncthreads();
    for (int i = tid; i < 1792; i += 512) *(v4u*)(xl + i * 16) = *(const v4u*)(ws + XF_OFF + (size_t)seg * 28672 + i * 16);
    if (tid < 128) wait_flags(rs, C_GU + 128u * (unsigned)min(tid, 111), seg + 1, 8);
    __syncthreads();
    f4 sx[2][2][2];
#pragma unroll
    for (int hi = 0; hi < 2; ++hi) {
#pragma unroll
        for (int a = 0; a < 2; ++a) { sx[hi][a][0] = f4{0,0,0,0}; sx[hi][a][1] = f4{0,0,0,0}; }
        const unsigned gb = (unsigned)(G_OFF + (size_t)(seg * 16 + 2 * w + hi) * 28672) + lane * 16;
#pragma unroll 7
        for (int ks = 0; ks < 14; ++ks) {
            v4u g0 = ld_sc1(rs, gb + ks * 1024, 0), g1 = ld_sc1(rs, gb + (14 + ks) * 1024, 0);
            v4u x0 = *(const v4u*)(xl + ks * 1024 + lane * 16), x1 = *(const v4u*)(xl + (14 + ks) * 1024 + lane * 16);
            sx[hi][0][0] = mfma(g0, x0, sx[hi][0][0]); sx[hi][0][1] = mfma(g0, x1, sx[hi][0][1]);
            sx[hi][1][0] = mfma(g1, x0, sx[hi][1][0]); sx[hi][1][1] = mfma(g1, x1, sx[hi][1][1]);
        }
    }
    const int cb = 7 * chalf + (w < 7 ? w : 6);
    v4u uf[2][16];
    unsigned uo[2], vo[2];
#pragma unroll
    for (int t = 0; t < 2; ++t) {
        const int c = 32 * cb + perm32(t, rho); const int j = c >> 3, c8 = c & 7;
        uo[t] = (unsigned)(U_OFF + (size_t)(seg * 56 + j) * 8192) + (c8 + 16 * g) * 16;
        vo[t] = (unsigned)(VP_OFF + (size_t)(seg * 56 + j) * 2048) + g * 128 + c8 * 16;
#pragma unroll
        for (int h = 0; h < 16; ++h) uf[t][h] = ld_sc1(rs, uo[t] + (h >> 1) * 1024 + (h & 1) * 128, 0);
    }
    if (tid < 64) { if (seg < 62) wait_flags(rs, C_REC, seg + 1, 8); else wait_flags(rs, C_DONE, 112, 2); }
    __syncthreads();
    f4 sm[2] = {f4{0,0,0,0}, f4{0,0,0,0}};
    {
        const unsigned gmb = (unsigned)(GM_OFF + (size_t)(seg * 16 + 2 * w + (rho >> 3)) * 7168) + g * 128 + (rho & 7) * 16;
        v4u gm[10];
#pragma unroll
        for (int ks = 0; ks < 10; ++ks) gm[ks] = ld_sc1(rs, gmb + ks * 512, 0);
        SB0
#pragma unroll
        for (int ks = 0; ks < 14; ++ks) {
            v4u x0 = *(const v4u*)(xl + ks * 1024 + lane * 16), x1 = *(const v4u*)(xl + (14 + ks) * 1024 + lane * 16);
            sm[0] = mfma(gm[ks % 10], x0, sm[0]); sm[1] = mfma(gm[ks % 10], x1, sm[1]);
            if (ks < 4) gm[ks] = ld_sc1(rs, gmb + (10 + ks) * 512, 0);
            SB0
        }
    }
    v4u vf[2][4];
#pragma unroll
    for (int t = 0; t < 2; ++t)
#pragma unroll
        for (int w4 = 0; w4 < 4; ++w4) vf[t][w4] = ld_sc1(rs, vo[t] + w4 * 512, 0);
#pragma unroll
    for (int hi = 0; hi < 2; ++hi) {
        const int h = 2 * w + hi;
        const bool own = ((g >> 1) == hi);
#pragma unroll
        for (int tp = 0; tp < 2; ++tp) {
            const int tq = perm32(tp, rho);
            float v[8];
#pragma unroll
            for (int tau = 0; tau < 2; ++tau)
#pragma unroll
                for (int r = 0; r < 4; ++r) { const int key = 16 * tau + 4 * g + r; v[4 * tau + r] = (key <= tq) ? sx[hi][tau][tp][r] : -3.0e38f; }
            float mx = fmaxf(fmaxf(fmaxf(v[0], v[1]), fmaxf(v[2], v[3])), fmaxf(fmaxf(v[4], v[5]), fmaxf(v[6], v[7])));
            const float mo = fmaxf(fmaxf(sm[tp][0], sm[tp][1]), fmaxf(sm[tp][2], sm[tp][3]));
            mx = own ? fmaxf(mx, mo) : mx;
            mx = xmax32(xmax16(mx));
            float sum = 0.f, em[4];
#pragma unroll
            for (int k = 0; k < 8; ++k) { v[k] = __builtin_amdgcn_exp2f(v[k] - mx); sum += v[k]; }
#pragma unroll
            for (int r = 0; r < 4; ++r) { em[r] = own ? __builtin_amdgcn_exp2f(sm[tp][r] - mx) : 0.f; sum += em[r]; }
            sum = xsum32(xsum16(sum));
            const float rz = __builtin_amdgcn_rcpf(sum);
            v2u p0; p0.x = pk2(v[0] * rz, v[1] * rz); p0.y = pk2(v[2] * rz, v[3] * rz);
            v2u p1; p1.x = pk2(v[4] * rz, v[5] * rz); p1.y = pk2(v[6] * rz, v[7] * rz);
            *(v2u*)(lds + (h * 2 + tp) * 1024 + (rho + 16 * (g >> 1)) * 16 + (g & 1) * 8) = p0;
            *(v2u*)(lds + (h * 2 + tp) * 1024 + (rho + 16 * (2 + (g >> 1))) * 16 + (g & 1) * 8) = p1;
            if (own) {
                v2u pm; pm.x = pk2(em[0] * rz, em[1] * rz); pm.y = pk2(em[2] * rz, em[3] * rz);
                *(v2u*)(lds + ((16 + (h >> 2)) * 2 + tp) * 1024 + (rho + 16 * (h & 3)) * 16 + (g & 1) * 8) = pm;
            }
        }
    }
    __syncthreads();
    if (w < 7) {
        f4 acc[2][2] = {{f4{0,0,0,0}, f4{0,0,0,0}}, {f4{0,0,0,0}, f4{0,0,0,0}}};
#pragma unroll
        for (int h = 0; h < 16; ++h) {
            v4u p0 = *(const v4u*)(lds + (h * 2) * 1024 + lane * 16), p1 = *(const v4u*)(lds + (h * 2 + 1) * 1024 + lane * 16);
#pragma unroll
            for (int t = 0; t < 2; ++t) { acc[t][0] = mfma(uf[t][h], p0, acc[t][0]); acc[t][1] = mfma(uf[t][h], p1, acc[t][1]); }
        }
#pragma unroll
        for (int w4 = 0; w4 < 4; ++w4) {
            v4u p0 = *(const v4u*)(lds + ((16 + w4) * 2) * 1024 + lane * 16), p1 = *(const v4u*)(lds + ((16 + w4) * 2 + 1) * 1024 + lane * 16);
#pragma unroll
            for (int t = 0; t < 2; ++t) { acc[t][0] = mfma(vf[t][w4], p0, acc[t][0]); acc[t][1] = mfma(vf[t][w4], p1, acc[t][1]); }
        }
#pragma unroll
        for (int tp = 0; tp < 2; ++tp) {
            const int tok = seg * 32 + perm32(tp, rho);
            *(v4u*)(out + (size_t)tok * OUTW + 448 + 32 * cb + 8 * g) = pack8(acc[0][tp], acc[1][tp]);
        }
    }
}

constexpr int LDS_MAIN = 135168;
__global__ void __launch_bounds__(512, 2) k_main(unsigned char* ws, u16* __restrict__ out, int dS, int dV, int dVm) {
    extern __shared__ __attribute__((aligned(16))) unsigned char lds[];
    const int bidx = blockIdx.x;
    const int idx = (bidx >> 3) * 4 + (bidx & 3);
    if ((bidx & 7) < 4) { rec_role(ws, lds, idx, dS, dV, dVm); return; }
    __amdgpu_buffer_rsrc_t rs = mkrs(ws, 0x7fffffff);
    if (idx < 112) gu_role(ws, lds, idx);
    else {
        const int u = idx - 112;
        a1_unit(ws, lds, u);
        signal_add(ws, C_A1);
        wg_wait(rs, C_A1, 16, 4);
        a15_unit(ws, lds, u);
        signal_add(ws, C_KV);
    }
    for (;;) {
        __syncthreads();
        if (threadIdx.x == 0) *(volatile unsigned*)(lds + LDS_MAIN - 16) = __hip_atomic_fetch_add((unsigned*)(ws + CTRL_OFF + C_TICKET), 1u, __ATOMIC_RELAXED, __HIP_MEMORY_SCOPE_AGENT);
        __syncthreads();
        const int t = __builtin_amdgcn_readfirstlane(*(volatile unsigned*)(lds + LDS_MAIN - 16));
        if (t >= 160) break;
        if (t < 32) a2_unit(ws, lds, t, out);
        else p2_unit(ws, lds, t - 32, out);
    }
}

#define REC_DS 88
#define REC_DV 82
#define REC_DVM 3
constexpr int g_dS = REC_DS, g_dV = REC_DV, g_dVm = REC_DVM;

extern "C" void kernel_launch(void* const* d_in, const int* in_sizes, int n_in, void* d_out, int out_size, void* d_ws, size_t ws_size, hipStream_t stream) {
    const u16* A_x = (const u16*)d_in[0]; const u16* A_wqkv = (const u16*)d_in[1]; const u16* A_wphi = (const u16*)d_in[2]; const u16* A_wo = (const u16*)d_in[3];
    const u16* B_x = (const u16*)d_in[4]; const u16* B_mem0 = (const u16*)d_in[5];
    const u16* Wq = (const u16*)d_in[6]; const u16* Wk = (const u16*)d_in[7]; const u16* Wv = (const u16*)d_in[8]; const u16* Wo = (const u16*)d_in[9];
    u16* out = (u16*)d_out;
    unsigned char* ws = (unsigned char*)(((uintptr_t)d_ws + 2 * MB - 1) & ~(uintptr_t)(2 * MB - 1));
    (void)hipFuncSetAttribute((const void*)k_pre, hipFuncAttributeMaxDynamicSharedMemorySize, 61440);
    (void)hipFuncSetAttribute((const void*)k_main, hipFuncAttributeMaxDynamicSharedMemorySize, LDS_MAIN);
    k_pre<<<256, 448, 61440, stream>>>(ws, Wq, Wk, Wv, Wo, B_x, B_mem0, A_x, A_wqkv, A_wphi, A_wo);
    k_main<<<256, 512, LDS_MAIN, stream>>>(ws, out, g_dS, g_dV, g_dVm);
}
```

```cpp
#include <hip/hip_runtime.h>
#include <hip/hip_bf16.h>
#include <stdint.h>

typedef unsigned short u16;
typedef unsigned v4u __attribute__((ext_vector_type(4)));
typedef unsigned v2u __attribute__((ext_vector_type(2)));
typedef float f4 __attribute__((ext_vector_type(4)));
typedef float f2 __attribute__((ext_vector_type(2)));
typedef short s8 __attribute__((ext_vector_type(8)));
typedef __bf16 bf2 __attribute__((ext_vector_type(2)));
#define DI __device__ __forceinline__

constexpr int D = 448, H = 16, DH = 256, MEM = 8, SEG = 32, NSEG = 64, S_TOK = 2048;
constexpr int NKS = 14;
constexpr int OUTW = 896;
constexpr unsigned SENT = 0xFFFFFFFFu;
constexpr float LOG2E = 1.4426950408889634f;

constexpr size_t MB = 1024 * 1024;
constexpr int KCOPY = 4;
constexpr size_t COMM_OFF = 0;
constexpr size_t COMM_BYTES = 2 * MB;
constexpr size_t CTRL_OFF = COMM_BYTES;
constexpr size_t AS_OFF = CTRL_OFF + 262144;
constexpr size_t AS_BYTES = (size_t)16 * 14 * 2 * 14 * 1024;
constexpr size_t BS_OFF = AS_OFF + AS_BYTES;
constexpr size_t BS_BYTES = (size_t)56 * 8 * 14 * 1024;
constexpr size_t XF_OFF = BS_OFF + BS_BYTES;
constexpr size_t XF_BYTES = (size_t)64 * 28 * 1024;
constexpr size_t G_OFF = XF_OFF + XF_BYTES;
constexpr size_t G_BYTES = (size_t)64 * 16 * 28 * 1024;
constexpr size_t U_OFF = G_OFF + G_BYTES;
constexpr size_t U_BYTES = (size_t)64 * 56 * 8 * 1024;
constexpr size_t GM_OFF = U_OFF + U_BYTES;
constexpr size_t GM_BYTES = (size_t)64 * 16 * 14 * 512;
constexpr size_t VP_OFF = GM_OFF + GM_BYTES;
constexpr size_t VP_BYTES = (size_t)64 * 56 * 4 * 512;
constexpr size_t AXF_OFF = VP_OFF + VP_BYTES;
constexpr size_t WQF_OFF = AXF_OFF + XF_BYTES;
constexpr size_t WQF_ONE = (size_t)16 * 2 * 14 * 1024;
constexpr size_t WOF_OFF = WQF_OFF + 3 * WQF_ONE;
constexpr size_t KVP_OFF = WOF_OFF + WQF_ONE;
constexpr size_t KVF_OFF = KVP_OFF + (size_t)16 * 16 * 8192;
constexpr size_t WS_END = KVF_OFF + 65536;
constexpr unsigned C_POL = 0, C_REC = 128, C_DONE = 256, C_A1 = 384, C_KV = 512, C_TICKET = 640, C_A0 = 768, C_GU = 1024;
constexpr unsigned C_DBG = 32768;

DI unsigned pk2(float a, float b) { f2 v = {a, b}; return __builtin_bit_cast(unsigned, __builtin_convertvector(v, bf2)); }
DI float asf(unsigned x) { return __builtin_bit_cast(float, x); }
DI unsigned asu(float x) { return __builtin_bit_cast(unsigned, x); }
DI float bflo(unsigned x) { return __builtin_bit_cast(float, x << 16); }
DI float bfhi(unsigned x) { return __builtin_bit_cast(float, x & 0xffff0000u); }
DI float bf2f(u16 x) { return __builtin_bit_cast(float, (unsigned)x << 16); }
DI f4 mfma(v4u a, v4u b, f4 c) { return __builtin_amdgcn_mfma_f32_16x16x32_bf16(__builtin_bit_cast(s8, a), __builtin_bit_cast(s8, b), c, 0, 0, 0); }
DI v4u pack8(f4 a, f4 b) { v4u r; r.x = pk2(a[0], a[1]); r.y = pk2(a[2], a[3]); r.z = pk2(b[0], b[1]); r.w = pk2(b[2], b[3]); return r; }
DI v4u nosent(v4u v) { v.x = min(v.x, 0xFFFFFFFEu); v.y = min(v.y, 0xFFFFFFFEu); v.z = min(v.z, 0xFFFFFFFEu); v.w = min(v.w, 0xFFFFFFFEu); return v; }
DI int perm32(int t, int rho) { return 8 * (rho >> 2) + 4 * t + (rho & 3); }
DI __amdgpu_buffer_rsrc_t mkrs(void* p, unsigned bytes) { return __builtin_amdgcn_make_buffer_rsrc(p, 0, bytes, 0x00020000); }
DI v4u ld_sc1(__amdgpu_buffer_rsrc_t rs, unsigned voff, unsigned soff) { return __builtin_amdgcn_raw_buffer_load_b128(rs, voff, soff, 16); }
DI v2u ld2_sc1(__amdgpu_buffer_rsrc_t rs, unsigned voff, unsigned soff) { return __builtin_amdgcn_raw_buffer_load_b64(rs, voff, soff, 16); }
DI void st_sc1(v4u v, __amdgpu_buffer_rsrc_t rs, unsigned voff, unsigned soff) { __builtin_amdgcn_raw_buffer_store_b128(v, rs, voff, soff, 16); }
DI void st2_sc1(v2u v, __amdgpu_buffer_rsrc_t rs, unsigned voff, unsigned soff) { __builtin_amdgcn_raw_buffer_store_b64(v, rs, voff, soff, 16); }
DI unsigned long long mtime() { unsigned long long t; asm volatile("s_memtime %0\n\ts_waitcnt lgkmcnt(0)" : "=s"(t) :: "memory"); return t; }

typedef short s4t __attribute__((ext_vector_type(4)));
DI v2u trread(const unsigned char* p) { return __builtin_bit_cast(v2u, __builtin_amdgcn_ds_read_tr16_b64_v4i16((__attribute__((address_space(3))) s4t*)p)); }
DI unsigned comm_step_base(int step, int copy) { return __builtin_amdgcn_readfirstlane((unsigned)(step & 7) * 262144u + (unsigned)copy * 65536u); }
DI unsigned comm_off(int chunk0, unsigned o, unsigned pol) { return (unsigned)(chunk0 + (o >> 13)) * 16384u + pol * 8192u + (o & 8191u); }

__global__ void __launch_bounds__(448) k_pre(unsigned char* ws, const u16* __restrict__ Wq, const u16* __restrict__ Wk, const u16* __restrict__ Wv, const u16* __restrict__ Wo,
                                             const u16* __restrict__ bx, const u16* __restrict__ mem0) {
    extern __shared__ __attribute__((aligned(16))) unsigned char lds[];
    const int tid = threadIdx.x, lane = tid & 63, w = __builtin_amdgcn_readfirstlane(tid >> 6);
    const int wg = blockIdx.x;
#define PP(i)
    const int h = (wg & 7) * 2 + ((wg >> 3) & 1), rest = wg >> 4;
    const int mat = rest >> 3, rh = (rest >> 2) & 1, q = rest & 3;
    const int rho = lane & 15, g = lane >> 4;
    const int rb = 7 * rh + w;
    __amdgpu_buffer_rsrc_t rs = mkrs(ws, 0x7fffffff);
    const int gt = wg * 448 + tid; constexpr int GN = 256 * 448;
    constexpr int WST = 240;
    v4u bt[8];
    if (mat == 0) {
        const int ct = 7 * q + w; const int d = 32 * (ct >> 1) + perm32(ct & 1, rho);
#pragma unroll
        for (int es = 0; es < 8; ++es) bt[es] = *(const v4u*)(Wq + ((size_t)d * H + h) * DH + es * 32 + g * 8);
    } else {
#pragma unroll
        for (int it = 0; it < 8; ++it) {
            const int idx = it * 448 + tid; const int e = idx / 14, c8 = idx % 14;
            bt[it] = *(const v4u*)(Wo + ((size_t)h * DH + e) * D + 112 * q + c8 * 8);
        }
    }
    const u16* Wr = mat ? Wv : Wk;
    v4u a[2][8];
#pragma unroll
    for (int t = 0; t < 2; ++t) {
        const int row = 32 * rb + perm32(t, rho);
#pragma unroll
        for (int es = 0; es < 8; ++es) a[t][es] = *(const v4u*)(Wr + ((size_t)row * H + h) * DH + es * 32 + g * 8);
    }
    v4u xfv;
    {
        const int f = gt >> 6; const int ks = f % 14; const int st = f / 14; const int tau = st & 1, seg = st >> 1;
        xfv = *(const v4u*)(bx + (size_t)(seg * 32 + perm32(tau, rho)) * D + ks * 32 + g * 8);
    }
    __builtin_amdgcn_sched_barrier(0);
    if (wg == 1 && tid < 128) __builtin_amdgcn_raw_buffer_store_b32(0u, rs, (unsigned)CTRL_OFF + tid * 128, 0, 16);
    for (unsigned i = gt; i < COMM_BYTES / 16; i += GN) {
        unsigned off = i * 16;
        unsigned within = off & 65535u, slot = off >> 18;
        unsigned chunk = (within >> 14) & 3, o = within & 8191u;
        if (chunk == 0 && (o >= 7184 || (slot == 0 && o < 7168))) continue;
        st_sc1(v4u{SENT, SENT, SENT, SENT}, rs, off, 0);
    }
    PP(0)
    if (mat == 0) {
#pragma unroll
        for (int es = 0; es < 8; ++es) *(v4u*)(lds + (w * 8 + es) * 1024 + lane * 16) = bt[es];
    } else {
#pragma unroll
        for (int it = 0; it < 8; ++it) { const int idx = it * 448 + tid; const int e = idx / 14, c8 = idx % 14; *(v4u*)(lds + e * WST + c8 * 16) = bt[it]; }
    }
    PP(3)
    __syncthreads();
    PP(4)
    f4 acc[2][7];
#pragma unroll
    for (int t = 0; t < 2; ++t)
#pragma unroll
        for (int c = 0; c < 7; ++c) acc[t][c] = f4{0, 0, 0, 0};
    if (mat == 0) {
#pragma unroll
        for (int es = 0; es < 8; ++es)
#pragma unroll
            for (int c = 0; c < 7; ++c) {
                v4u bfr = *(const v4u*)(lds + (c * 8 + es) * 1024 + lane * 16);
                acc[0][c] = mfma(a[0][es], bfr, acc[0][c]);
                acc[1][c] = mfma(a[1][es], bfr, acc[1][c]);
            }
        const float sc = LOG2E * 0.0625f;
#pragma unroll
        for (int c = 0; c < 7; ++c) {
            const int ct = 7 * q + c;
            st_sc1(pack8(acc[0][c] * sc, acc[1][c] * sc), rs, (unsigned)(AS_OFF + ((size_t)((h * 14 + (ct >> 1)) * 2 + (ct & 1)) * 14 + rb) * 1024) + lane * 16, 0);
        }
    } else {
        const unsigned trb = (8 * g + (rho >> 2)) * WST + (lane & 3) * 8;
#pragma unroll
        for (int es = 0; es < 8; ++es)
#pragma unroll
            for (int c = 0; c < 7; ++c) {
                v2u lo = trread(lds + trb + (32 * es) * WST + c * 32), hi = trread(lds + trb + (32 * es + 4) * WST + c * 32);
                v4u bfr = {lo.x, lo.y, hi.x, hi.y};
                acc[0][c] = mfma(a[0][es], bfr, acc[0][c]);
                acc[1][c] = mfma(a[1][es], bfr, acc[1][c]);
            }
#pragma unroll
        for (int c = 0; c < 7; ++c) {
            const int ct = 7 * q + c;
            const int j = 2 * ct + (rho >> 3);
            st_sc1(pack8(acc[0][c], acc[1][c]), rs, (unsigned)(BS_OFF + ((size_t)(j * 8 + (h >> 1)) * 14 + rb) * 1024) + (((h & 1) * 8 + (rho & 7)) + 16 * g) * 16, 0);
        }
    }
    st_sc1(xfv, rs, (unsigned)XF_OFF + gt * 16, 0);
    if (wg < 8) {
        const int j = tid >> 3, irow = tid & 7;
        st_sc1(nosent(*(const v4u*)(mem0 + irow * D + j * 8)), rs, (wg >> 1) * 65536u + (wg & 1) * 8192u + tid * 16, 0);
    }
    PP(5)
}

DI unsigned umax3(unsigned a, unsigned b, unsigned c) { return max(max(a, b), c); }
DI unsigned vmax4(unsigned t, v4u v) { return umax3(umax3(t, v.x, v.y), v.z, v.w); }
DI float dpp_xor8(float v) { return asf((unsigned)__builtin_amdgcn_update_dpp(0, (int)asu(v), 0x128, 0xf, 0xf, false)); }
DI unsigned dpp_xor8u(unsigned v) { return (unsigned)__builtin_amdgcn_update_dpp(0, (int)v, 0x128, 0xf, 0xf, false); }
constexpr unsigned SPIN_MAX = 200000;
DI float dpp_x1(float v) { return asf((unsigned)__builtin_amdgcn_update_dpp(0, (int)asu(v), 0xB1, 0xf, 0xf, false)); }
DI float dpp_x2(float v) { return asf((unsigned)__builtin_amdgcn_update_dpp(0, (int)asu(v), 0x4E, 0xf, 0xf, false)); }
DI f4 quadsum(f4 v) {
#pragma unroll
    for (int r = 0; r < 4; ++r) v[r] += dpp_x1(v[r]);
#pragma unroll
    for (int r = 0; r < 4; ++r) v[r] += dpp_x2(v[r]);
    return v;
}
DI float xmax16(float x) { auto r = __builtin_amdgcn_permlane16_swap(asu(x), asu(x), false, false); unsigned a = r[0], b = r[1]; return fmaxf(asf(a), asf(b)); }
DI float xmax32(float x) { auto r = __builtin_amdgcn_permlane32_swap(asu(x), asu(x), false, false); unsigned a = r[0], b = r[1]; return fmaxf(asf(a), asf(b)); }
DI float xsum16(float x) { auto r = __builtin_amdgcn_permlane16_swap(asu(x), asu(x), false, false); unsigned a = r[0], b = r[1]; return asf(a) + asf(b); }
DI float xsum32(float x) { auto r = __builtin_amdgcn_permlane32_swap(asu(x), asu(x), false, false); unsigned a = r[0], b = r[1]; return asf(a) + asf(b); }
#define LOADM(mf) v4u mf[14]; _Pragma("unroll") for (int ks = 0; ks < 14; ++ks) mf[ks] = *(const v4u*)(ldsm + ks * 512 + mfrag_off); __builtin_amdgcn_sched_barrier(0);
DI void bar() { asm volatile("s_waitcnt lgkmcnt(0)" ::: "memory"); __builtin_amdgcn_s_barrier(); asm volatile("" ::: "memory"); }
DI unsigned now32() { return (unsigned)mtime(); }
DI void wait_until(unsigned target) {
    int rem = (int)(target - now32());
    while (rem > 80) { __builtin_amdgcn_s_sleep(1); rem -= 64; }
    while (rem > 0) { asm volatile("s_nop 7" ::: "memory"); rem -= 16; }
}

DI unsigned rt32() { unsigned long long t; asm volatile("s_memrealtime %0\n\ts_waitcnt lgkmcnt(0)" : "=s"(t) :: "memory"); return (unsigned)t; }
DI unsigned approach_rt(unsigned target, int lead) {
    int rem;
    for (;;) {
        rem = (int)(target - rt32());
        if (rem <= lead + 4) break;
        int n = (rem - lead) >> 2;
        while (n-- > 0) __builtin_amdgcn_s_sleep(1);
    }
    return now32() + (unsigned)(rem * 23 - 110);
}
DI void clock_ref(unsigned& re, unsigned& me) {
    unsigned long long m0, m1, r[12];
    asm volatile("s_memtime %0\n\t"
        "s_memrealtime %1\n\ts_nop 1\n\ts_memrealtime %2\n\ts_nop 1\n\ts_memrealtime %3\n\ts_nop 1\n\ts_memrealtime %4\n\ts_nop 1\n\t"
        "s_memrealtime %5\n\ts_nop 1\n\ts_memrealtime %6\n\ts_nop 1\n\ts_memrealtime %7\n\ts_nop 1\n\ts_memrealtime %8\n\ts_nop 1\n\t"
        "s_memrealtime %9\n\ts_nop 1\n\ts_memrealtime %10\n\ts_nop 1\n\ts_memrealtime %11\n\ts_nop 1\n\ts_memrealtime %12\n\ts_nop 1\n\t"
        "s_memtime %13\n\ts_waitcnt lgkmcnt(0)"
        : "=&s"(m0), "=&s"(r[0]), "=&s"(r[1]), "=&s"(r[2]), "=&s"(r[3]), "=&s"(r[4]), "=&s"(r[5]), "=&s"(r[6]), "=&s"(r[7]), "=&s"(r[8]), "=&s"(r[9]), "=&s"(r[10]), "=&s"(r[11]), "=&s"(m1) :: "memory");
    unsigned cnt = 0;
#pragma unroll
    for (int i = 1; i < 12; ++i) cnt += ((unsigned)r[i] == (unsigned)r[0]) ? 1u : 0u;
    re = (unsigned)r[0] + 4u;
    me = (unsigned)m0 + ((((unsigned)m1 - (unsigned)m0) * (2u * cnt + 1u) * 43u) >> 10);
}
DI void sleep_to(unsigned tc) {
    for (;;) {
        const int rem = (int)(tc - now32());
        if (rem <= 0) return;
        if (rem > 260) { int n = (rem - 160) / 96; while (n-- > 0) __builtin_amdgcn_s_sleep(1); }
    }
}
DI void spin_to(unsigned tc) { while ((int)(tc - now32()) > 0) {} }
DI void wait_rt(unsigned target) { spin_to(approach_rt(target, 10)); }
template <int NP, bool CTRL>
DI unsigned gather_m(__amdgpu_buffer_rsrc_t rs, unsigned sb, unsigned char* ldsm, int p0, int lane, int skipline = -1) {
    const unsigned o0 = p0 * 1024 + lane * 16;
    for (unsigned spin = 0;; ++spin) {
        asm volatile("" ::: "memory");
        v4u a[NP]; v2u c = {0, 0};
        bool sk[NP];
#pragma unroll
        for (int k = 0; k < NP; ++k) { sk[k] = (int)((o0 + k * 1024) >> 7) == skipline; a[k] = ld_sc1(rs, (o0 + k * 1024) ^ (sk[k] ? 128u : 0u), sb); }
        if (CTRL) c = ld2_sc1(rs, 7168u, sb);
#pragma unroll
        for (int k = 0; k < NP; ++k) *(v4u*)(ldsm + (sk[k] ? 7424u + (lane & 7) * 16 : o0 + k * 1024)) = a[k];
        if (CTRL && lane == 0) *(v2u*)(ldsm + 7168) = c;
        unsigned t;
        if (NP == 2) t = max(vmax4(0, a[0]), vmax4(0, a[1])); else t = vmax4(0, a[0]);
        if (CTRL) t = umax3(t, c.x, c.y);
        if (__all(t != SENT) || spin > SPIN_MAX) return spin;
        __builtin_amdgcn_s_sleep(1);
    }
}
#define LOADP(mf) v4u mf[7]; _Pragma("unroll") for (int q_ = 0; q_ < 7; ++q_) mf[q_] = *(const v4u*)(ldsm + q_ * 1024 + pfrag_off); __builtin_amdgcn_sched_barrier(0);
DI unsigned reduce8(const float* rd, int lane) {
    const float* q = rd + (lane >> 3) * 100 + (lane & 7) * 12;
    f4 a = *(const f4*)q, b = *(const f4*)(q + 4);
    a += b;
    float v = (a[0] + a[1]) + (a[2] + a[3]);
    float o = dpp_x1(v);
    unsigned r = (lane & 1) ? pk2(o, v) : pk2(v, o);
    return min(r, 0xFFFFFFFEu);
}
#define READCTRL() { SAVER1 v2u c_ = *(const v2u*)(ldsm + 7168); R2 = __builtin_amdgcn_readfirstlane(c_.x); R1n = __builtin_amdgcn_readfirstlane(c_.y); }

#define SAVER1
typedef short s4 __attribute__((ext_vector_type(4)));
DI f4 mfma16(v2u a, v2u b, f4 c) { return __builtin_amdgcn_mfma_f32_16x16x16bf16_1k(__builtin_bit_cast(s4, a), __builtin_bit_cast(s4, b), c, 0, 0, 0); }
DI float vmax(float a, float b) { float r; asm("s_nop 1\n\tv_max_f32 %0, %1, %2" : "=v"(r) : "v"(a), "v"(b)); return r; }
DI float vmax3(float a, float b, float c) { float r; asm("v_max3_f32 %0, %1, %2, %3" : "=v"(r) : "v"(a), "v"(b), "v"(c)); return r; }
DI float vmax3_mfma(float a, float b, float c) { float r; asm("s_nop 3\n\tv_max3_f32 %0, %1, %2, %3" : "=v"(r) : "v"(a), "v"(b), "v"(c)); return r; }
DI float rmax16(float x) { auto r = __builtin_amdgcn_permlane16_swap(asu(x), asu(x), false, false); unsigned a = r[0], b = r[1]; return vmax(asf(a), asf(b)); }
DI float rmax32(float x) { auto r = __builtin_amdgcn_permlane32_swap(asu(x), asu(x), false, false); unsigned a = r[0], b = r[1]; return vmax(asf(a), asf(b)); }
constexpr int HS = 1408;
constexpr int ST_OFF = 16 * HS;
constexpr int ZL_OFF = ST_OFF + 1024;
constexpr int L_M = 0, L_RED = 8192, L_RED2 = 12800, L_GM = 16128, L_VP = 17152, L_FL = 19200, L_UF = 20480;

DI unsigned gather_ctrl(__amdgpu_buffer_rsrc_t rs, unsigned sb, unsigned char* ldsm, int lane) {
    for (unsigned spin = 0;; ++spin) {
        asm volatile("" ::: "memory");
        v2u c = ld2_sc1(rs, 7168u, sb);
        if (lane == 0) *(v2u*)(ldsm + 7168) = c;
        if (__all(max(c.x, c.y) != SENT) || spin > SPIN_MAX) return spin;
        __builtin_amdgcn_s_sleep(1);
    }
}
#define REREF 0
#define ASLEEP 2
#define HSLEEP 2
#define SB0 __builtin_amdgcn_sched_barrier(0);

DI void glds16(const unsigned char* gsrc, unsigned lds_dst) {
    unsigned keep;
    asm volatile("s_mov_b32 %0, m0\n\ts_mov_b32 m0, %2\n\ts_nop 0\n\tglobal_load_lds_dwordx4 %1, off sc1\n\ts_mov_b32 m0, %0" : "=&s"(keep) : "v"(gsrc), "s"(lds_dst) : "memory");
}
DI void glds4(const unsigned char* gsrc, unsigned lds_dst) {
    unsigned keep;
    asm volatile("s_mov_b32 %0, m0\n\ts_mov_b32 m0, %2\n\ts_nop 0\n\tglobal_load_lds_dword %1, off sc1\n\ts_mov_b32 m0, %0" : "=&s"(keep) : "v"(gsrc), "s"(lds_dst) : "memory");
}
DI unsigned ldflag(__amdgpu_buffer_rsrc_t rs, unsigned off) { return __builtin_amdgcn_raw_buffer_load_b32(rs, (unsigned)CTRL_OFF + off, 0, 16); }
DI unsigned wait_flags(__amdgpu_buffer_rsrc_t rs, unsigned off, unsigned need, int slp) {
    unsigned v;
    for (unsigned spin = 0;; ++spin) {
        asm volatile("" ::: "memory");
        v = ldflag(rs, off);
        if (__all(v >= need) || spin > 4 * SPIN_MAX) break;
        for (int i = 0; i < slp; ++i) __builtin_amdgcn_s_sleep(4);
    }
#pragma unroll
    for (int o = 32; o >= 1; o >>= 1) v = min(v, (unsigned)__shfl_xor((int)v, o));
    return __builtin_amdgcn_readfirstlane(v);
}

#define LDSV(off) (*(volatile __attribute__((address_space(3))) unsigned*)((__attribute__((address_space(3))) unsigned char*)lds + (off)))
DI void rec_role(unsigned char* ws, unsigned char* lds, const int role, int dS, int dV, int dVm) {
    const int tid = threadIdx.x, lane = tid & 63, w = __builtin_amdgcn_readfirstlane(tid >> 6);
    const int rho = lane & 15, g = lane >> 4;
    __amdgpu_buffer_rsrc_t rs = mkrs(ws, 0x7fffffff);
    unsigned char* ldsm = lds + L_M;
    float* red = (float*)(lds + L_RED);
    float* red2 = (float*)(lds + L_RED2);
    if (role == 112) {
        unsigned best = 0xffffffffu;
        for (int r = 0; r < 14; ++r) {
            const v4u z = {SENT, SENT, SENT, SENT};
            const unsigned off = (unsigned)((w >> 1) + 4 * (r & 1)) * 262144u + ((r >> 1) & 3) * 65536u + 7680u + ((r >> 3) + 2 * (r & 1)) * 128u + (w & 1) * 8192u;
            const unsigned t0 = now32();
            if (lane < 8) st_sc1(z, rs, off + lane * 16, 0);
            asm volatile("s_waitcnt vmcnt(0)" ::: "memory");
            const unsigned dt = now32() - t0;
            if (r > 0) best = min(best, dt);
        }
        if (lane == 0) ((unsigned*)lds)[w] = best;
        __syncthreads();
        if (tid == 0) {
            const unsigned* bl = (const unsigned*)lds;
            const unsigned b0 = (bl[0] + bl[2]) + (bl[4] + bl[6]), b1 = (bl[1] + bl[3]) + (bl[5] + bl[7]);
            __builtin_amdgcn_raw_buffer_store_b32(b1 < b0 ? 2u : 1u, rs, (unsigned)CTRL_OFF + C_POL, 0, 16);
        }
        __syncthreads();
    }
    const unsigned polo = (wait_flags(rs, C_POL, 1, 1) - 1u) * 8192u;
    const unsigned myc = (unsigned)(role & 3) * 65536u + polo;
    unsigned nfail = 0, nfail2 = 0;
    unsigned long long* dbg = (unsigned long long*)(ws + CTRL_OFF + C_DBG);
#define STAMP(cond, idx) if ((cond) && role == 0 && tid == 0) dbg[idx] = __builtin_amdgcn_s_memrealtime();
#define TL(idx)
#define GT(idx, ref)
#define BT(i) {}
    constexpr unsigned TOFF = 70, TOFF2 = 30;
#define CLKUPD() { unsigned r1_, m1_; clock_ref(r1_, m1_); const unsigned dr = r1_ - r0, dm = m1_ - m0; \
        if (dr > 60u && dr < TA + TB + 12u) { const int err = (int)(dm * 256u) - (int)(cq * dr); cq = (unsigned)min(max((int)cq + (err >> 8), 4096), 6400); } r0 = r1_; m0 = m1_; }
#define WAITR1() { unsigned tg_ = m0 - TOFF + (unsigned)(((int)(R1n - r0) * (int)cq) >> 8); \
        if (REREF && (int)(tg_ - now32()) > 700) { sleep_to(tg_ - 520u); unsigned ra_, ma_; clock_ref(ra_, ma_); tg_ = ma_ - TOFF + (unsigned)(((int)(R1n - ra_) * (int)cq) >> 8); } \
        sleep_to(tg_); }
    const unsigned pfrag_off = (rho >> 3) * 512 + g * 128 + (rho & 7) * 16;
    const unsigned TA = dS & 255, TB = dV & 255, TE = dVm & 255, TLATE = (dS >> 8) & 255, XUP = (dV >> 8) & 255, XDN = (dVm >> 8) & 255, X0 = (dVm >> 16) & 255;
    unsigned R2 = 0, R1n = 0;
    const bool is_mx = role >= 112;
    const bool is_v = !is_mx && (((role >> 2) & 1) == 0);
    const int j = (role >> 3) * 4 + (role & 3);
    const int h = role / 7, p = role % 7;
    const int hp = (lane >> 3) & 1, i8 = lane & 7;
    const v4u SENT4 = {SENT, SENT, SENT, SENT};

    if (is_mx) {
        const int hx = role - 112;
        if (w >= 4) {
            const unsigned orst = comm_off(1, hx * HS + 896 + (lane & 31) * 16, 0) + (lane >> 5) * 65536u + polo;
            const unsigned orst2 = comm_off(1, ST_OFF + (hx >> 1) * 128 + (hx & 1) * 64 + (lane & 1) * 16, 0) + ((lane >> 1) & 3) * 65536u + polo;
            const unsigned gfl2 = C_GU + 128u * (unsigned)min((14 * hx) / 4 + (lane & 7), (14 * hx + 13) / 4);
            unsigned kn = 0;
            unsigned r0 = 0, m0 = 0, cq = 5900u;
            if (w == 7) clock_ref(r0, m0);
            if (w == 5 && lane == 0) LDSV(L_UF) = 0u;
            for (int step = 0; step < NSEG; ++step) {
                const unsigned sbr = comm_step_base(step, 0) + myc;
                if (step > 0 && w == 7) WAITR1()
                bar();
                if (w < 7) nfail += gather_m<2, false>(rs, sbr, ldsm, 2 * (w - 4), lane) ? 1 : 0;
                else nfail += gather_m<1, true>(rs, sbr, ldsm, 6, lane) ? 1 : 0;
                bar();
                READCTRL()
                bar();
                if (w == 4 && step >= 4) {
                    __builtin_amdgcn_s_sleep(4);
                    const unsigned sbz = comm_step_base(step + 4, 0);
                    st_sc1(SENT4, rs, orst, sbz); st_sc1(SENT4, rs, orst + 131072u, sbz);
                    if (lane < 8) st_sc1(SENT4, rs, orst2, sbz);
                }
                if (w == 7) CLKUPD()
                if (w == 5 && kn < 64u) {
                    __builtin_amdgcn_s_sleep(4);
                    kn = wait_flags(rs, gfl2, 0, 1);
                    if (lane == 0) LDSV(L_UF) = kn;
                }
            }
        } else {
            __builtin_amdgcn_s_setprio(2);
            const int tau = w & 1, kh = w >> 1;
            v4u wa[8], wb[8];
            const unsigned gbase = (unsigned)(G_OFF + ((size_t)hx * 2 + tau) * 14 * 1024 + kh * 8192) + lane * 16;
            const unsigned g67 = gbase + (kh ? 4096u : 6144u);
#define GLOAD(wt, sb_) { _Pragma("unroll") for (int ks = 0; ks < 6; ++ks) wt[ks] = ld_sc1(rs, gbase + ks * 1024, sb_); \
                     wt[6] = ld_sc1(rs, g67, sb_); wt[7] = ld_sc1(rs, g67 + 1024, sb_); }
            const unsigned gfl = C_GU + 128u * (unsigned)min((14 * hx) / 4 + (lane & 7), (14 * hx + 13) / 4);
            unsigned known = wait_flags(rs, gfl, 2, 1);
            GLOAD(wa, 0)
            GLOAD(wb, 458752)
            float* sc = red;
            const int tq = lane & 7, ir = lane >> 3;
            const unsigned oes = comm_off(1, hx * HS + 896 + lane * 8, 0) + w * 65536u + polo;
            const unsigned oss = comm_off(1, ST_OFF + (hx >> 1) * 128 + (hx & 1) * 64 + ir * 4, 0) + w * 65536u + polo;
#define MXGT
#define MXSTEP(wt, step) { \
                const bool last = ((step) == NSEG - 1); \
                const unsigned sbs = comm_step_base(step, 0); \
                __builtin_amdgcn_s_barrier(); \
                bar(); \
                f4 aE = {0, 0, 0, 0}, aO = {0, 0, 0, 0}; \
                v4u mf[4]; \
                _Pragma("unroll") for (int q = 0; q < 3; ++q) mf[q] = *(const v4u*)(ldsm + (4 * kh + q) * 1024 + pfrag_off); \
                mf[3] = *(const v4u*)(ldsm + 3 * 1024 + pfrag_off); \
                SB0 \
                _Pragma("unroll") for (int q = 0; q < 3; ++q) { aE = mfma(wt[2 * q], mf[q], aE); aO = mfma(wt[2 * q + 1], mf[q], aO); } \
                if (kh == 0) { aE = mfma(wt[6], mf[3], aE); aO = mfma(wt[7], mf[3], aO); } \
                f4 xs = hp ? aO : aE; \
                *(f4*)(sc + ((2 * kh + hp) * 8 + i8) * 36 + 16 * tau + 4 * g) = xs; \
                bar(); \
                const float* sp = sc + ir * 36 + 4 * tq; \
                f4 a0 = (*(const f4*)(sp) + *(const f4*)(sp + 288)) + (*(const f4*)(sp + 576) + *(const f4*)(sp + 864)); \
                float mx; \
                asm("v_max3_f32 %0, %1, %2, %3\n\tv_max_f32 %0, %0, %4\n\ts_nop 1\n\t" \
                    "v_max_f32_dpp %0, %0, %0 quad_perm:[1,0,3,2] row_mask:0xf bank_mask:0xf\n\ts_nop 1\n\t" \
                    "v_max_f32_dpp %0, %0, %0 quad_perm:[2,3,0,1] row_mask:0xf bank_mask:0xf\n\ts_nop 1\n\t" \
                    "v_max_f32_dpp %0, %0, %0 row_half_mirror row_mask:0xf bank_mask:0xf" \
                    : "=&v"(mx) : "v"(a0[0]), "v"(a0[1]), "v"(a0[2]), "v"(a0[3])); \
                if (!last) { \
                    unsigned e0 = pk2(__builtin_amdgcn_exp2f(a0[0] - mx), __builtin_amdgcn_exp2f(a0[1] - mx)); \
                    unsigned e1 = pk2(__builtin_amdgcn_exp2f(a0[2] - mx), __builtin_amdgcn_exp2f(a0[3] - mx)); \
                    v2u eo; eo.x = min(e0, 0xFFFFFFFEu); eo.y = min(e1, 0xFFFFFFFEu); \
                    st2_sc1(eo, rs, oes, sbs); \
                    if (tq == 1) __builtin_amdgcn_raw_buffer_store_b32(min(asu(mx), 0xFFFFFFFEu), rs, oss, sbs, 16); \
                    MXGT \
                } \
                { const int sn = min((step) + 2, NSEG - 1); \
                  known = max(known, (unsigned)__builtin_amdgcn_readfirstlane(LDSV(L_UF))); \
                  if (known < (unsigned)sn + 1) known = wait_flags(rs, gfl, sn + 1, 1); \
                  const unsigned sb = __builtin_amdgcn_readfirstlane(sn * 458752); \
                  GLOAD(wt, sb) } }
            for (int step = 0; step < NSEG; step += 2) {
                MXSTEP(wa, step)
                MXSTEP(wb, step + 1)
            }
        }
    } else if (w >= 4) {
        const int wv = w - 4;
        v4u wtv[2][14];
        if (is_v) {
            const unsigned char* src = ws + BS_OFF + ((size_t)j * 8 + 2 * wv) * 14 * 1024;
#pragma unroll
            for (int t = 0; t < 2; ++t)
#pragma unroll
                for (int f = 0; f < 14; ++f) wtv[t][f] = *(const v4u*)(src + (t * 14 + f) * 1024 + lane * 16);
        } else {
#pragma unroll
            for (int t = 0; t < 2; ++t)
#pragma unroll
                for (int f = 0; f < 14; ++f) wtv[t][f] = v4u{0, 0, 0, 0};
        }
        __builtin_amdgcn_s_waitcnt(0x0F70);
        const bool cond = is_v && j == 0;
        const bool slow = w >= 6;
        const unsigned ufl = C_GU + 128u * (56 + j);
        const unsigned char* usrc = ws + U_OFF + ((size_t)j * 8 + 4 * (wv & 1)) * 1024 + lane * 16;
        const unsigned udst = (unsigned)(size_t)(__attribute__((address_space(3))) unsigned char*)lds + L_UF + (wv & 1) * 4096;
#define UDMA(sg) { const unsigned char* p_ = usrc + (size_t)(sg) * 458752; const unsigned d_ = __builtin_amdgcn_readfirstlane(udst + ((sg) & 3) * 8192); \
            glds16(p_, d_); glds16(p_ + 1024, d_ + 1024); glds16(p_ + 2048, d_ + 2048); glds16(p_ + 3072, d_ + 3072); }
#define VPRIME() { \
                f4 e0 = {0, 0, 0, 0}, o0 = {0, 0, 0, 0}, e1 = {0, 0, 0, 0}, o1 = {0, 0, 0, 0}; \
                _Pragma("unroll") for (int q = 0; q < 7; ++q) { \
                    e0 = mfma(mf[q], wtv[0][2 * q], e0); o0 = mfma(mf[q], wtv[0][2 * q + 1], o0); \
                    e1 = mfma(mf[q], wtv[1][2 * q], e1); o1 = mfma(mf[q], wtv[1][2 * q + 1], o1); } \
                f4 cs; \
                _Pragma("unroll") for (int r = 0; r < 4; ++r) { \
                    auto sw = __builtin_amdgcn_permlane32_swap(asu(o0[r]), asu(e1[r]), false, false); \
                    unsigned d0 = sw[0], d1 = sw[1]; \
                    cs[r] = g < 2 ? e0[r] + asf(d1) : o1[r] + asf(d0); } \
                v2u vp; vp.x = pk2(cs[0], cs[1]); vp.y = pk2(cs[2], cs[3]); \
                *(v2u*)(lds + L_VP + wv * 512 + (2 * (g >> 1) + hp) * 128 + i8 * 16 + (g & 1) * 8) = vp; }
#define BYPROD(st_) { \
            if (w == 7) { v4u gmv = *(const v4u*)(lds + L_GM + lane * 16); \
                st_sc1(gmv, rs, (unsigned)(GM_OFF + ((size_t)h * 14 + 2 * p) * 512) + lane * 16, __builtin_amdgcn_readfirstlane((st_) * 16 * 14 * 512)); } \
            if (w == 6 && is_v) { v4u v0 = *(const v4u*)(lds + L_VP + lane * 16), v1 = *(const v4u*)(lds + L_VP + 1024 + lane * 16); \
                const unsigned sbv = __builtin_amdgcn_readfirstlane((st_) * 56 * 4 * 512); \
                st_sc1(v0, rs, (unsigned)(VP_OFF + (size_t)j * 2048) + lane * 16, sbv); \
                st_sc1(v1, rs, (unsigned)(VP_OFF + (size_t)j * 2048 + 1024) + lane * 16, sbv); } }
#define RESETS() if (w == 7 && step >= 4) { \
                    if (dorst) st_sc1(SENT4, rs, orst, comm_step_base(step + 4, 0)); \
                    if (cond && lane < 4) st_sc1(SENT4, rs, polo + 7168u + lane * 65536u, comm_step_base(step + 4, 0)); }
        if (w == 7) {
            unsigned known = 0;
            unsigned r0, m0, cq = 5900u; clock_ref(r0, m0);
            if (is_v) { known = wait_flags(rs, ufl, 2, 1); UDMA(0) UDMA(1) asm volatile("s_waitcnt vmcnt(0)" ::: "memory"); }
            const unsigned orst = (lane < 32 ? comm_off(1, h * HS + p * 128 + (lane & 7) * 16, 0) : (unsigned)(j * 128 + (lane & 7) * 16)) + ((lane >> 3) & 3) * 65536u + polo;
            const bool dorst = lane < 32 || is_v;
            for (int step = 0; step < NSEG; ++step) {
                if (step > 0) WAITR1()
                bar();
                BT(0)
                CLKUPD()
                if (lane == 0) *(v4u*)(lds + L_FL + 512) = v4u{r0, m0 - TOFF2, cq, 0u};
                bar();
                __builtin_amdgcn_s_sleep(HSLEEP);
                READCTRL()
                LOADP(mf)
                bar();
                if (is_v) VPRIME()
                bar();
                asm volatile("s_waitcnt vmcnt(0)" ::: "memory");
                if (is_v && step + 3 < NSEG) UDMA(step + 2)
                {
                    v4u gmv = *(const v4u*)(lds + L_GM + lane * 16);
                    st_sc1(gmv, rs, (unsigned)(GM_OFF + ((size_t)h * 14 + 2 * p) * 512) + lane * 16, __builtin_amdgcn_readfirstlane(step * 16 * 14 * 512));
                }
                if (step >= 4) {
                    if (dorst) st_sc1(SENT4, rs, orst, comm_step_base(step + 4, 0));
                    if (cond && lane < 4) st_sc1(SENT4, rs, polo + 7168u + lane * 65536u, comm_step_base(step + 4, 0));
                }
                if (is_v) {
                    bar();
                }
            }
            bar();
            asm volatile("s_waitcnt vmcnt(0)" ::: "memory");
            __builtin_amdgcn_s_barrier();
        } else if (w == 6) {
            unsigned known = 0;
            if (is_v) { known = wait_flags(rs, ufl, 2, 1); UDMA(0) UDMA(1) }
            const bool reporter = (role == 4);
            const unsigned char* fsrc = ws + CTRL_OFF + ufl;
            const unsigned fdst = __builtin_amdgcn_readfirstlane((unsigned)(size_t)(__attribute__((address_space(3))) unsigned char*)lds + L_FL);
            if (lane == 0) LDSV(L_FL) = 0u;
#define VPSTORE(st_) { v4u v0 = *(const v4u*)(lds + L_VP + lane * 16), v1 = *(const v4u*)(lds + L_VP + 1024 + lane * 16); \
                const unsigned sbv = __builtin_amdgcn_readfirstlane((st_) * 56 * 4 * 512); \
                st_sc1(v0, rs, (unsigned)(VP_OFF + (size_t)j * 2048) + lane * 16, sbv); \
                st_sc1(v1, rs, (unsigned)(VP_OFF + (size_t)j * 2048 + 1024) + lane * 16, sbv); }
            for (int step = 0; step < NSEG; ++step) {
                bar();
                if (is_v) {
                    asm volatile("s_waitcnt vmcnt(0)" ::: "memory");
                    __builtin_amdgcn_s_sleep(ASLEEP);
                    if (step + 3 < NSEG) {
                        if (known < (unsigned)step + 3) known = max(known, (unsigned)__builtin_amdgcn_readfirstlane(LDSV(L_FL)));
                        if (known < (unsigned)step + 3) known = wait_flags(rs, ufl, step + 3, 1);
                        UDMA(step + 2)
                    }
                    if (step > 0) VPSTORE(step - 1)
                    if (known < (unsigned)step + 6 && known < 64u && lane == 0) glds4(fsrc, fdst);
                }
                bar();
                __builtin_amdgcn_s_sleep(HSLEEP);
                LOADP(mf)
                bar();
                if (is_v) VPRIME()
                bar();
                if (reporter && lane == 0 && step >= 3) __builtin_amdgcn_raw_buffer_store_b32((unsigned)(step - 2), rs, (unsigned)CTRL_OFF + C_REC, 0, 16);
                if (is_v) bar();
            }
            bar();
            if (is_v) VPSTORE(NSEG - 1)
            asm volatile("s_waitcnt vmcnt(0)" ::: "memory");
            __builtin_amdgcn_s_barrier();
        } else {
            unsigned extra = X0, calm = 0;
            if (cond && w == 5) {
                wait_flags(rs, ufl, 2, 1);
                unsigned t = rt32() + 150u;
                v2u c; c.x = min(t, 0xFFFFFFFEu); c.y = min(t + TB, 0xFFFFFFFEu);
                if (lane < KCOPY) st2_sc1(c, rs, polo + 7168u + lane * 65536u, 0);
                if (lane == 0) *(v2u*)(ldsm + 7168) = c;
            }
            const unsigned opub = polo + j * 128 + (lane >> 1) * 4 + (lane & 1) * 65536u;
            const unsigned opubS = comm_off(1, h * HS + p * 128 + (lane >> 1) * 4, 0) + (lane & 1) * 65536u + polo;
            for (int step = 0; step < NSEG; ++step) {
                const bool last = (step == NSEG - 1);
                const unsigned sbs = comm_step_base(step, 0);
                const unsigned sbr = sbs + myc;
                const unsigned sbn = comm_step_base(step + 1, 0);
                bar();
                const int skl = (is_v && step > 0) ? j : -1;
                if (w == 4) nfail += gather_m<1, false>(rs, sbr, ldsm, 5, lane, skl) ? 1 : 0;
                else if (cond) nfail += gather_m<1, false>(rs, sbr, ldsm, 6, lane, skl) ? 1 : 0;
                else nfail += gather_m<1, true>(rs, sbr, ldsm, 6, lane, skl) ? 1 : 0;
                bar();
                if (w == 5) BT(1)
                READCTRL()
                __builtin_amdgcn_s_sleep(HSLEEP);
                LOADP(mf)
                if (w == 4) __builtin_amdgcn_s_setprio(3);
                bar();
                if (w == 5) BT(2)
                if (w == 4) {
                    if (!last) {
                        unsigned o = reduce8(red, lane);
                        __builtin_amdgcn_raw_buffer_store_b32(o, rs, opubS, sbs, 16);
                        __builtin_amdgcn_raw_buffer_store_b32(o, rs, opubS + 131072u, sbs, 16);
                        BT(5)
                        if ((lane & 1) == 0) *(unsigned*)(lds + L_FL + 640 + (lane >> 1) * 4) = o;
                    }
                    __builtin_amdgcn_s_setprio(0);
                }
                if (is_v) {
                    VPRIME()
                    bar();
                    if (w == 5) BT(3)
                    if (w == 4) __builtin_amdgcn_s_setprio(3);
                    bar();
                    if (w == 5 && !cond) BT(4)
                    if (!last) {
                        if (w == 4) {
                            unsigned o = reduce8(red2, lane);
                            __builtin_amdgcn_raw_buffer_store_b32(o, rs, opub, sbn, 16);
                            __builtin_amdgcn_raw_buffer_store_b32(o, rs, opub + 131072u, sbn, 16);
                            BT(6)
                            if ((lane & 1) == 0) *(unsigned*)(ldsm + j * 128 + (lane >> 1) * 4) = o;
                            __builtin_amdgcn_s_setprio(0);
                        }
                        if (cond && w == 5) {
                            const unsigned tp = rt32();
                            unsigned base = R1n;
                            if ((int)(tp + 4u - R1n) > 0) { base = tp + TLATE; extra = min(extra + XUP, 30u); calm = 0; }
                            else if (++calm >= XDN) { calm = 0; extra = extra ? extra - 1u : 0u; }
                            v2u c; c.x = min(base + TA + extra, 0xFFFFFFFEu); c.y = min(base + TA + TB + 2u * extra, 0xFFFFFFFEu);
                            if (lane < KCOPY) st2_sc1(c, rs, polo + 7168u + lane * 65536u, sbn);
                            if (lane == 0) *(v2u*)(ldsm + 7168) = c;
                        }
                    }
                } else bar();
            }
            bar();
            __builtin_amdgcn_s_barrier();
            if (w == 4 && lane == 0) __hip_atomic_fetch_add((unsigned*)(ws + CTRL_OFF + C_DONE), 1u, __ATOMIC_RELAXED, __HIP_MEMORY_SCOPE_AGENT);
        }
    } else {
        __builtin_amdgcn_s_setprio(2);
        const int b = 2 * p + (w >> 1), t = w & 1;
        v4u wt[14];
        {
            const unsigned char* src = ws + AS_OFF + ((size_t)(h * 14 + b) * 2 + t) * 14 * 1024;
#pragma unroll
            for (int ks = 0; ks < 14; ++ks) wt[ks] = *(const v4u*)(src + ks * 1024 + lane * 16);
        }
        __builtin_amdgcn_s_waitcnt(0x0F70);
        unsigned op[2][2], oe[2], ost;
        v4u pf[2][2], ef[2]; unsigned stt;
#pragma unroll
        for (int a = 0; a < 2; ++a) {
            const int hd = 4 * w + 2 * a + hp;
#pragma unroll
            for (int u = 0; u < 2; ++u) {
                const int q = 4 * u + g;
                const bool z = (q == 7) || (hd == h && q == p);
                op[a][u] = z ? 0x80000000u : comm_off(1, hd * HS + q * 128 + i8 * 16, 0);
            }
            oe[a] = comm_off(1, hd * HS + 896 + i8 * 64 + g * 16, 0);
        }
        const int hl = 4 * w + 2 * (g >> 1) + hp;
        ost = comm_off(1, ST_OFF + (hl >> 1) * 128 + (hl & 1) * 64 + i8 * 4, 0);
        const bool ownhead = (hl == h);
        v4u selLo = {0, 0, 0, 0}, selHi = {0, 0, 0, 0}, oneLo, oneHi;
        {
            const unsigned one = 0x3F80u << (16 * (rho & 1));
            const int wd = (rho & 7) >> 1;
            v4u sv; sv.x = wd == 0 ? one : 0; sv.y = wd == 1 ? one : 0; sv.z = wd == 2 ? one : 0; sv.w = wd == 3 ? one : 0;
            if (rho < 8) selLo = sv; else selHi = sv;
            const unsigned o2 = 0x3F803F80u;
            oneLo = rho < 8 ? v4u{o2, o2, o2, o2} : v4u{0, 0, 0, 0};
            oneHi = rho < 8 ? v4u{0, 0, 0, 0} : v4u{o2, o2, o2, o2};
        }
        for (int step = 0; step < NSEG; ++step) {
            const bool last = (step == NSEG - 1);
            const unsigned sbs = comm_step_base(step, 0);
            const unsigned sbr = sbs + myc;
            STAMP(step == 1, 0); STAMP(step == 63, 1);
            bar();
            TL(0)
            const int skl = (is_v && step > 0) ? j : -1;
            if (w == 0) nfail += gather_m<2, false>(rs, sbr, ldsm, 0, lane, skl) ? 1 : 0;
            else nfail += gather_m<1, false>(rs, sbr, ldsm, w + 1, lane, skl) ? 1 : 0;
            bar();
            TL(1)
            f4 aE = {0, 0, 0, 0}, aO = {0, 0, 0, 0};
            v4u mf[7];
#pragma unroll
            for (int q_ = 0; q_ < 7; ++q_) mf[q_] = *(const v4u*)(ldsm + q_ * 1024 + pfrag_off);
            v2u mb = *(const v2u*)(ldsm + b * 512 + g * 128 + i8 * 16 + t * 8);
            const unsigned cwx = *(const unsigned*)(ldsm + 7168);
            const v4u ckr = *(const v4u*)(lds + L_FL + 512);
            SB0
#pragma unroll
            for (int q = 0; q < 7; ++q) { aE = mfma(wt[2 * q], mf[q], aE); aO = mfma(wt[2 * q + 1], mf[q], aO); }
            f4 xs = hp ? aO : aE;
            v2u gh; gh.x = pk2(xs[0], xs[1]); gh.y = pk2(xs[2], xs[3]);
            f4 sp = mfma16(gh, mb, f4{0, 0, 0, 0});
            if (rho < 8) {
                float* rq = red + rho * 100 + (4 * (g & 1)) * 12 + 2 * w + (g >> 1);
                rq[0] = sp[0]; rq[12] = sp[1]; rq[24] = sp[2]; rq[36] = sp[3];
            }
            SB0
            TL(7)
            bar();
#define GMSTAGE() { f4 fs; _Pragma("unroll") for (int r = 0; r < 4; ++r) fs[r] = xs[r] + dpp_xor8(xs[r]); \
                v2u gm; gm.x = pk2(fs[0], fs[1]); gm.y = pk2(fs[2], fs[3]); \
                if (rho < 8) *(v2u*)(lds + L_GM + (w >> 1) * 512 + g * 128 + rho * 16 + t * 8) = gm; }
            if (!is_v) { GMSTAGE() bar(); continue; }
            if (last) { GMSTAGE() bar(); bar(); continue; }
#define E_LOADS(sb_) { stt = __builtin_amdgcn_raw_buffer_load_b32(rs, ost, sb_, 16); ef[0] = ld_sc1(rs, oe[0], sb_); ef[1] = ld_sc1(rs, oe[1], sb_); SB0 }
#define P_LOADS(sb_) { pf[0][0] = ld_sc1(rs, op[0][0], sb_); SB0 pf[1][0] = ld_sc1(rs, op[1][0], sb_); SB0 pf[0][1] = ld_sc1(rs, op[0][1], sb_); SB0 pf[1][1] = ld_sc1(rs, op[1][1], sb_); SB0 }
            const unsigned tc2 = (unsigned)__builtin_amdgcn_readfirstlane(ckr.y) + (unsigned)(((int)(__builtin_amdgcn_readfirstlane(cwx) - __builtin_amdgcn_readfirstlane(ckr.x)) * (int)__builtin_amdgcn_readfirstlane(ckr.z)) >> 8);
            spin_to(tc2 - TE * 23u);
            E_LOADS(sbr)
            asm volatile("" ::: "memory");
            spin_to(tc2);
            TL(3)
            P_LOADS(sbr)
            asm volatile("" ::: "memory");
            SB0
            if (w == 1) BT(7)
            GMSTAGE()
            v4u uf[2];
            uf[0] = *(const v4u*)(lds + L_UF + (step & 3) * 8192 + w * 2048 + lane * 16); uf[1] = *(const v4u*)(lds + L_UF + (step & 3) * 8192 + w * 2048 + 1024 + lane * 16);
            bar();
            v2u vpA, vpB;
            f4 sini = {0, 0, 0, 0};
            {
                v2u vp = *(const v2u*)(lds + L_VP + w * 512 + (2 * (g >> 1) + hp) * 128 + i8 * 16 + (g & 1) * 8);
                v2u ow = *(const v2u*)(lds + L_FL + 640 + i8 * 16 + (g & 1) * 8);
                vpA = g < 2 ? vp : v2u{0, 0};
                vpB = g < 2 ? v2u{0, 0} : vp;
                if (ownhead) { sini[0] = bflo(ow.x); sini[1] = bfhi(ow.x); sini[2] = bflo(ow.y); sini[3] = bfhi(ow.y); }
            }
            asm volatile("" ::: "memory");
            SB0
            TL(4)
            bool okall;
            auto combine = [&]() __attribute__((always_inline)) {
                f4 ls = mfma(oneLo, ef[0], f4{0, 0, 0, 0});
                ls = mfma(oneHi, ef[1], ls);
                f4 x0 = mfma(uf[0], ef[0], f4{0, 0, 0, 0});
                f4 x1 = mfma(uf[1], ef[1], f4{0, 0, 0, 0});
                unsigned t2 = vmax4(vmax4(stt, ef[0]), ef[1]);
                float lsv = ls[0];
                asm volatile("" : "+v"(t2), "+v"(lsv));
                SB0
                f4 sacc = mfma(selLo, pf[0][0], sini);
                sacc = mfma(selHi, pf[1][0], sacc);
                sacc = mfma(selLo, pf[0][1], sacc);
                sacc = mfma(selHi, pf[1][1], sacc);
                SB0
                unsigned t0 = vmax4(vmax4(t2, pf[0][0]), pf[0][1]), t1 = vmax4(vmax4(0, pf[1][0]), pf[1][1]);
                asm volatile("" : "+v"(t0), "+v"(t1));
                SB0
                const float mloc = asf(stt);
                float mx = vmax3(vmax3_mfma(sacc[0], sacc[1], sacc[2]), sacc[3], mloc);
                mx = rmax16(mx);
                float p0 = __builtin_amdgcn_exp2f(sacc[0] - mx), p1 = __builtin_amdgcn_exp2f(sacc[1] - mx), p2 = __builtin_amdgcn_exp2f(sacc[2] - mx), p3 = __builtin_amdgcn_exp2f(sacc[3] - mx);
                float f = __builtin_amdgcn_exp2f(mloc - mx);
                v2u pb; pb.x = pk2(p0, p1); pb.y = pk2(p2, p3);
                float zq = (p0 + p1) + (p2 + p3);
                SB0
                f4 cmA = mfma16(vpA, pb, f4{0, 0, 0, 0});
                f4 cmB = mfma16(vpB, pb, f4{0, 0, 0, 0});
                SB0
                float z = xsum16(zq);
                auto fs = __builtin_amdgcn_permlane32_swap(asu(f), asu(f), false, false);
                unsigned fa = fs[0], fb = fs[1];
                z += f * lsv;
                float rz = __builtin_amdgcn_rcpf(z);
                okall = __all(max(t0, t1) != SENT);
                f4 ya = x0 * asf(fa) + cmA, yb = x1 * asf(fb) + cmB;
                auto rs2 = __builtin_amdgcn_permlane32_swap(asu(rz), asu(rz), false, false);
                unsigned ra = rs2[0], rb = rs2[1];
                f4 cx = ya * asf(ra) + yb * asf(rb);
                if (hp == (g >> 1)) {
                    float* rq = red2 + i8 * 100 + (4 * (g & 1)) * 12 + 2 * w + hp;
                    rq[0] = cx[0]; rq[12] = cx[1]; rq[24] = cx[2]; rq[36] = cx[3];
                }
            };
            combine();
            if (!okall) {
                nfail2++;
                for (unsigned spin = 0; spin < SPIN_MAX && !okall; ++spin) {
                    __builtin_amdgcn_s_sleep(1);
                    asm volatile("" ::: "memory");
                    E_LOADS(sbr)
                    P_LOADS(sbr)
                    combine();
                }
            }
            TL(6)
            bar();
        }
        bar();
        __builtin_amdgcn_s_barrier();
    }
    if (lane == 0 && w < 4) dbg[16 + role * 4 + w] = nfail | ((unsigned long long)nfail2 << 32);
}

DI void signal_add(unsigned char* ws, unsigned off) {
    asm volatile("s_waitcnt vmcnt(0)" ::: "memory");
    __syncthreads();
    if (threadIdx.x == 0) __hip_atomic_fetch_add((unsigned*)(ws + CTRL_OFF + off), 1u, __ATOMIC_RELAXED, __HIP_MEMORY_SCOPE_AGENT);
}
DI void wg_wait(__amdgpu_buffer_rsrc_t rs, unsigned off_lane, unsigned need, int slp) {
    if (threadIdx.x < 64) wait_flags(rs, off_lane, need, slp);
    __syncthreads();
}

DI void gu_role(unsigned char* ws, unsigned char* lds, const int gw) {
    const int tid = threadIdx.x, lane = tid & 63, w8 = __builtin_amdgcn_readfirstlane(tid >> 6);
    const int w = w8 & 3, sp = w8 >> 2;
    const bool isG = gw < 56;
    const int nat0 = (((lane & 15) >> 2) & 1) * 14464 + ((4 * ((lane & 15) >> 3) + (lane & 3)) + 16 * (lane >> 4)) * 16;
    int sdst[7];
#pragma unroll
    for (int i = 0; i < 7; ++i) sdst[i] = (i * 512 + tid) * 16 + ((i * 8 + w8) / 14) * 128;
    v4u stg[7];
#pragma unroll
    for (int i = 0; i < 7; ++i) stg[i] = *(const v4u*)(ws + XF_OFF + (i * 512 + tid) * 16);
    v4u wt[2][14];
    const unsigned char* wsrc = isG ? ws + AS_OFF + (size_t)(4 * gw + w) * 28 * 1024 : ws + BS_OFF + ((size_t)(gw - 56) * 8 + 2 * w) * 14 * 1024;
#pragma unroll
    for (int ks = 0; ks < 14; ++ks) wt[0][ks] = *(const v4u*)(wsrc + (sp * 14 + ks) * 1024 + lane * 16);
#pragma unroll
    for (int ks = 0; ks < 14; ++ks) *(v4u*)(lds + (w8 * 14 + ks) * 1024 + lane * 16) = wt[0][ks];
    __syncthreads();
#pragma unroll
    for (int t = 0; t < 2; ++t)
#pragma unroll
        for (int ks = 0; ks < 14; ++ks) wt[t][ks] = *(const v4u*)(lds + ((w + 4 * t) * 14 + ks) * 1024 + lane * 16);
    __amdgpu_buffer_rsrc_t rs = mkrs(ws, 0x7fffffff);
    __syncthreads();
#pragma unroll
    for (int i = 0; i < 7; ++i) *(v4u*)(lds + sdst[i]) = stg[i];
    __syncthreads();
    const int bb = 4 * gw + w, hh = bb / 14, bq = bb % 14;
    const unsigned obase = isG ? (unsigned)(G_OFF + ((size_t)(hh * 2) * 14 + bq) * 1024) + lane * 16 : (unsigned)(U_OFF + ((size_t)(gw - 56) * 8 + 2 * w) * 1024) + lane * 16;
    const unsigned ostep = isG ? 14336u : 1024u;
    for (int pr = 0; pr < 32; ++pr) {
        const unsigned char* cur = lds + (pr & 1) * 57856 + sp * 28928;
        if (pr + 1 < 32) {
#pragma unroll
            for (int i = 0; i < 7; ++i) stg[i] = *(const v4u*)(ws + XF_OFF + (size_t)(pr + 1) * 57344 + (i * 512 + tid) * 16);
        }
        f4 acc[2][2] = {{f4{0,0,0,0}, f4{0,0,0,0}}, {f4{0,0,0,0}, f4{0,0,0,0}}};
        {
            const unsigned char* p0 = cur + (isG ? nat0 : lane * 16);
            const unsigned char* p1 = p0 + (isG ? 128 : 14464);
            v4u xa[3], xb[3];
#pragma unroll
            for (int k = 0; k < 3; ++k) { xa[k] = *(const v4u*)(p0 + k * 1024); xb[k] = *(const v4u*)(p1 + k * 1024); }
            if (isG) {
#pragma unroll
                for (int ks = 0; ks < 14; ++ks) {
                    const v4u x0 = xa[ks % 3], x1 = xb[ks % 3];
                    acc[0][0] = mfma(wt[0][ks], x0, acc[0][0]); acc[1][0] = mfma(wt[1][ks], x0, acc[1][0]);
                    acc[0][1] = mfma(wt[0][ks], x1, acc[0][1]); acc[1][1] = mfma(wt[1][ks], x1, acc[1][1]);
                    if (ks + 3 < 14) { xa[ks % 3] = *(const v4u*)(p0 + (ks + 3) * 1024); xb[ks % 3] = *(const v4u*)(p1 + (ks + 3) * 1024); }
                    SB0
                }
            } else {
#pragma unroll
                for (int ks = 0; ks < 14; ++ks) {
                    const v4u x0 = xa[ks % 3], x1 = xb[ks % 3];
                    acc[0][0] = mfma(x0, wt[0][ks], acc[0][0]); acc[1][0] = mfma(x1, wt[0][ks], acc[1][0]);
                    acc[0][1] = mfma(x0, wt[1][ks], acc[0][1]); acc[1][1] = mfma(x1, wt[1][ks], acc[1][1]);
                    if (ks + 3 < 14) { xa[ks % 3] = *(const v4u*)(p0 + (ks + 3) * 1024); xb[ks % 3] = *(const v4u*)(p1 + (ks + 3) * 1024); }
                    SB0
                }
            }
        }
        if (pr + 1 < 32) {
            unsigned char* nxt = lds + ((pr + 1) & 1) * 57856;
#pragma unroll
            for (int i = 0; i < 7; ++i) *(v4u*)(nxt + sdst[i]) = stg[i];
        }
        const unsigned so = __builtin_amdgcn_readfirstlane((2 * pr + sp) * 458752);
        st_sc1(pack8(acc[0][0], acc[1][0]), rs, obase, so);
        st_sc1(pack8(acc[0][1], acc[1][1]), rs, obase + ostep, so);
        const bool drain = (pr < 2) || (pr == 31);
        if (drain) asm volatile("s_waitcnt vmcnt(0)" ::: "memory");
        else asm volatile("s_waitcnt vmcnt(2)" ::: "memory");
        bar();
        if (tid == 0) __builtin_amdgcn_raw_buffer_store_b32((unsigned)(drain ? 2 * pr + 2 : 2 * pr), rs, (unsigned)CTRL_OFF + C_GU + 128u * gw, 0, 16);
    }
}

DI void a0_prep(unsigned char* ws, const int u, const u16* __restrict__ wqkv, const u16* __restrict__ wphi, const u16* __restrict__ awo) {
    const int tid = threadIdx.x, lane = tid & 63, w = __builtin_amdgcn_readfirstlane(tid >> 6);
    const int rho = lane & 15, g = lane >> 4;
    __amdgpu_buffer_rsrc_t rs = mkrs(ws, 0x7fffffff);
    {
        const int which = w >> 2, h2 = u;
        v4u bf[2];
#pragma unroll
        for (int t = 0; t < 2; ++t) {
            const int f = perm32(t, rho);
            unsigned e[8];
#pragma unroll
            for (int jj = 0; jj < 8; ++jj) { const int d = 8 * g + jj; e[jj] = (d < 28 && f < 28) ? wphi[(h2 * 28 + d) * 28 + f] : 0u; }
            bf[t] = v4u{e[0] | (e[1] << 16), e[2] | (e[3] << 16), e[4] | (e[5] << 16), e[6] | (e[7] << 16)};
        }
        v4u af[4][2];
#pragma unroll
        for (int i = 0; i < 4; ++i) {
            const int ks = min((w & 3) + 4 * i, 13);
#pragma unroll
            for (int t = 0; t < 2; ++t) {
                const u16* p = wqkv + (size_t)(32 * ks + perm32(t, rho)) * 1344 + which * 448 + h2 * 28 + g * 8;
                v2u lo = *(const v2u*)p, hi = {0, 0};
                if (g < 3) hi = *(const v2u*)(p + 4);
                af[i][t] = v4u{lo.x, lo.y, hi.x, hi.y};
            }
        }
#pragma unroll
        for (int i = 0; i < 4; ++i) {
            const int ks = (w & 3) + 4 * i;
            if (ks < 14) {
#pragma unroll
                for (int t = 0; t < 2; ++t) {
                    f4 d0 = mfma(af[i][0], bf[t], f4{0, 0, 0, 0}), d1 = mfma(af[i][1], bf[t], f4{0, 0, 0, 0});
                    st_sc1(pack8(d0 * LOG2E, d1 * LOG2E), rs, (unsigned)(WQF_OFF + which * WQF_ONE + ((size_t)(h2 * 2 + t) * 14 + ks) * 1024) + lane * 16, 0);
                }
            }
        }
    }
#pragma unroll
    for (int i = 0; i < 4; ++i) {
        const int fi = w + 8 * i;
        if (fi < 28) {
            const int t = fi / 14, ks = fi % 14;
            const int d = 16 * t + rho;
            unsigned e[8], e2[8];
            const int cb = fi >> 1, t2 = fi & 1; const int c = 32 * cb + perm32(t2, rho);
#pragma unroll
            for (int jj = 0; jj < 8; ++jj) {
                e[jj] = d < 28 ? wqkv[(size_t)(32 * ks + 8 * g + jj) * 1344 + 896 + u * 28 + d] : 0u;
                const int sl = 8 * g + jj; e2[jj] = sl < 28 ? awo[(size_t)(u * 28 + sl) * 448 + c] : 0u;
            }
            st_sc1(v4u{e[0] | (e[1] << 16), e[2] | (e[3] << 16), e[4] | (e[5] << 16), e[6] | (e[7] << 16)}, rs, (unsigned)(WQF_OFF + 2 * WQF_ONE + (size_t)(u * 28 + fi) * 1024) + lane * 16, 0);
            st_sc1(v4u{e2[0] | (e2[1] << 16), e2[2] | (e2[3] << 16), e2[4] | (e2[5] << 16), e2[6] | (e2[7] << 16)}, rs, (unsigned)(WOF_OFF + (size_t)((cb * 2 + t2) * 16 + u) * 1024) + lane * 16, 0);
        }
    }
}
DI void phi_pair(f4 d0, f4 d1, int g, v4u& pos, v4u& neg) {
    float v[8] = {d0[0], d0[1], d0[2], d0[3], d1[0], d1[1], d1[2], d1[3]};
    const bool tail = (g == 3);
    float mx = fmaxf(fmaxf(v[0], v[1]), fmaxf(v[2], v[3])), mn = fminf(fminf(v[0], v[1]), fminf(v[2], v[3]));
    float mx2 = fmaxf(fmaxf(v[4], v[5]), fmaxf(v[6], v[7])), mn2 = fminf(fminf(v[4], v[5]), fminf(v[6], v[7]));
    mx = tail ? mx : fmaxf(mx, mx2); mn = tail ? mn : fminf(mn, mn2);
    mx = xmax32(xmax16(mx)); mn = -xmax32(xmax16(-mn));
    float ep[8], en[8], sp = 0.f, sn = 0.f;
#pragma unroll
    for (int k = 0; k < 8; ++k) {
        ep[k] = __builtin_amdgcn_exp2f(v[k] - mx); en[k] = __builtin_amdgcn_exp2f(mn - v[k]);
        if (k >= 4) { ep[k] = tail ? 0.f : ep[k]; en[k] = tail ? 0.f : en[k]; }
        sp += ep[k]; sn += en[k];
    }
    sp = xsum32(xsum16(sp)); sn = xsum32(xsum16(sn));
    const float rp = __builtin_amdgcn_rcpf(sp), rn = __builtin_amdgcn_rcpf(sn);
    pos = v4u{pk2(ep[0] * rp, ep[1] * rp), pk2(ep[2] * rp, ep[3] * rp), pk2(ep[4] * rp, ep[5] * rp), pk2(ep[6] * rp, ep[7] * rp)};
    neg = v4u{pk2(en[0] * rn, en[1] * rn), pk2(en[2] * rn, en[3] * rn), pk2(en[4] * rn, en[5] * rn), pk2(en[6] * rn, en[7] * rn)};
}
DI void stage_x64(const u16* __restrict__ ax, unsigned char* lds, int blk64) {
    const int tid = threadIdx.x, lane = tid & 63, w = tid >> 6;
    v4u stg[7];
#pragma unroll
    for (int i = 0; i < 7; ++i) {
        const int f = i * 8 + w; const int q = f / 14, ks = f % 14;
        stg[i] = *(const v4u*)(ax + (size_t)(blk64 * 64 + 32 * (q >> 1) + perm32(q & 1, lane & 15)) * D + ks * 32 + (lane >> 4) * 8);
    }
#pragma unroll
    for (int i = 0; i < 7; ++i) *(v4u*)(lds + (i * 512 + tid) * 16) = stg[i];
}

DI void a1_unit(unsigned char* ws, unsigned char* lds, const int u, const u16* __restrict__ ax) {
    const int tid = threadIdx.x, lane = tid & 63, w = __builtin_amdgcn_readfirstlane(tid >> 6);
    const int rho = lane & 15, g = lane >> 4;
    __amdgpu_buffer_rsrc_t rs = mkrs(ws, 0x7fffffff);
    unsigned char* T = lds + 57344 + w * 9216;
    constexpr int TS = 144;
    f4 kv[2][4][2];
#pragma unroll
    for (int a = 0; a < 2; ++a)
#pragma unroll
        for (int b = 0; b < 4; ++b) { kv[a][b][0] = f4{0,0,0,0}; kv[a][b][1] = f4{0,0,0,0}; }
    for (int pass = 0; pass < 2; ++pass) {
        __syncthreads();
        stage_x64(ax, lds, 2 * u + pass);
        __syncthreads();
#pragma unroll
        for (int hi = 0; hi < 2; ++hi) {
            const int h = 2 * w + hi;
            const unsigned wk = (unsigned)(WQF_OFF + WQF_ONE + (size_t)h * 28672) + lane * 16;
            const unsigned wv = (unsigned)(WQF_OFF + 2 * WQF_ONE + (size_t)h * 28672) + lane * 16;
            f4 ak[2][4], av[2][4];
#pragma unroll
            for (int t = 0; t < 2; ++t)
#pragma unroll
                for (int q = 0; q < 4; ++q) { ak[t][q] = f4{0,0,0,0}; av[t][q] = f4{0,0,0,0}; }
#pragma unroll 2
            for (int ks = 0; ks < 14; ++ks) {
                v4u k0 = ld_sc1(rs, wk + ks * 1024, 0), k1 = ld_sc1(rs, wk + (14 + ks) * 1024, 0);
                v4u v0 = ld_sc1(rs, wv + ks * 1024, 0), v1 = ld_sc1(rs, wv + (14 + ks) * 1024, 0);
#pragma unroll
                for (int q = 0; q < 4; ++q) {
                    v4u x = *(const v4u*)(lds + (q * 14 + ks) * 1024 + lane * 16);
                    ak[0][q] = mfma(k0, x, ak[0][q]); ak[1][q] = mfma(k1, x, ak[1][q]);
                    av[0][q] = mfma(x, v0, av[0][q]); av[1][q] = mfma(x, v1, av[1][q]);
                }
            }
#pragma unroll
            for (int q = 0; q < 4; ++q) {
                v4u pos, neg; phi_pair(ak[0][q], ak[1][q], g, pos, neg);
                const int tok = 32 * (q >> 1) + perm32(q & 1, rho);
                *(v4u*)(T + tok * TS + g * 16) = pos;
                *(v4u*)(T + tok * TS + 64 + g * 16) = neg;
            }
            asm volatile("s_waitcnt lgkmcnt(0)" ::: "memory");
            v4u vf[2][2];
#pragma unroll
            for (int bk = 0; bk < 2; ++bk)
#pragma unroll
                for (int t = 0; t < 2; ++t) {
                    vf[bk][t] = pack8(av[t][2 * bk], av[t][2 * bk + 1]);
                    if (t == 1 && rho == 12) vf[bk][t] = v4u{0x3F803F80u, 0x3F803F80u, 0x3F803F80u, 0x3F803F80u};
                }
            const unsigned trb = (8 * g + (rho >> 2)) * TS + (lane & 3) * 8;
#pragma unroll
            for (int bk = 0; bk < 2; ++bk)
#pragma unroll
                for (int ft = 0; ft < 4; ++ft) {
                    v2u lo = trread(T + trb + (32 * bk) * TS + ft * 32), hi2 = trread(T + trb + (32 * bk + 4) * TS + ft * 32);
                    v4u pa = {lo.x, lo.y, hi2.x, hi2.y};
                    kv[hi][ft][0] = mfma(pa, vf[bk][0], kv[hi][ft][0]);
                    kv[hi][ft][1] = mfma(pa, vf[bk][1], kv[hi][ft][1]);
                }
            asm volatile("s_waitcnt lgkmcnt(0)" ::: "memory");
        }
    }
#pragma unroll
    for (int hi = 0; hi < 2; ++hi)
#pragma unroll
        for (int ft = 0; ft < 4; ++ft)
#pragma unroll
            for (int dt = 0; dt < 2; ++dt)
                st_sc1(__builtin_bit_cast(v4u, kv[hi][ft][dt]), rs, (unsigned)(KVP_OFF + ((size_t)(u * 16 + 2 * w + hi) * 8 + ft * 2 + dt) * 1024) + lane * 16, 0);
}
DI void a15_unit(unsigned char* ws, unsigned char* lds, const int h) {
    const int tid = threadIdx.x, lane = tid & 63, w = tid >> 6;
    __amdgpu_buffer_rsrc_t rs = mkrs(ws, 0x7fffffff);
    f4 s = {0, 0, 0, 0};
    v4u t[16];
#pragma unroll
    for (int u = 0; u < 16; ++u) t[u] = ld_sc1(rs, (unsigned)(KVP_OFF + ((size_t)(u * 16 + h) * 8) * 1024) + tid * 16, 0);
#pragma unroll
    for (int u = 0; u < 16; ++u) s += __builtin_bit_cast(f4, t[u]);
    const int ft = w >> 1, dt = w & 1, rho = lane & 15, g = lane >> 4;
    __syncthreads();
    v2u o; o.x = pk2(s[0], s[1]); o.y = pk2(s[2], s[3]);
    *(v2u*)(lds + (16 * dt + rho) * 128 + (16 * ft + 4 * g) * 2) = o;
    __syncthreads();
    if (tid < 256) {
        const int fr = tid >> 6;
        const int tt = fr >> 1, kb = fr & 1;
        v4u v = *(const v4u*)(lds + perm32(tt, rho) * 128 + (32 * kb + 8 * g) * 2);
        st_sc1(v, rs, (unsigned)(KVF_OFF + (size_t)(h * 4 + fr) * 1024) + lane * 16, 0);
    }
}
DI void a2_unit(unsigned char* ws, unsigned char* lds, const int u, u16* __restrict__ out, const u16* __restrict__ ax) {
    int tid = threadIdx.x; asm volatile("" : "+v"(tid));
    const int lane = tid & 63, w = __builtin_amdgcn_readfirstlane(tid >> 6);
    const int rho = lane & 15, g = lane >> 4;
    __amdgpu_buffer_rsrc_t rs = mkrs(ws, 0x7fffffff);
    unsigned char* O = lds + 57344;
    __syncthreads();
    stage_x64(ax, lds, u);
    wg_wait(rs, C_A0, 16, 4);
    v4u pos[2][4], neg[2][4];
#pragma unroll
    for (int hi = 0; hi < 2; ++hi) {
        const int h = 2 * w + hi;
        const unsigned wq = (unsigned)(WQF_OFF + (size_t)h * 28672) + lane * 16;
        f4 aq[2][4];
#pragma unroll
        for (int t = 0; t < 2; ++t)
#pragma unroll
            for (int q = 0; q < 4; ++q) aq[t][q] = f4{0,0,0,0};
#pragma unroll 2
        for (int ks = 0; ks < 14; ++ks) {
            v4u k0 = ld_sc1(rs, wq + ks * 1024, 0), k1 = ld_sc1(rs, wq + (14 + ks) * 1024, 0);
#pragma unroll
            for (int q = 0; q < 4; ++q) {
                v4u x = *(const v4u*)(lds + (q * 14 + ks) * 1024 + lane * 16);
                aq[0][q] = mfma(k0, x, aq[0][q]); aq[1][q] = mfma(k1, x, aq[1][q]);
            }
        }
#pragma unroll
        for (int q = 0; q < 4; ++q) phi_pair(aq[0][q], aq[1][q], g, pos[hi][q], neg[hi][q]);
    }
    wg_wait(rs, C_KV, 16, 4);
#pragma unroll
    for (int hi = 0; hi < 2; ++hi) {
        const int h = 2 * w + hi;
        v4u kf[4];
#pragma unroll
        for (int fr = 0; fr < 4; ++fr) kf[fr] = ld_sc1(rs, (unsigned)(KVF_OFF + (size_t)(h * 4 + fr) * 1024) + lane * 16, 0);
#pragma unroll
        for (int q = 0; q < 4; ++q) {
            f4 n0 = mfma(kf[0], pos[hi][q], f4{0,0,0,0}); n0 = mfma(kf[1], neg[hi][q], n0);
            f4 n1 = mfma(kf[2], pos[hi][q], f4{0,0,0,0}); n1 = mfma(kf[3], neg[hi][q], n1);
            const float den = asf((unsigned)__builtin_amdgcn_ds_bpermute((48 + rho) * 4, (int)asu(n1[0])));
            const float rd = 1.0f / (den + 1e-6f);
            if (g == 3) n1 = f4{0, 0, 0, 0};
            *(v4u*)(O + (h * 4 + q) * 1024 + lane * 16) = pack8(n0 * rd, n1 * rd);
        }
    }
    __syncthreads();
    for (int cb = w; cb < 14; cb += 8) {
        f4 acc[2][4];
#pragma unroll
        for (int t = 0; t < 2; ++t)
#pragma unroll
            for (int q = 0; q < 4; ++q) acc[t][q] = f4{0,0,0,0};
        const unsigned wo = (unsigned)(WOF_OFF + (size_t)cb * 32768) + lane * 16;
#pragma unroll 4
        for (int h = 0; h < 16; ++h) {
            v4u w0 = ld_sc1(rs, wo + h * 1024, 0), w1 = ld_sc1(rs, wo + (16 + h) * 1024, 0);
#pragma unroll
            for (int q = 0; q < 4; ++q) {
                v4u o = *(const v4u*)(O + (h * 4 + q) * 1024 + lane * 16);
                acc[0][q] = mfma(w0, o, acc[0][q]); acc[1][q] = mfma(w1, o, acc[1][q]);
            }
        }
#pragma unroll
        for (int q = 0; q < 4; ++q) {
            const int tok = u * 64 + 32 * (q >> 1) + perm32(q & 1, rho);
            *(v4u*)(out + (size_t)tok * OUTW + 32 * cb + 8 * g) = pack8(acc[0][q], acc[1][q]);
        }
    }
}

DI void p2_unit(unsigned char* ws, unsigned char* lds, const int unit, u16* __restrict__ out) {
    const int seg = unit >> 1, chalf = unit & 1;
    int tid = threadIdx.x; asm volatile("" : "+v"(tid));
    const int lane = tid & 63, w = __builtin_amdgcn_readfirstlane(tid >> 6);
    const int rho = lane & 15, g = lane >> 4;
    __amdgpu_buffer_rsrc_t rs = mkrs(ws, 0x7fffffff);
    unsigned char* xl = lds + 40960;
    __syncthreads();
    for (int i = tid; i < 1792; i += 512) *(v4u*)(xl + i * 16) = *(const v4u*)(ws + XF_OFF + (size_t)seg * 28672 + i * 16);
    if (tid < 128) wait_flags(rs, C_GU + 128u * (unsigned)min(tid, 111), seg + 1, 8);
    __syncthreads();
    f4 sx[2][2][2];
#pragma unroll
    for (int hi = 0; hi < 2; ++hi) {
#pragma unroll
        for (int a = 0; a < 2; ++a) { sx[hi][a][0] = f4{0,0,0,0}; sx[hi][a][1] = f4{0,0,0,0}; }
        const unsigned gb = (unsigned)(G_OFF + (size_t)(seg * 16 + 2 * w + hi) * 28672) + lane * 16;
#pragma unroll 7
        for (int ks = 0; ks < 14; ++ks) {
            v4u g0 = ld_sc1(rs, gb + ks * 1024, 0), g1 = ld_sc1(rs, gb + (14 + ks) * 1024, 0);
            v4u x0 = *(const v4u*)(xl + ks * 1024 + lane * 16), x1 = *(const v4u*)(xl + (14 + ks) * 1024 + lane * 16);
            sx[hi][0][0] = mfma(g0, x0, sx[hi][0][0]); sx[hi][0][1] = mfma(g0, x1, sx[hi][0][1]);
            sx[hi][1][0] = mfma(g1, x0, sx[hi][1][0]); sx[hi][1][1] = mfma(g1, x1, sx[hi][1][1]);
        }
    }
    const int cb = 7 * chalf + (w < 7 ? w : 6);
    v4u uf[2][16];
    unsigned uo[2], vo[2];
#pragma unroll
    for (int t = 0; t < 2; ++t) {
        const int c = 32 * cb + perm32(t, rho); const int j = c >> 3, c8 = c & 7;
        uo[t] = (unsigned)(U_OFF + (size_t)(seg * 56 + j) * 8192) + (c8 + 16 * g) * 16;
        vo[t] = (unsigned)(VP_OFF + (size_t)(seg * 56 + j) * 2048) + g * 128 + c8 * 16;
#pragma unroll
        for (int h = 0; h < 16; ++h) uf[t][h] = ld_sc1(rs, uo[t] + (h >> 1) * 1024 + (h & 1) * 128, 0);
    }
    if (tid < 64) { if (seg < 61) wait_flags(rs, C_REC, seg + 1, 8); else wait_flags(rs, C_DONE, 112, 2); }
    __syncthreads();
    f4 sm[2] = {f4{0,0,0,0}, f4{0,0,0,0}};
    {
        const unsigned gmb = (unsigned)(GM_OFF + (size_t)(seg * 16 + 2 * w + (rho >> 3)) * 7168) + g * 128 + (rho & 7) * 16;
        v4u gm[10];
#pragma unroll
        for (int ks = 0; ks < 10; ++ks) gm[ks] = ld_sc1(rs, gmb + ks * 512, 0);
        SB0
#pragma unroll
        for (int ks = 0; ks < 14; ++ks) {
            v4u x0 = *(const v4u*)(xl + ks * 1024 + lane * 16), x1 = *(const v4u*)(xl + (14 + ks) * 1024 + lane * 16);
            sm[0] = mfma(gm[ks % 10], x0, sm[0]); sm[1] = mfma(gm[ks % 10], x1, sm[1]);
            if (ks < 4) gm[ks] = ld_sc1(rs, gmb + (10 + ks) * 512, 0);
            SB0
        }
    }
    v4u vf[2][4];
#pragma unroll
    for (int t = 0; t < 2; ++t)
#pragma unroll
        for (int w4 = 0; w4 < 4; ++w4) vf[t][w4] = ld_sc1(rs, vo[t] + w4 * 512, 0);
#pragma unroll
    for (int hi = 0; hi < 2; ++hi) {
        const int h = 2 * w + hi;
        const bool own = ((g >> 1) == hi);
#pragma unroll
        for (int tp = 0; tp < 2; ++tp) {
            const int tq = perm32(tp, rho);
            float v[8];
#pragma unroll
            for (int tau = 0; tau < 2; ++tau)
#pragma unroll
                for (int r = 0; r < 4; ++r) { const int key = 16 * tau + 4 * g + r; v[4 * tau + r] = (key <= tq) ? sx[hi][tau][tp][r] : -3.0e38f; }
            float mx = fmaxf(fmaxf(fmaxf(v[0], v[1]), fmaxf(v[2], v[3])), fmaxf(fmaxf(v[4], v[5]), fmaxf(v[6], v[7])));
            const float mo = fmaxf(fmaxf(sm[tp][0], sm[tp][1]), fmaxf(sm[tp][2], sm[tp][3]));
            mx = own ? fmaxf(mx, mo) : mx;
            mx = xmax32(xmax16(mx));
            float sum = 0.f, em[4];
#pragma unroll
            for (int k = 0; k < 8; ++k) { v[k] = __builtin_amdgcn_exp2f(v[k] - mx); sum += v[k]; }
#pragma unroll
            for (int r = 0; r < 4; ++r) { em[r] = own ? __builtin_amdgcn_exp2f(sm[tp][r] - mx) : 0.f; sum += em[r]; }
            sum = xsum32(xsum16(sum));
            const float rz = __builtin_amdgcn_rcpf(sum);
            v2u p0; p0.x = pk2(v[0] * rz, v[1] * rz); p0.y = pk2(v[2] * rz, v[3] * rz);
            v2u p1; p1.x = pk2(v[4] * rz, v[5] * rz); p1.y = pk2(v[6] * rz, v[7] * rz);
            *(v2u*)(lds + (h * 2 + tp) * 1024 + (rho + 16 * (g >> 1)) * 16 + (g & 1) * 8) = p0;
            *(v2u*)(lds + (h * 2 + tp) * 1024 + (rho + 16 * (2 + (g >> 1))) * 16 + (g & 1) * 8) = p1;
            if (own) {
                v2u pm; pm.x = pk2(em[0] * rz, em[1] * rz); pm.y = pk2(em[2] * rz, em[3] * rz);
                *(v2u*)(lds + ((16 + (h >> 2)) * 2 + tp) * 1024 + (rho + 16 * (h & 3)) * 16 + (g & 1) * 8) = pm;
            }
        }
    }
    __syncthreads();
    if (w < 7) {
        f4 acc[2][2] = {{f4{0,0,0,0}, f4{0,0,0,0}}, {f4{0,0,0,0}, f4{0,0,0,0}}};
#pragma unroll
        for (int h = 0; h < 16; ++h) {
            v4u p0 = *(const v4u*)(lds + (h * 2) * 1024 + lane * 16), p1 = *(const v4u*)(lds + (h * 2 + 1) * 1024 + lane * 16);
#pragma unroll
            for (int t = 0; t < 2; ++t) { acc[t][0] = mfma(uf[t][h], p0, acc[t][0]); acc[t][1] = mfma(uf[t][h], p1, acc[t][1]); }
        }
#pragma unroll
        for (int w4 = 0; w4 < 4; ++w4) {
            v4u p0 = *(const v4u*)(lds + ((16 + w4) * 2) * 1024 + lane * 16), p1 = *(const v4u*)(lds + ((16 + w4) * 2 + 1) * 1024 + lane * 16);
#pragma unroll
            for (int t = 0; t < 2; ++t) { acc[t][0] = mfma(vf[t][w4], p0, acc[t][0]); acc[t][1] = mfma(vf[t][w4], p1, acc[t][1]); }
        }
#pragma unroll
        for (int tp = 0; tp < 2; ++tp) {
            const int tok = seg * 32 + perm32(tp, rho);
            *(v4u*)(out + (size_t)tok * OUTW + 448 + 32 * cb + 8 * g) = pack8(acc[0][tp], acc[1][tp]);
        }
    }
}

constexpr int LDS_MAIN = 135168;
__global__ void __launch_bounds__(512, 2) k_main(unsigned char* ws, u16* __restrict__ out, const u16* __restrict__ ax, const u16* __restrict__ wqkv, const u16* __restrict__ wphi, const u16* __restrict__ awo, int dS, int dV, int dVm, int pad_) {
    extern __shared__ __attribute__((aligned(16))) unsigned char lds[];
    const int bidx = blockIdx.x;
    const int idx = (bidx >> 3) * 4 + (bidx & 3);
#define PT(i)
    if ((bidx & 7) < 4) { rec_role(ws, lds, idx, dS, dV, dVm); return; }
    __amdgpu_buffer_rsrc_t rs = mkrs(ws, 0x7fffffff);
    if (idx < 112) gu_role(ws, lds, idx);
    else {
        const int u = idx - 112;
        a0_prep(ws, u, wqkv, wphi, awo);
        signal_add(ws, C_A0);
        wg_wait(rs, C_A0, 16, 2);
        a1_unit(ws, lds, u, ax);
        signal_add(ws, C_A1);
        wg_wait(rs, C_A1, 16, 4);
        a15_unit(ws, lds, u);
        signal_add(ws, C_KV);
    }
    PT(0)
    for (;;) {
        __syncthreads();
        if (threadIdx.x == 0) LDSV(LDS_MAIN - 16) = __hip_atomic_fetch_add((unsigned*)(ws + CTRL_OFF + C_TICKET), 1u, __ATOMIC_RELAXED, __HIP_MEMORY_SCOPE_AGENT);
        __syncthreads();
        const int t = __builtin_amdgcn_readfirstlane(LDSV(LDS_MAIN - 16));
        if (t >= 160) break;
        if (t < 32) a2_unit(ws, lds, t, out, ax);
        else p2_unit(ws, lds, t - 32, out);
        if (t < 32) { PT(1) } else { PT(2) }
    }
}

#define REC_DS (86 | (50 << 8))
#define REC_DV (80 | (0 << 8))
#define REC_DVM (3 | (2 << 8) | (0 << 16))
constexpr int g_dS = REC_DS, g_dV = REC_DV, g_dVm = REC_DVM;

extern "C" void kernel_launch(void* const* d_in, const int* in_sizes, int n_in, void* d_out, int out_size, void* d_ws, size_t ws_size, hipStream_t stream) {
    const u16* A_x = (const u16*)d_in[0]; const u16* A_wqkv = (const u16*)d_in[1]; const u16* A_wphi = (const u16*)d_in[2]; const u16* A_wo = (const u16*)d_in[3];
    const u16* B_x = (const u16*)d_in[4]; const u16* B_mem0 = (const u16*)d_in[5];
    const u16* Wq = (const u16*)d_in[6]; const u16* Wk = (const u16*)d_in[7]; const u16* Wv = (const u16*)d_in[8]; const u16* Wo = (const u16*)d_in[9];
    u16* out = (u16*)d_out;
    unsigned char* ws = (unsigned char*)(((uintptr_t)d_ws + 2 * MB - 1) & ~(uintptr_t)(2 * MB - 1));
    (void)hipFuncSetAttribute((const void*)k_pre, hipFuncAttributeMaxDynamicSharedMemorySize, 61440);
    (void)hipFuncSetAttribute((const void*)k_main, hipFuncAttributeMaxDynamicSharedMemorySize, LDS_MAIN);
    k_pre<<<256, 448, 61440, stream>>>(ws, Wq, Wk, Wv, Wo, B_x, B_mem0);
    k_main<<<256, 512, LDS_MAIN, stream>>>(ws, out, A_x, A_wqkv, A_wphi, A_wo, g_dS, g_dV, g_dVm, 0);
}
```

```cpp
#include <hip/hip_runtime.h>
#include <hip/hip_bf16.h>
#include <stdint.h>

typedef unsigned short u16;
typedef unsigned v4u __attribute__((ext_vector_type(4)));
typedef unsigned v2u __attribute__((ext_vector_type(2)));
typedef float f4 __attribute__((ext_vector_type(4)));
typedef float f2 __attribute__((ext_vector_type(2)));
typedef short s8 __attribute__((ext_vector_type(8)));
typedef __bf16 bf2 __attribute__((ext_vector_type(2)));
#define DI __device__ __forceinline__

constexpr int D = 448, H = 16, DH = 256, MEM = 8, SEG = 32, NSEG = 64, S_TOK = 2048;
constexpr int NKS = 14;
constexpr int OUTW = 896;
constexpr unsigned SENT = 0xFFFFFFFFu;
constexpr float LOG2E = 1.4426950408889634f;

constexpr size_t MB = 1024 * 1024;
constexpr int KCOPY = 4;
constexpr size_t COMM_OFF = 0;
constexpr size_t COMM_BYTES = 2 * MB;
constexpr size_t CTRL_OFF = COMM_BYTES;
constexpr size_t AS_OFF = CTRL_OFF + 262144;
constexpr size_t AS_BYTES = (size_t)16 * 14 * 2 * 14 * 1024;
constexpr size_t BS_OFF = AS_OFF + AS_BYTES;
constexpr size_t BS_BYTES = (size_t)56 * 8 * 14 * 1024;
constexpr size_t XF_OFF = BS_OFF + BS_BYTES;
constexpr size_t XF_BYTES = (size_t)64 * 28 * 1024;
constexpr size_t G_OFF = XF_OFF + XF_BYTES;
constexpr size_t G_BYTES = (size_t)64 * 16 * 28 * 1024;
constexpr size_t U_OFF = G_OFF + G_BYTES;
constexpr size_t U_BYTES = (size_t)64 * 56 * 8 * 1024;
constexpr size_t GM_OFF = U_OFF + U_BYTES;
constexpr size_t GM_BYTES = (size_t)64 * 16 * 14 * 512;
constexpr size_t VP_OFF = GM_OFF + GM_BYTES;
constexpr size_t VP_BYTES = (size_t)64 * 56 * 4 * 512;
constexpr size_t AXF_OFF = VP_OFF + VP_BYTES;
constexpr size_t WQF_OFF = AXF_OFF + XF_BYTES;
constexpr size_t WQF_ONE = (size_t)16 * 2 * 14 * 1024;
constexpr size_t WOF_OFF = WQF_OFF + 3 * WQF_ONE;
constexpr size_t KVP_OFF = WOF_OFF + WQF_ONE;
constexpr size_t KVF_OFF = KVP_OFF + (size_t)16 * 16 * 8192;
constexpr size_t WS_END = KVF_OFF + 65536;
constexpr unsigned C_POL = 0, C_REC = 128, C_DONE = 256, C_A1 = 384, C_KV = 512, C_TICKET = 640, C_A0 = 768, C_GU = 1024;
constexpr unsigned C_DN2 = 16384;
constexpr unsigned C_DBG = 32768;

DI unsigned pk2(float a, float b) { f2 v = {a, b}; return __builtin_bit_cast(unsigned, __builtin_convertvector(v, bf2)); }
DI float asf(unsigned x) { return __builtin_bit_cast(float, x); }
DI unsigned asu(float x) { return __builtin_bit_cast(unsigned, x); }
DI float bflo(unsigned x) { return __builtin_bit_cast(float, x << 16); }
DI float bfhi(unsigned x) { return __builtin_bit_cast(float, x & 0xffff0000u); }
DI float bf2f(u16 x) { return __builtin_bit_cast(float, (unsigned)x << 16); }
DI f4 mfma(v4u a, v4u b, f4 c) { return __builtin_amdgcn_mfma_f32_16x16x32_bf16(__builtin_bit_cast(s8, a), __builtin_bit_cast(s8, b), c, 0, 0, 0); }
DI v4u pack8(f4 a, f4 b) { v4u r; r.x = pk2(a[0], a[1]); r.y = pk2(a[2], a[3]); r.z = pk2(b[0], b[1]); r.w = pk2(b[2], b[3]); return r; }
DI v4u nosent(v4u v) { v.x = min(v.x, 0xFFFFFFFEu); v.y = min(v.y, 0xFFFFFFFEu); v.z = min(v.z, 0xFFFFFFFEu); v.w = min(v.w, 0xFFFFFFFEu); return v; }
DI int perm32(int t, int rho) { return 8 * (rho >> 2) + 4 * t + (rho & 3); }
DI __amdgpu_buffer_rsrc_t mkrs(void* p, unsigned bytes) { return __builtin_amdgcn_make_buffer_rsrc(p, 0, bytes, 0x00020000); }
DI v4u ld_sc1(__amdgpu_buffer_rsrc_t rs, unsigned voff, unsigned soff) { return __builtin_amdgcn_raw_buffer_load_b128(rs, voff, soff, 16); }
DI v2u ld2_sc1(__amdgpu_buffer_rsrc_t rs, unsigned voff, unsigned soff) { return __builtin_amdgcn_raw_buffer_load_b64(rs, voff, soff, 16); }
DI void st_sc1(v4u v, __amdgpu_buffer_rsrc_t rs, unsigned voff, unsigned soff) { __builtin_amdgcn_raw_buffer_store_b128(v, rs, voff, soff, 16); }
DI void st2_sc1(v2u v, __amdgpu_buffer_rsrc_t rs, unsigned voff, unsigned soff) { __builtin_amdgcn_raw_buffer_store_b64(v, rs, voff, soff, 16); }
DI unsigned long long mtime() { unsigned long long t; asm volatile("s_memtime %0\n\ts_waitcnt lgkmcnt(0)" : "=s"(t) :: "memory"); return t; }

typedef short s4t __attribute__((ext_vector_type(4)));
DI v2u trread(const unsigned char* p) { return __builtin_bit_cast(v2u, __builtin_amdgcn_ds_read_tr16_b64_v4i16((__attribute__((address_space(3))) s4t*)p)); }
DI unsigned comm_step_base(int step, int copy) { return __builtin_amdgcn_readfirstlane((unsigned)(step & 7) * 262144u + (unsigned)copy * 65536u); }
DI unsigned comm_off(int chunk0, unsigned o, unsigned pol) { return (unsigned)(chunk0 + (o >> 13)) * 16384u + pol * 8192u + (o & 8191u); }

__global__ void __launch_bounds__(448) k_pre(unsigned char* ws, const u16* __restrict__ Wq, const u16* __restrict__ Wk, const u16* __restrict__ Wv, const u16* __restrict__ Wo,
                                             const u16* __restrict__ bx, const u16* __restrict__ mem0) {
    extern __shared__ __attribute__((aligned(16))) unsigned char lds[];
    const int tid = threadIdx.x, lane = tid & 63, w = __builtin_amdgcn_readfirstlane(tid >> 6);
    const int wg = blockIdx.x;
#define PP(i)
    const int h = (wg & 7) * 2 + ((wg >> 3) & 1), rest = wg >> 4;
    const int mat = rest >> 3, rh = (rest >> 2) & 1, q = rest & 3;
    const int rho = lane & 15, g = lane >> 4;
    const int rb = 7 * rh + w;
    __amdgpu_buffer_rsrc_t rs = mkrs(ws, 0x7fffffff);
    const int gt = wg * 448 + tid; constexpr int GN = 256 * 448;
    constexpr int WST = 240;
    v4u bt[8];
    if (mat == 0) {
        const int ct = 7 * q + w; const int d = 32 * (ct >> 1) + perm32(ct & 1, rho);
#pragma unroll
        for (int es = 0; es < 8; ++es) bt[es] = *(const v4u*)(Wq + ((size_t)d * H + h) * DH + es * 32 + g * 8);
    } else {
#pragma unroll
        for (int it = 0; it < 8; ++it) {
            const int idx = it * 448 + tid; const int e = idx / 14, c8 = idx % 14;
            bt[it] = *(const v4u*)(Wo + ((size_t)h * DH + e) * D + 112 * q + c8 * 8);
        }
    }
    const u16* Wr = mat ? Wv : Wk;
    v4u a[2][8];
#pragma unroll
    for (int t = 0; t < 2; ++t) {
        const int row = 32 * rb + perm32(t, rho);
#pragma unroll
        for (int es = 0; es < 8; ++es) a[t][es] = *(const v4u*)(Wr + ((size_t)row * H + h) * DH + es * 32 + g * 8);
    }
    v4u xfv;
    {
        const int f = gt >> 6; const int ks = f % 14; const int st = f / 14; const int tau = st & 1, seg = st >> 1;
        xfv = *(const v4u*)(bx + (size_t)(seg * 32 + perm32(tau, rho)) * D + ks * 32 + g * 8);
    }
    __builtin_amdgcn_sched_barrier(0);
    if (wg == 1 && tid < 128) __builtin_amdgcn_raw_buffer_store_b32(0u, rs, (unsigned)CTRL_OFF + tid * 128, 0, 16);
    if (wg == 2 && tid < 128) __builtin_amdgcn_raw_buffer_store_b32(0u, rs, (unsigned)CTRL_OFF + C_DN2 + tid * 4, 0, 16);
    for (unsigned i = gt; i < COMM_BYTES / 16; i += GN) {
        unsigned off = i * 16;
        unsigned within = off & 65535u, slot = off >> 18;
        unsigned chunk = (within >> 14) & 3, o = within & 8191u;
        if (chunk == 0 && (o >= 7184 || (slot == 0 && o < 7168))) continue;
        st_sc1(v4u{SENT, SENT, SENT, SENT}, rs, off, 0);
    }
    PP(0)
    if (mat == 0) {
#pragma unroll
        for (int es = 0; es < 8; ++es) *(v4u*)(lds + (w * 8 + es) * 1024 + lane * 16) = bt[es];
    } else {
#pragma unroll
        for (int it = 0; it < 8; ++it) { const int idx = it * 448 + tid; const int e = idx / 14, c8 = idx % 14; *(v4u*)(lds + e * WST + c8 * 16) = bt[it]; }
    }
    PP(3)
    __syncthreads();
    PP(4)
    f4 acc[2][7];
#pragma unroll
    for (int t = 0; t < 2; ++t)
#pragma unroll
        for (int c = 0; c < 7; ++c) acc[t][c] = f4{0, 0, 0, 0};
    if (mat == 0) {
#pragma unroll
        for (int es = 0; es < 8; ++es)
#pragma unroll
            for (int c = 0; c < 7; ++c) {
                v4u bfr = *(const v4u*)(lds + (c * 8 + es) * 1024 + lane * 16);
                acc[0][c] = mfma(a[0][es], bfr, acc[0][c]);
                acc[1][c] = mfma(a[1][es], bfr, acc[1][c]);
            }
        const float sc = LOG2E * 0.0625f;
#pragma unroll
        for (int c = 0; c < 7; ++c) {
            const int ct = 7 * q + c;
            st_sc1(pack8(acc[0][c] * sc, acc[1][c] * sc), rs, (unsigned)(AS_OFF + ((size_t)((h * 14 + (ct >> 1)) * 2 + (ct & 1)) * 14 + rb) * 1024) + lane * 16, 0);
        }
    } else {
        const unsigned trb = (8 * g + (rho >> 2)) * WST + (lane & 3) * 8;
#pragma unroll
        for (int es = 0; es < 8; ++es)
#pragma unroll
            for (int c = 0; c < 7; ++c) {
                v2u lo = trread(lds + trb + (32 * es) * WST + c * 32), hi = trread(lds + trb + (32 * es + 4) * WST + c * 32);
                v4u bfr = {lo.x, lo.y, hi.x, hi.y};
                acc[0][c] = mfma(a[0][es], bfr, acc[0][c]);
                acc[1][c] = mfma(a[1][es], bfr, acc[1][c]);
            }
#pragma unroll
        for (int c = 0; c < 7; ++c) {
            const int ct = 7 * q + c;
            const int j = 2 * ct + (rho >> 3);
            st_sc1(pack8(acc[0][c], acc[1][c]), rs, (unsigned)(BS_OFF + ((size_t)(j * 8 + (h >> 1)) * 14 + rb) * 1024) + (((h & 1) * 8 + (rho & 7)) + 16 * g) * 16, 0);
        }
    }
    st_sc1(xfv, rs, (unsigned)XF_OFF + gt * 16, 0);
    if (wg < 8) {
        const int j = tid >> 3, irow = tid & 7;
        st_sc1(nosent(*(const v4u*)(mem0 + irow * D + j * 8)), rs, (wg >> 1) * 65536u + (wg & 1) * 8192u + tid * 16, 0);
    }
    PP(5)
}

DI unsigned umax3(unsigned a, unsigned b, unsigned c) { return max(max(a, b), c); }
DI unsigned vmax4(unsigned t, v4u v) { return umax3(umax3(t, v.x, v.y), v.z, v.w); }
DI float dpp_xor8(float v) { return asf((unsigned)__builtin_amdgcn_update_dpp(0, (int)asu(v), 0x128, 0xf, 0xf, false)); }
DI unsigned dpp_xor8u(unsigned v) { return (unsigned)__builtin_amdgcn_update_dpp(0, (int)v, 0x128, 0xf, 0xf, false); }
constexpr unsigned SPIN_MAX = 200000;
DI float dpp_x1(float v) { return asf((unsigned)__builtin_amdgcn_update_dpp(0, (int)asu(v), 0xB1, 0xf, 0xf, false)); }
DI float dpp_x2(float v) { return asf((unsigned)__builtin_amdgcn_update_dpp(0, (int)asu(v), 0x4E, 0xf, 0xf, false)); }
DI f4 quadsum(f4 v) {
#pragma unroll
    for (int r = 0; r < 4; ++r) v[r] += dpp_x1(v[r]);
#pragma unroll
    for (int r = 0; r < 4; ++r) v[r] += dpp_x2(v[r]);
    return v;
}
DI float xmax16(float x) { auto r = __builtin_amdgcn_permlane16_swap(asu(x), asu(x), false, false); unsigned a = r[0], b = r[1]; return fmaxf(asf(a), asf(b)); }
DI float xmax32(float x) { auto r = __builtin_amdgcn_permlane32_swap(asu(x), asu(x), false, false); unsigned a = r[0], b = r[1]; return fmaxf(asf(a), asf(b)); }
DI float xsum16(float x) { auto r = __builtin_amdgcn_permlane16_swap(asu(x), asu(x), false, false); unsigned a = r[0], b = r[1]; return asf(a) + asf(b); }
DI float xsum32(float x) { auto r = __builtin_amdgcn_permlane32_swap(asu(x), asu(x), false, false); unsigned a = r[0], b = r[1]; return asf(a) + asf(b); }
#define LOADM(mf) v4u mf[14]; _Pragma("unroll") for (int ks = 0; ks < 14; ++ks) mf[ks] = *(const v4u*)(ldsm + ks * 512 + mfrag_off); __builtin_amdgcn_sched_barrier(0);
DI void bar() { asm volatile("s_waitcnt lgkmcnt(0)" ::: "memory"); __builtin_amdgcn_s_barrier(); asm volatile("" ::: "memory"); }
DI unsigned now32() { return (unsigned)mtime(); }
DI void wait_until(unsigned target) {
    int rem = (int)(target - now32());
    while (rem > 80) { __builtin_amdgcn_s_sleep(1); rem -= 64; }
    while (rem > 0) { asm volatile("s_nop 7" ::: "memory"); rem -= 16; }
}

DI unsigned rt32() { unsigned long long t; asm volatile("s_memrealtime %0\n\ts_waitcnt lgkmcnt(0)" : "=s"(t) :: "memory"); return (unsigned)t; }
DI unsigned approach_rt(unsigned target, int lead) {
    int rem;
    for (;;) {
        rem = (int)(target - rt32());
        if (rem <= lead + 4) break;
        int n = (rem - lead) >> 2;
        while (n-- > 0) __builtin_amdgcn_s_sleep(1);
    }
    return now32() + (unsigned)(rem * 23 - 110);
}
DI void clock_ref(unsigned& re, unsigned& me) {
    unsigned long long m0, m1, r[12];
    asm volatile("s_memtime %0\n\t"
        "s_memrealtime %1\n\ts_nop 1\n\ts_memrealtime %2\n\ts_nop 1\n\ts_memrealtime %3\n\ts_nop 1\n\ts_memrealtime %4\n\ts_nop 1\n\t"
        "s_memrealtime %5\n\ts_nop 1\n\ts_memrealtime %6\n\ts_nop 1\n\ts_memrealtime %7\n\ts_nop 1\n\ts_memrealtime %8\n\ts_nop 1\n\t"
        "s_memrealtime %9\n\ts_nop 1\n\ts_memrealtime %10\n\ts_nop 1\n\ts_memrealtime %11\n\ts_nop 1\n\ts_memrealtime %12\n\ts_nop 1\n\t"
        "s_memtime %13\n\ts_waitcnt lgkmcnt(0)"
        : "=&s"(m0), "=&s"(r[0]), "=&s"(r[1]), "=&s"(r[2]), "=&s"(r[3]), "=&s"(r[4]), "=&s"(r[5]), "=&s"(r[6]), "=&s"(r[7]), "=&s"(r[8]), "=&s"(r[9]), "=&s"(r[10]), "=&s"(r[11]), "=&s"(m1) :: "memory");
    unsigned cnt = 0;
#pragma unroll
    for (int i = 1; i < 12; ++i) cnt += ((unsigned)r[i] == (unsigned)r[0]) ? 1u : 0u;
    re = (unsigned)r[0] + 4u;
    me = (unsigned)m0 + ((((unsigned)m1 - (unsigned)m0) * (2u * cnt + 1u) * 43u) >> 10);
}
DI void sleep_to(unsigned tc) {
    for (;;) {
        const int rem = (int)(tc - now32());
        if (rem <= 0) return;
        if (rem > 260) { int n = (rem - 160) / 96; while (n-- > 0) __builtin_amdgcn_s_sleep(1); }
    }
}
DI void spin_to(unsigned tc) { while ((int)(tc - now32()) > 0) {} }
DI void wait_rt(unsigned target) { spin_to(approach_rt(target, 10)); }
template <int NP, bool CTRL>
DI unsigned gather_m(__amdgpu_buffer_rsrc_t rs, unsigned sb, unsigned char* ldsm, int p0, int lane, int skipline = -1) {
    const unsigned o0 = p0 * 1024 + lane * 16;
    for (unsigned spin = 0;; ++spin) {
        asm volatile("" ::: "memory");
        v4u a[NP]; v2u c = {0, 0};
        bool sk[NP];
#pragma unroll
        for (int k = 0; k < NP; ++k) { sk[k] = (int)((o0 + k * 1024) >> 7) == skipline; a[k] = ld_sc1(rs, (o0 + k * 1024) ^ (sk[k] ? 128u : 0u), sb); }
        if (CTRL) c = ld2_sc1(rs, 7168u, sb);
#pragma unroll
        for (int k = 0; k < NP; ++k) *(v4u*)(ldsm + (sk[k] ? 7424u + (lane & 7) * 16 : o0 + k * 1024)) = a[k];
        if (CTRL && lane == 0) *(v2u*)(ldsm + 7168) = c;
        unsigned t;
        if (NP == 2) t = max(vmax4(0, a[0]), vmax4(0, a[1])); else t = vmax4(0, a[0]);
        if (CTRL) t = umax3(t, c.x, c.y);
        if (__all(t != SENT) || spin > SPIN_MAX) return spin;
        __builtin_amdgcn_s_sleep(1);
    }
}
template <int NP, bool CTRL>
DI unsigned gather_fast(__amdgpu_buffer_rsrc_t rs, unsigned sb, unsigned char* ldsm, const unsigned (&vo)[2], const unsigned (&lo)[2], int p0, int lane, int skipline) {
    asm volatile("; gather %0 %1" :: "i"(NP), "i"((int)CTRL) : "memory");
    v4u a0 = ld_sc1(rs, vo[0], sb), a1; v2u c;
    if (NP == 2) a1 = ld_sc1(rs, vo[1], sb);
    if (CTRL) c = ld2_sc1(rs, 7168u, sb);
    __builtin_amdgcn_sched_barrier(0);
    *(v4u*)(ldsm + lo[0]) = a0;
    unsigned t = vmax4(0, a0);
    if (NP == 2) { *(v4u*)(ldsm + lo[1]) = a1; t = vmax4(t, a1); }
    if (CTRL) { *(v2u*)(ldsm + lo[1]) = c; t = umax3(t, c.x, c.y); }
    if (__builtin_expect(__all(t != SENT), 1)) return 0;
    gather_m<NP, CTRL>(rs, sb, ldsm, p0, lane, skipline);
    return 1;
}
#define GADDR(vo, lo, p0, np, skl) { _Pragma("unroll") for (int k_ = 0; k_ < 2; ++k_) { const unsigned o_ = ((p0) + k_) * 1024 + lane * 16; const bool sk_ = (int)(o_ >> 7) == (skl); \
        vo[k_] = o_ ^ (sk_ ? 128u : 0u); lo[k_] = sk_ ? 7424u + (lane & 7) * 16 : o_; } if ((np) == 1) lo[1] = lane == 0 ? 7168u : 7552u + (lane & 7) * 8; }
#define LOADP(mf) v4u mf[7]; _Pragma("unroll") for (int q_ = 0; q_ < 7; ++q_) mf[q_] = *(const v4u*)(ldsm + q_ * 1024 + pfrag_off); __builtin_amdgcn_sched_barrier(0);
DI unsigned reduce8(const float* rd, int lane) {
    const float* q = rd + (lane >> 3) * 100 + (lane & 7) * 12;
    f4 a = *(const f4*)q, b = *(const f4*)(q + 4);
    a += b;
    float v = (a[0] + a[1]) + (a[2] + a[3]);
    float o = dpp_x1(v);
    unsigned r = (lane & 1) ? pk2(o, v) : pk2(v, o);
    return min(r, 0xFFFFFFFEu);
}
#define READCTRL() { SAVER1 v2u c_ = *(const v2u*)(ldsm + 7168); R2 = __builtin_amdgcn_readfirstlane(c_.x); R1n = __builtin_amdgcn_readfirstlane(c_.y); }

#define SAVER1
typedef short s4 __attribute__((ext_vector_type(4)));
DI f4 mfma16(v2u a, v2u b, f4 c) { return __builtin_amdgcn_mfma_f32_16x16x16bf16_1k(__builtin_bit_cast(s4, a), __builtin_bit_cast(s4, b), c, 0, 0, 0); }
DI float vmax(float a, float b) { float r; asm("s_nop 1\n\tv_max_f32 %0, %1, %2" : "=v"(r) : "v"(a), "v"(b)); return r; }
DI float vmax3(float a, float b, float c) { float r; asm("v_max3_f32 %0, %1, %2, %3" : "=v"(r) : "v"(a), "v"(b), "v"(c)); return r; }
DI float vmax3_mfma(float a, float b, float c) { float r; asm("s_nop 3\n\tv_max3_f32 %0, %1, %2, %3" : "=v"(r) : "v"(a), "v"(b), "v"(c)); return r; }
DI float rmax16(float x) { auto r = __builtin_amdgcn_permlane16_swap(asu(x), asu(x), false, false); unsigned a = r[0], b = r[1]; return vmax(asf(a), asf(b)); }
DI float rmax32(float x) { auto r = __builtin_amdgcn_permlane32_swap(asu(x), asu(x), false, false); unsigned a = r[0], b = r[1]; return vmax(asf(a), asf(b)); }
constexpr int HS = 1408;
constexpr int ST_OFF = 16 * HS;
constexpr int ZL_OFF = ST_OFF + 1024;
constexpr int L_M = 0, L_RED = 8192, L_RED2 = 12800, L_GM = 16128, L_VP = 17152, L_FL = 19200, L_UF = 20480;

DI unsigned gather_ctrl(__amdgpu_buffer_rsrc_t rs, unsigned sb, unsigned char* ldsm, int lane) {
    for (unsigned spin = 0;; ++spin) {
        asm volatile("" ::: "memory");
        v2u c = ld2_sc1(rs, 7168u, sb);
        if (lane == 0) *(v2u*)(ldsm + 7168) = c;
        if (__all(max(c.x, c.y) != SENT) || spin > SPIN_MAX) return spin;
        __builtin_amdgcn_s_sleep(1);
    }
}
#define REREF 0
#define RSLEEP 0
#define ASLEEP 2
#define HSLEEP 2
#define SB0 __builtin_amdgcn_sched_barrier(0);

DI void glds16(const unsigned char* gsrc, unsigned lds_dst) {
    unsigned keep;
    asm volatile("s_mov_b32 %0, m0\n\ts_mov_b32 m0, %2\n\ts_nop 0\n\tglobal_load_lds_dwordx4 %1, off sc1\n\ts_mov_b32 m0, %0" : "=&s"(keep) : "v"(gsrc), "s"(lds_dst) : "memory");
}
DI void glds16x4(const unsigned char* gsrc, unsigned lds_dst) {
    unsigned keep;
    asm volatile("s_mov_b32 %0, m0\n\ts_mov_b32 m0, %2\n\ts_nop 0\n\t"
                 "global_load_lds_dwordx4 %1, off sc1\n\tglobal_load_lds_dwordx4 %1, off offset:1024 sc1\n\t"
                 "global_load_lds_dwordx4 %1, off offset:2048 sc1\n\tglobal_load_lds_dwordx4 %1, off offset:3072 sc1\n\t"
                 "s_mov_b32 m0, %0" : "=&s"(keep) : "v"(gsrc), "s"(lds_dst) : "memory");
}
DI void glds4(const unsigned char* gsrc, unsigned lds_dst) {
    unsigned keep;
    asm volatile("s_mov_b32 %0, m0\n\ts_mov_b32 m0, %2\n\ts_nop 0\n\tglobal_load_lds_dword %1, off sc1\n\ts_mov_b32 m0, %0" : "=&s"(keep) : "v"(gsrc), "s"(lds_dst) : "memory");
}
DI unsigned ldflag(__amdgpu_buffer_rsrc_t rs, unsigned off) { return __builtin_amdgcn_raw_buffer_load_b32(rs, (unsigned)CTRL_OFF + off, 0, 16); }
DI unsigned wait_flags(__amdgpu_buffer_rsrc_t rs, unsigned off, unsigned need, int slp) {
    unsigned v;
    for (unsigned spin = 0;; ++spin) {
        asm volatile("" ::: "memory");
        v = ldflag(rs, off);
        if (__all(v >= need) || spin > 4 * SPIN_MAX) break;
        for (int i = 0; i < slp; ++i) __builtin_amdgcn_s_sleep(4);
    }
#pragma unroll
    for (int o = 32; o >= 1; o >>= 1) v = min(v, (unsigned)__shfl_xor((int)v, o));
    return __builtin_amdgcn_readfirstlane(v);
}

#define LDSV(off) (*(volatile __attribute__((address_space(3))) unsigned*)((__attribute__((address_space(3))) unsigned char*)lds + (off)))
DI void rec_role(unsigned char* ws, unsigned char* lds, const int role, int dS, int dV, int dVm) {
    const int tid = threadIdx.x, lane = tid & 63, w = __builtin_amdgcn_readfirstlane(tid >> 6);
    const int rho = lane & 15, g = lane >> 4;
    __amdgpu_buffer_rsrc_t rs = mkrs(ws, 0x7fffffff);
    unsigned char* ldsm = lds + L_M;
    float* red = (float*)(lds + L_RED);
    float* red2 = (float*)(lds + L_RED2);
    if (role == 112) {
        unsigned best = 0xffffffffu;
        for (int r = 0; r < 10; ++r) {
            const v4u z = {SENT, SENT, SENT, SENT};
            const unsigned off = (unsigned)((w >> 1) + 4 * (r & 1)) * 262144u + ((r >> 1) & 3) * 65536u + 7680u + ((r >> 3) + 2 * (r & 1)) * 128u + (w & 1) * 8192u;
            const unsigned t0 = now32();
            if (lane < 8) st_sc1(z, rs, off + lane * 16, 0);
            asm volatile("s_waitcnt vmcnt(0)" ::: "memory");
            const unsigned dt = now32() - t0;
            if (r > 0) best = min(best, dt);
        }
        if (lane == 0) ((unsigned*)lds)[w] = best;
        __syncthreads();
        if (tid == 0) {
            const unsigned* bl = (const unsigned*)lds;
            const unsigned b0 = (bl[0] + bl[2]) + (bl[4] + bl[6]), b1 = (bl[1] + bl[3]) + (bl[5] + bl[7]);
            __builtin_amdgcn_raw_buffer_store_b32(b1 < b0 ? 2u : 1u, rs, (unsigned)CTRL_OFF + C_POL, 0, 16);
        }
        __syncthreads();
    }
#define POLINIT() const unsigned polo = (wait_flags(rs, C_POL, 1, 1) - 1u) * 8192u; const unsigned myc = (unsigned)(role & 3) * 65536u + polo;
    if (role < 112) { for (int i = 0; i < RSLEEP; ++i) __builtin_amdgcn_s_sleep(16); }
    unsigned nfail = 0, nfail2 = 0;
    unsigned long long* dbg = (unsigned long long*)(ws + CTRL_OFF + C_DBG);
#define STAMP(cond, idx)
#define TL(idx)
#define GT(idx, ref)
#define BT(i) {}
    constexpr unsigned TOFF = 70, TOFF2 = 30;
#define CLKUPD() { unsigned r1_, m1_; clock_ref(r1_, m1_); const unsigned dr = r1_ - r0, dm = m1_ - m0; \
        if (dr > 60u && dr < TA + TB + 32u) { const int err = (int)(dm * 256u) - (int)(cq * dr); cq = (unsigned)min(max((int)cq + (err >> 8), 4096), 6400); } r0 = r1_; m0 = m1_; }
#define WAITR1() { unsigned tg_ = m0 - TOFF + (unsigned)(((int)(R1n - r0) * (int)cq) >> 8); \
        if (REREF && (int)(tg_ - now32()) > 700) { sleep_to(tg_ - 520u); unsigned ra_, ma_; clock_ref(ra_, ma_); tg_ = ma_ - TOFF + (unsigned)(((int)(R1n - ra_) * (int)cq) >> 8); } \
        sleep_to(tg_); }
    const unsigned pfrag_off = (rho >> 3) * 512 + g * 128 + (rho & 7) * 16;
    const unsigned TA = dS & 255, TB = dV & 255, TE = dVm & 255, TLATE = (dS >> 8) & 255, XUP = (dV >> 8) & 255, XDN = (dVm >> 8) & 255, X0 = (dVm >> 16) & 255;
    unsigned R2 = 0, R1n = 0;
    const bool is_mx = role >= 112;
    const bool is_v = !is_mx && (((role >> 2) & 1) == 0);
    const int j = (role >> 3) * 4 + (role & 3);
    const int h = role / 7, p = role % 7;
    const int hp = (lane >> 3) & 1, i8 = lane & 7;
    const v4u SENT4 = {SENT, SENT, SENT, SENT};

    if (is_mx) {
        const int hx = role - 112;
        POLINIT()
        if (w >= 4) {
            const unsigned orst = comm_off(1, hx * HS + 896 + (lane & 31) * 16, 0) + (lane >> 5) * 65536u + polo;
            const unsigned orst2 = comm_off(1, ST_OFF + (hx >> 1) * 128 + (hx & 1) * 64 + (lane & 1) * 16, 0) + ((lane >> 1) & 3) * 65536u + polo;
            const unsigned gfl2 = C_GU + 128u * (unsigned)min((14 * hx) / 4 + (lane & 7), (14 * hx + 13) / 4);
            unsigned kn = 0, gvo[2], glo[2];
            GADDR(gvo, glo, 2 * (w - 4), (w < 7 ? 2 : 1), -1)
            unsigned r0 = 0, m0 = 0, cq = 5900u;
            if (w == 7) clock_ref(r0, m0);
            if (w == 5 && lane == 0) LDSV(L_UF) = 0u;
            for (int step = 0; step < NSEG; ++step) {
                const unsigned sbr = comm_step_base(step, 0) + myc;
                if (step > 0 && w == 7) WAITR1()
                bar();
                if (w < 7) nfail += gather_fast<2, false>(rs, sbr, ldsm, gvo, glo, 2 * (w - 4), lane, -1);
                else nfail += gather_fast<1, true>(rs, sbr, ldsm, gvo, glo, 6, lane, -1);
                bar();
                READCTRL()
                bar();
                if (w == 4 && step >= 4) {
                    __builtin_amdgcn_s_sleep(4);
                    const unsigned sbz = comm_step_base(step + 4, 0);
                    st_sc1(SENT4, rs, orst, sbz); st_sc1(SENT4, rs, orst + 131072u, sbz);
                    if (lane < 8) st_sc1(SENT4, rs, orst2, sbz);
                }
                if (w == 7) CLKUPD()
                if (w == 5 && kn < 64u) {
                    __builtin_amdgcn_s_sleep(4);
                    kn = wait_flags(rs, gfl2, 0, 1);
                    if (lane == 0) LDSV(L_UF) = kn;
                }
            }
        } else {
            __builtin_amdgcn_s_setprio(2);
            const int tau = w & 1, kh = w >> 1;
            v4u wa[8], wb[8];
            const unsigned gbase = (unsigned)(G_OFF + ((size_t)hx * 2 + tau) * 14 * 1024 + kh * 8192) + lane * 16;
            const unsigned g67 = gbase + (kh ? 4096u : 6144u);
#define GLOAD(wt, sb_) { _Pragma("unroll") for (int ks = 0; ks < 6; ++ks) wt[ks] = ld_sc1(rs, gbase + ks * 1024, sb_); \
                     wt[6] = ld_sc1(rs, g67, sb_); wt[7] = ld_sc1(rs, g67 + 1024, sb_); }
            const unsigned gfl = C_GU + 128u * (unsigned)min((14 * hx) / 4 + (lane & 7), (14 * hx + 13) / 4);
            unsigned known = wait_flags(rs, gfl, 2, 1);
            GLOAD(wa, 0)
            GLOAD(wb, 458752)
            float* sc = red;
            const int tq = lane & 7, ir = lane >> 3;
            const unsigned oes = comm_off(1, hx * HS + 896 + lane * 8, 0) + w * 65536u + polo;
            const unsigned oss = comm_off(1, ST_OFF + (hx >> 1) * 128 + (hx & 1) * 64 + ir * 4, 0) + w * 65536u + polo;
#define MXGT
#define MXSTEP(wt, step) { \
                const bool last = ((step) == NSEG - 1); \
                const unsigned sbs = comm_step_base(step, 0); \
                __builtin_amdgcn_s_barrier(); \
                bar(); \
                f4 aE = {0, 0, 0, 0}, aO = {0, 0, 0, 0}; \
                v4u mf[4]; \
                _Pragma("unroll") for (int q = 0; q < 3; ++q) mf[q] = *(const v4u*)(ldsm + (4 * kh + q) * 1024 + pfrag_off); \
                mf[3] = *(const v4u*)(ldsm + 3 * 1024 + pfrag_off); \
                SB0 \
                _Pragma("unroll") for (int q = 0; q < 3; ++q) { aE = mfma(wt[2 * q], mf[q], aE); aO = mfma(wt[2 * q + 1], mf[q], aO); } \
                if (kh == 0) { aE = mfma(wt[6], mf[3], aE); aO = mfma(wt[7], mf[3], aO); } \
                f4 xs = hp ? aO : aE; \
                *(f4*)(sc + ((2 * kh + hp) * 8 + i8) * 36 + 16 * tau + 4 * g) = xs; \
                bar(); \
                const float* sp = sc + ir * 36 + 4 * tq; \
                f4 a0 = (*(const f4*)(sp) + *(const f4*)(sp + 288)) + (*(const f4*)(sp + 576) + *(const f4*)(sp + 864)); \
                float mx; \
                asm("v_max3_f32 %0, %1, %2, %3\n\tv_max_f32 %0, %0, %4\n\ts_nop 1\n\t" \
                    "v_max_f32_dpp %0, %0, %0 quad_perm:[1,0,3,2] row_mask:0xf bank_mask:0xf\n\ts_nop 1\n\t" \
                    "v_max_f32_dpp %0, %0, %0 quad_perm:[2,3,0,1] row_mask:0xf bank_mask:0xf\n\ts_nop 1\n\t" \
                    "v_max_f32_dpp %0, %0, %0 row_half_mirror row_mask:0xf bank_mask:0xf" \
                    : "=&v"(mx) : "v"(a0[0]), "v"(a0[1]), "v"(a0[2]), "v"(a0[3])); \
                if (!last) { \
                    unsigned e0 = pk2(__builtin_amdgcn_exp2f(a0[0] - mx), __builtin_amdgcn_exp2f(a0[1] - mx)); \
                    unsigned e1 = pk2(__builtin_amdgcn_exp2f(a0[2] - mx), __builtin_amdgcn_exp2f(a0[3] - mx)); \
                    v2u eo; eo.x = min(e0, 0xFFFFFFFEu); eo.y = min(e1, 0xFFFFFFFEu); \
                    st2_sc1(eo, rs, oes, sbs); \
                    if (tq == 1) __builtin_amdgcn_raw_buffer_store_b32(min(asu(mx), 0xFFFFFFFEu), rs, oss, sbs, 16); \
                    MXGT \
                } \
                { const int sn = min((step) + 2, NSEG - 1); \
                  known = max(known, (unsigned)__builtin_amdgcn_readfirstlane(LDSV(L_UF))); \
                  if (known < (unsigned)sn + 1) known = wait_flags(rs, gfl, sn + 1, 1); \
                  const unsigned sb = __builtin_amdgcn_readfirstlane(sn * 458752); \
                  GLOAD(wt, sb) } }
            for (int step = 0; step < NSEG; step += 2) {
                MXSTEP(wa, step)
                MXSTEP(wb, step + 1)
            }
        }
    } else if (w >= 4) {
        const int wv = w - 4;
        v4u wtv[2][14];
        if (is_v) {
            const unsigned char* src = ws + BS_OFF + ((size_t)j * 8 + 2 * wv) * 14 * 1024;
#pragma unroll
            for (int t = 0; t < 2; ++t)
#pragma unroll
                for (int f = 0; f < 14; ++f) wtv[t][f] = *(const v4u*)(src + (t * 14 + f) * 1024 + lane * 16);
        } else {
#pragma unroll
            for (int t = 0; t < 2; ++t)
#pragma unroll
                for (int f = 0; f < 14; ++f) wtv[t][f] = v4u{0, 0, 0, 0};
        }
        POLINIT()
        __builtin_amdgcn_s_waitcnt(0x0F70);
        const bool cond = is_v && j == 0;
        const bool slow = w >= 6;
        const unsigned ufl = C_GU + 128u * (56 + j);
        const unsigned char* usrc = ws + U_OFF + ((size_t)j * 8 + 4 * (wv & 1)) * 1024 + lane * 16;
        const unsigned udst = (unsigned)(size_t)(__attribute__((address_space(3))) unsigned char*)lds + L_UF + (wv & 1) * 4096;
#define UDMA(sg) { const unsigned char* p_ = usrc + (size_t)(sg) * 458752; const unsigned d_ = __builtin_amdgcn_readfirstlane(udst + ((sg) & 3) * 8192); \
            glds16x4(p_, d_); }
#define VPRIME() { \
                f4 e0 = {0, 0, 0, 0}, o0 = {0, 0, 0, 0}, e1 = {0, 0, 0, 0}, o1 = {0, 0, 0, 0}; \
                _Pragma("unroll") for (int q = 0; q < 7; ++q) { \
                    e0 = mfma(mf[q], wtv[0][2 * q], e0); o0 = mfma(mf[q], wtv[0][2 * q + 1], o0); \
                    e1 = mfma(mf[q], wtv[1][2 * q], e1); o1 = mfma(mf[q], wtv[1][2 * q + 1], o1); } \
                f4 cs; \
                _Pragma("unroll") for (int r = 0; r < 4; ++r) { \
                    auto sw = __builtin_amdgcn_permlane32_swap(asu(o0[r]), asu(e1[r]), false, false); \
                    unsigned d0 = sw[0], d1 = sw[1]; \
                    cs[r] = g < 2 ? e0[r] + asf(d1) : o1[r] + asf(d0); } \
                v2u vp; vp.x = pk2(cs[0], cs[1]); vp.y = pk2(cs[2], cs[3]); \
                *(v2u*)(lds + L_VP + wv * 512 + (2 * (g >> 1) + hp) * 128 + i8 * 16 + (g & 1) * 8) = vp; }
#define BYPROD(st_) { \
            if (w == 7) { v4u gmv = *(const v4u*)(lds + L_GM + lane * 16); \
                st_sc1(gmv, rs, (unsigned)(GM_OFF + ((size_t)h * 14 + 2 * p) * 512) + lane * 16, __builtin_amdgcn_readfirstlane((st_) * 16 * 14 * 512)); } \
            if (w == 6 && is_v) { v4u v0 = *(const v4u*)(lds + L_VP + lane * 16), v1 = *(const v4u*)(lds + L_VP + 1024 + lane * 16); \
                const unsigned sbv = __builtin_amdgcn_readfirstlane((st_) * 56 * 4 * 512); \
                st_sc1(v0, rs, (unsigned)(VP_OFF + (size_t)j * 2048) + lane * 16, sbv); \
                st_sc1(v1, rs, (unsigned)(VP_OFF + (size_t)j * 2048 + 1024) + lane * 16, sbv); } }
#define RESETS() if (w == 7 && step >= 4) { \
                    if (dorst) st_sc1(SENT4, rs, orst, comm_step_base(step + 4, 0)); \
                    if (cond && lane < 4) st_sc1(SENT4, rs, polo + 7168u + lane * 65536u, comm_step_base(step + 4, 0)); }
        const unsigned orst = (lane < 32 ? comm_off(1, h * HS + p * 128 + (lane & 7) * 16, 0) : (unsigned)(j * 128 + (lane & 7) * 16)) + ((lane >> 3) & 3) * 65536u + polo;
        const bool dorst = lane < 32 || is_v;
#define CSTORES() { \
                    if (cond && lane < 4) st_sc1(SENT4, rs, polo + 7168u + lane * 65536u, comm_step_base(step + 4, 0)); \
                    if (dorst) st_sc1(SENT4, rs, orst, comm_step_base(step + 4, 0)); \
                    v4u gmv = *(const v4u*)(lds + L_GM + lane * 16); \
                    st_sc1(gmv, rs, (unsigned)(GM_OFF + ((size_t)h * 14 + 2 * p) * 512) + lane * 16, __builtin_amdgcn_readfirstlane(step * 16 * 14 * 512)); }
        if (w == 7) {
            unsigned known = 0;
            unsigned r0, m0, cq = 5900u; clock_ref(r0, m0);
            if (is_v) { known = wait_flags(rs, ufl, 2, 1); UDMA(0) UDMA(1) asm volatile("s_waitcnt vmcnt(0)" ::: "memory"); }
            for (int step = 0; step < NSEG; ++step) {
                if (step > 0) WAITR1()
                bar();
                BT(0)
                CLKUPD()
                if (lane == 0) *(v4u*)(lds + L_FL + 512) = v4u{r0, m0 - TOFF2, cq, 0u};
                bar();
                __builtin_amdgcn_s_sleep(HSLEEP);
                READCTRL()
                LOADP(mf)
                bar();
                if (is_v) VPRIME()
                bar();
                asm volatile("s_waitcnt vmcnt(0)" ::: "memory");
                if (is_v && step + 3 < NSEG) UDMA(step + 2)
                if (is_v) bar();
                else CSTORES()
            }
            bar();
            asm volatile("s_waitcnt vmcnt(0)" ::: "memory");
            __builtin_amdgcn_s_barrier();
        } else if (w == 6) {
            unsigned known = 0;
            if (is_v) { known = wait_flags(rs, ufl, 2, 1); UDMA(0) UDMA(1) asm volatile("s_waitcnt vmcnt(0)" ::: "memory"); }
            const bool reporter = (role == 4);
            const unsigned char* fsrc = ws + CTRL_OFF + ufl;
            const unsigned fdst = __builtin_amdgcn_readfirstlane((unsigned)(size_t)(__attribute__((address_space(3))) unsigned char*)lds + L_FL);
            if (lane == 0) LDSV(L_FL) = 0u;
#define VPSTORE(st_) { v4u v0 = *(const v4u*)(lds + L_VP + lane * 16), v1 = *(const v4u*)(lds + L_VP + 1024 + lane * 16); \
                const unsigned sbv = __builtin_amdgcn_readfirstlane((st_) * 56 * 4 * 512); \
                st_sc1(v0, rs, (unsigned)(VP_OFF + (size_t)j * 2048) + lane * 16, sbv); \
                st_sc1(v1, rs, (unsigned)(VP_OFF + (size_t)j * 2048 + 1024) + lane * 16, sbv); }
            for (int step = 0; step < NSEG; ++step) {
                bar();
                if (is_v) {
                    asm volatile("s_waitcnt vmcnt(2)" ::: "memory");
                    __builtin_amdgcn_s_sleep(ASLEEP);
                    if (step + 3 < NSEG) {
                        if (known < (unsigned)step + 3) known = max(known, (unsigned)__builtin_amdgcn_readfirstlane(LDSV(L_FL)));
                        if (known < (unsigned)step + 3) known = wait_flags(rs, ufl, step + 3, 1);
                        UDMA(step + 2)
                    }
                    if (step > 0) VPSTORE(step - 1)
                    if (known < (unsigned)step + 6 && known < 64u && lane == 0) glds4(fsrc, fdst);
                }
                bar();
                __builtin_amdgcn_s_sleep(HSLEEP);
                LOADP(mf)
                bar();
                if (is_v) VPRIME()
                bar();
                if (reporter && lane == 0 && step >= 3) __builtin_amdgcn_raw_buffer_store_b32((unsigned)(step - 2), rs, (unsigned)CTRL_OFF + C_REC, 0, 16);
                if (is_v) { CSTORES() bar(); }
            }
            bar();
            if (is_v) VPSTORE(NSEG - 1)
            asm volatile("s_waitcnt vmcnt(0)" ::: "memory");
            __builtin_amdgcn_s_barrier();
        } else {
            unsigned extra = X0, calm = 0, ont = 0, gvo[2] = {0, 0}, glo[2] = {0, 0};
            if (cond && w == 5) {
                wait_flags(rs, ufl, 2, 1);
                unsigned t = rt32() + 120u;
                v2u c; c.x = min(t, 0xFFFFFFFEu); c.y = min(t + TB, 0xFFFFFFFEu);
                if (lane < KCOPY) st2_sc1(c, rs, polo + 7168u + lane * 65536u, 0);
                if (lane == 0) *(v2u*)(ldsm + 7168) = c;
            }
            const unsigned opub = polo + j * 128 + (lane >> 1) * 4 + (lane & 1) * 65536u;
            const unsigned opubS = comm_off(1, h * HS + p * 128 + (lane >> 1) * 4, 0) + (lane & 1) * 65536u + polo;
            for (int step = 0; step < NSEG; ++step) {
                const bool last = (step == NSEG - 1);
                const unsigned sbs = comm_step_base(step, 0);
                const unsigned sbr = sbs + myc;
                const unsigned sbn = comm_step_base(step + 1, 0);
                const int skl = (is_v && step > 0) ? j : -1;
                if (step < 2) GADDR(gvo, glo, w + 1, 1, skl)
                bar();
                if (w == 4 || cond) nfail += gather_fast<1, false>(rs, sbr, ldsm, gvo, glo, w + 1, lane, skl);
                else nfail += gather_fast<1, true>(rs, sbr, ldsm, gvo, glo, 6, lane, skl);
                bar();
                if (w == 5) BT(1)
                READCTRL()
                __builtin_amdgcn_s_sleep(HSLEEP);
                LOADP(mf)
                if (w == 4) __builtin_amdgcn_s_setprio(3);
                bar();
                if (w == 5) BT(2)
                if (w == 4) {
                    if (!last) {
                        unsigned o = reduce8(red, lane);
                        __builtin_amdgcn_raw_buffer_store_b32(o, rs, opubS, sbs, 16);
                        __builtin_amdgcn_raw_buffer_store_b32(o, rs, opubS + 131072u, sbs, 16);
                        BT(5)
                        if ((lane & 1) == 0) *(unsigned*)(lds + L_FL + 640 + (lane >> 1) * 4) = o;
                    }
                    __builtin_amdgcn_s_setprio(0);
                }
                if (is_v) {
                    VPRIME()
                    bar();
                    if (w == 5) BT(3)
                    if (w == 4) __builtin_amdgcn_s_setprio(3);
                    bar();
                    if (w == 5 && !cond) BT(4)
                    if (!last) {
                        if (w == 4) {
                            unsigned o = reduce8(red2, lane);
                            __builtin_amdgcn_raw_buffer_store_b32(o, rs, opub, sbn, 16);
                            __builtin_amdgcn_raw_buffer_store_b32(o, rs, opub + 131072u, sbn, 16);
                            BT(6)
                            if ((lane & 1) == 0) *(unsigned*)(ldsm + j * 128 + (lane >> 1) * 4) = o;
                            __builtin_amdgcn_s_setprio(0);
                        }
                        if (cond && w == 5) {
                            const unsigned tp = rt32();
                            unsigned base = R1n;
                            if ((int)(tp + 4u - R1n) > 0) { base = tp + TLATE; if (ont) extra = min(extra + XUP, 10u); ont = 0; calm = 0; }
                            else { ont = 1; if (++calm >= XDN) { calm = 0; extra = extra ? extra - 1u : 0u; } }
                            v2u c; c.x = min(base + TA + extra, 0xFFFFFFFEu); c.y = min(base + TA + TB + 2u * extra, 0xFFFFFFFEu);
                            if (lane < KCOPY) st2_sc1(c, rs, polo + 7168u + lane * 65536u, sbn);
                            if (lane == 0) *(v2u*)(ldsm + 7168) = c;
                        }
                    }
                } else bar();
            }
            bar();
            __builtin_amdgcn_s_barrier();
            if (w == 4 && lane == 0) __builtin_amdgcn_raw_buffer_store_b32(1u, rs, (unsigned)CTRL_OFF + C_DN2 + role * 4, 0, 16);
        }
    } else {
        __builtin_amdgcn_s_setprio(2);
        const int b = 2 * p + (w >> 1), t = w & 1;
        v4u wt[14];
        {
            const unsigned char* src = ws + AS_OFF + ((size_t)(h * 14 + b) * 2 + t) * 14 * 1024;
#pragma unroll
            for (int ks = 0; ks < 14; ++ks) wt[ks] = *(const v4u*)(src + ks * 1024 + lane * 16);
        }
        POLINIT()
        __builtin_amdgcn_s_waitcnt(0x0F70);
        unsigned op[2][2], oe[2], ost, gvo[2] = {0, 0}, glo[2] = {0, 0};
        v4u pf[2][2], ef[2]; unsigned stt;
#pragma unroll
        for (int a = 0; a < 2; ++a) {
            const int hd = 4 * w + 2 * a + hp;
#pragma unroll
            for (int u = 0; u < 2; ++u) {
                const int q = 4 * u + g;
                const bool z = (q == 7) || (hd == h && q == p);
                op[a][u] = z ? 0x80000000u : comm_off(1, hd * HS + q * 128 + i8 * 16, 0);
            }
            oe[a] = comm_off(1, hd * HS + 896 + i8 * 64 + g * 16, 0);
        }
        const int hl = 4 * w + 2 * (g >> 1) + hp;
        ost = comm_off(1, ST_OFF + (hl >> 1) * 128 + (hl & 1) * 64 + i8 * 4, 0);
        const bool ownhead = (hl == h);
        v4u selLo = {0, 0, 0, 0}, selHi = {0, 0, 0, 0}, oneLo, oneHi;
        {
            const unsigned one = 0x3F80u << (16 * (rho & 1));
            const int wd = (rho & 7) >> 1;
            v4u sv; sv.x = wd == 0 ? one : 0; sv.y = wd == 1 ? one : 0; sv.z = wd == 2 ? one : 0; sv.w = wd == 3 ? one : 0;
            if (rho < 8) selLo = sv; else selHi = sv;
            const unsigned o2 = 0x3F803F80u;
            oneLo = rho < 8 ? v4u{o2, o2, o2, o2} : v4u{0, 0, 0, 0};
            oneHi = rho < 8 ? v4u{0, 0, 0, 0} : v4u{o2, o2, o2, o2};
        }
        for (int step = 0; step < NSEG; ++step) {
            const bool last = (step == NSEG - 1);
            const unsigned sbs = comm_step_base(step, 0);
            const unsigned sbr = sbs + myc;
            STAMP(step == 1, 0); STAMP(step == 63, 1);
            if (step < 2) GADDR(gvo, glo, (w == 0 ? 0 : w + 1), 2, ((is_v && step > 0) ? j : -1))
            bar();
            TL(0)
            const int skl = (is_v && step > 0) ? j : -1;
            if (w == 0) nfail += gather_fast<2, false>(rs, sbr, ldsm, gvo, glo, 0, lane, skl);
            else nfail += gather_fast<1, false>(rs, sbr, ldsm, gvo, glo, w + 1, lane, skl);
            bar();
            TL(1)
            f4 aE = {0, 0, 0, 0}, aO = {0, 0, 0, 0};
            v4u mf[7];
#pragma unroll
            for (int q_ = 0; q_ < 7; ++q_) mf[q_] = *(const v4u*)(ldsm + q_ * 1024 + pfrag_off);
            v2u mb = *(const v2u*)(ldsm + b * 512 + g * 128 + i8 * 16 + t * 8);
            const unsigned cwx = *(const unsigned*)(ldsm + 7168);
            const v4u ckr = *(const v4u*)(lds + L_FL + 512);
            SB0
#pragma unroll
            for (int q = 0; q < 7; ++q) { aE = mfma(wt[2 * q], mf[q], aE); aO = mfma(wt[2 * q + 1], mf[q], aO); }
            f4 xs = hp ? aO : aE;
            v2u gh; gh.x = pk2(xs[0], xs[1]); gh.y = pk2(xs[2], xs[3]);
            f4 sp = mfma16(gh, mb, f4{0, 0, 0, 0});
            if (rho < 8) {
                float* rq = red + rho * 100 + (4 * (g & 1)) * 12 + 2 * w + (g >> 1);
                rq[0] = sp[0]; rq[12] = sp[1]; rq[24] = sp[2]; rq[36] = sp[3];
            }
            SB0
            TL(7)
            bar();
#define GMSTAGE() { f4 fs; _Pragma("unroll") for (int r = 0; r < 4; ++r) fs[r] = xs[r] + dpp_xor8(xs[r]); \
                v2u gm; gm.x = pk2(fs[0], fs[1]); gm.y = pk2(fs[2], fs[3]); \
                if (rho < 8) *(v2u*)(lds + L_GM + (w >> 1) * 512 + g * 128 + rho * 16 + t * 8) = gm; }
            if (!is_v) { GMSTAGE() bar(); continue; }
            if (last) { GMSTAGE() bar(); bar(); continue; }
#define E_LOADS(sb_) { stt = __builtin_amdgcn_raw_buffer_load_b32(rs, ost, sb_, 16); ef[0] = ld_sc1(rs, oe[0], sb_); ef[1] = ld_sc1(rs, oe[1], sb_); SB0 }
#define P_LOADS(sb_) { pf[0][0] = ld_sc1(rs, op[0][0], sb_); SB0 pf[1][0] = ld_sc1(rs, op[1][0], sb_); SB0 pf[0][1] = ld_sc1(rs, op[0][1], sb_); SB0 pf[1][1] = ld_sc1(rs, op[1][1], sb_); SB0 }
            const unsigned tc2 = (unsigned)__builtin_amdgcn_readfirstlane(ckr.y) + (unsigned)(((int)(__builtin_amdgcn_readfirstlane(cwx) - __builtin_amdgcn_readfirstlane(ckr.x)) * (int)__builtin_amdgcn_readfirstlane(ckr.z)) >> 8);
            spin_to(tc2 - TE * 23u);
            E_LOADS(sbr)
            asm volatile("" ::: "memory");
            spin_to(tc2);
            TL(3)
            P_LOADS(sbr)
            asm volatile("" ::: "memory");
            SB0
            if (w == 1) BT(7)
            GMSTAGE()
            v4u uf[2];
            uf[0] = *(const v4u*)(lds + L_UF + (step & 3) * 8192 + w * 2048 + lane * 16); uf[1] = *(const v4u*)(lds + L_UF + (step & 3) * 8192 + w * 2048 + 1024 + lane * 16);
            bar();
            v2u vpA, vpB;
            f4 sini = {0, 0, 0, 0};
            {
                v2u vp = *(const v2u*)(lds + L_VP + w * 512 + (2 * (g >> 1) + hp) * 128 + i8 * 16 + (g & 1) * 8);
                v2u ow = *(const v2u*)(lds + L_FL + 640 + i8 * 16 + (g & 1) * 8);
                vpA = g < 2 ? vp : v2u{0, 0};
                vpB = g < 2 ? v2u{0, 0} : vp;
                if (ownhead) { sini[0] = bflo(ow.x); sini[1] = bfhi(ow.x); sini[2] = bflo(ow.y); sini[3] = bfhi(ow.y); }
            }
            asm volatile("" ::: "memory");
            SB0
            TL(4)
            bool okall;
            auto combine = [&]() __attribute__((always_inline)) {
                f4 ls = mfma(oneLo, ef[0], f4{0, 0, 0, 0});
                ls = mfma(oneHi, ef[1], ls);
                f4 x0 = mfma(uf[0], ef[0], f4{0, 0, 0, 0});
                f4 x1 = mfma(uf[1], ef[1], f4{0, 0, 0, 0});
                unsigned t2 = vmax4(vmax4(stt, ef[0]), ef[1]);
                float lsv = ls[0];
                asm volatile("" : "+v"(t2), "+v"(lsv));
                SB0
                f4 sacc = mfma(selLo, pf[0][0], sini);
                sacc = mfma(selHi, pf[1][0], sacc);
                sacc = mfma(selLo, pf[0][1], sacc);
                sacc = mfma(selHi, pf[1][1], sacc);
                SB0
                unsigned t0 = vmax4(vmax4(t2, pf[0][0]), pf[0][1]), t1 = vmax4(vmax4(0, pf[1][0]), pf[1][1]);
                asm volatile("" : "+v"(t0), "+v"(t1));
                SB0
                const float mloc = asf(stt);
                float mx = vmax3(vmax3_mfma(sacc[0], sacc[1], sacc[2]), sacc[3], mloc);
                mx = rmax16(mx);
                float p0 = __builtin_amdgcn_exp2f(sacc[0] - mx), p1 = __builtin_amdgcn_exp2f(sacc[1] - mx), p2 = __builtin_amdgcn_exp2f(sacc[2] - mx), p3 = __builtin_amdgcn_exp2f(sacc[3] - mx);
                float f = __builtin_amdgcn_exp2f(mloc - mx);
                v2u pb; pb.x = pk2(p0, p1); pb.y = pk2(p2, p3);
                float zq = (p0 + p1) + (p2 + p3);
                SB0
                f4 cmA = mfma16(vpA, pb, f4{0, 0, 0, 0});
                f4 cmB = mfma16(vpB, pb, f4{0, 0, 0, 0});
                SB0
                float z = xsum16(zq);
                auto fs = __builtin_amdgcn_permlane32_swap(asu(f), asu(f), false, false);
                unsigned fa = fs[0], fb = fs[1];
                z += f * lsv;
                float rz = __builtin_amdgcn_rcpf(z);
                okall = __all(max(t0, t1) != SENT);
                f4 ya = x0 * asf(fa) + cmA, yb = x1 * asf(fb) + cmB;
                auto rs2 = __builtin_amdgcn_permlane32_swap(asu(rz), asu(rz), false, false);
                unsigned ra = rs2[0], rb = rs2[1];
                f4 cx = ya * asf(ra) + yb * asf(rb);
                if (hp == (g >> 1)) {
                    float* rq = red2 + i8 * 100 + (4 * (g & 1)) * 12 + 2 * w + hp;
                    rq[0] = cx[0]; rq[12] = cx[1]; rq[24] = cx[2]; rq[36] = cx[3];
                }
            };
            combine();
            if (!okall) {
                nfail2++;
                for (unsigned spin = 0; spin < SPIN_MAX && !okall; ++spin) {
                    __builtin_amdgcn_s_sleep(1);
                    asm volatile("" ::: "memory");
                    E_LOADS(sbr)
                    P_LOADS(sbr)
                    combine();
                }
            }
            TL(6)
            bar();
        }
        bar();
        __builtin_amdgcn_s_barrier();
    }
}

DI void signal_add(unsigned char* ws, unsigned off) {
    asm volatile("s_waitcnt vmcnt(0)" ::: "memory");
    __syncthreads();
    if (threadIdx.x == 0) __hip_atomic_fetch_add((unsigned*)(ws + CTRL_OFF + off), 1u, __ATOMIC_RELAXED, __HIP_MEMORY_SCOPE_AGENT);
}
DI void wg_wait(__amdgpu_buffer_rsrc_t rs, unsigned off_lane, unsigned need, int slp) {
    if (threadIdx.x < 64) wait_flags(rs, off_lane, need, slp);
    __syncthreads();
}

DI void gldsx16(const unsigned char* gsrc, unsigned lds_dst) {
    unsigned keep;
    asm volatile("s_mov_b32 %0, m0\n\ts_mov_b32 m0, %2\n\ts_nop 0\n\tglobal_load_lds_dwordx4 %1, off\n\ts_mov_b32 m0, %0" : "=&s"(keep) : "v"(gsrc), "s"(lds_dst) : "memory");
}
DI void gu_role(unsigned char* ws, unsigned char* lds, const int gw) {
    const int tid = threadIdx.x, lane = tid & 63, w8 = __builtin_amdgcn_readfirstlane(tid >> 6);
    const int w = w8 & 3, sp = w8 >> 2;
    const bool isG = gw < 56;
    const unsigned ldsb = __builtin_amdgcn_readfirstlane((unsigned)(size_t)(__attribute__((address_space(3))) unsigned char*)lds);
    const int nat0 = (((lane & 15) >> 2) & 1) * 14464 + ((4 * ((lane & 15) >> 3) + (lane & 3)) + 16 * (lane >> 4)) * 16;
    const unsigned char* xsrc = ws + XF_OFF + w8 * 1024 + lane * 16;
#define XDMA(pr_) { _Pragma("unroll") for (int i = 0; i < 7; ++i) gldsx16(xsrc + (size_t)(pr_) * 57344 + i * 8192, ldsb + ((pr_) & 1) * 57856 + (i * 8 + w8) * 1024 + ((i * 8 + w8) / 14) * 128); }
    XDMA(0)
    v4u wt[2][14];
    const unsigned char* wsrc = isG ? ws + AS_OFF + (size_t)(4 * gw + w) * 28 * 1024 : ws + BS_OFF + ((size_t)(gw - 56) * 8 + 2 * w) * 14 * 1024;
    unsigned char* xch = lds + 57856;
    v4u tmp[14];
#pragma unroll
    for (int ks = 0; ks < 14; ++ks) tmp[ks] = *(const v4u*)(wsrc + (sp * 14 + ks) * 1024 + lane * 16);
    SB0
#pragma unroll
    for (int half = 0; half < 2; ++half) {
        if (half) bar();
#pragma unroll
        for (int ks = 0; ks < 7; ++ks) *(v4u*)(xch + (w8 * 7 + ks) * 1024 + lane * 16) = tmp[half * 7 + ks];
        bar();
#pragma unroll
        for (int t = 0; t < 2; ++t)
#pragma unroll
            for (int ks = 0; ks < 7; ++ks) wt[t][half * 7 + ks] = *(const v4u*)(xch + ((w + 4 * t) * 7 + ks) * 1024 + lane * 16);
    }
    __amdgpu_buffer_rsrc_t rs = mkrs(ws, 0x7fffffff);
    asm volatile("s_waitcnt vmcnt(0)" ::: "memory");
    bar();
    const int bb = 4 * gw + w, hh = bb / 14, bq = bb % 14;
    const unsigned obase = isG ? (unsigned)(G_OFF + ((size_t)(hh * 2) * 14 + bq) * 1024) + lane * 16 : (unsigned)(U_OFF + ((size_t)(gw - 56) * 8 + 2 * w) * 1024) + lane * 16;
    const unsigned ostep = isG ? 14336u : 1024u;
    for (int pr = 0; pr < 32; ++pr) {
        const unsigned char* cur = lds + (pr & 1) * 57856 + sp * 28928;
        if (pr + 1 < 32) XDMA(pr + 1)
        f4 acc[2][2] = {{f4{0,0,0,0}, f4{0,0,0,0}}, {f4{0,0,0,0}, f4{0,0,0,0}}};
        {
            const unsigned char* p0 = cur + (isG ? nat0 : lane * 16);
            const unsigned char* p1 = p0 + (isG ? 128 : 14464);
            v4u xa[3], xb[3];
#pragma unroll
            for (int k = 0; k < 3; ++k) { xa[k] = *(const v4u*)(p0 + k * 1024); xb[k] = *(const v4u*)(p1 + k * 1024); }
            if (isG) {
#pragma unroll
                for (int ks = 0; ks < 14; ++ks) {
                    const v4u x0 = xa[ks % 3], x1 = xb[ks % 3];
                    acc[0][0] = mfma(wt[0][ks], x0, acc[0][0]); acc[1][0] = mfma(wt[1][ks], x0, acc[1][0]);
                    acc[0][1] = mfma(wt[0][ks], x1, acc[0][1]); acc[1][1] = mfma(wt[1][ks], x1, acc[1][1]);
                    if (ks + 3 < 14) { xa[ks % 3] = *(const v4u*)(p0 + (ks + 3) * 1024); xb[ks % 3] = *(const v4u*)(p1 + (ks + 3) * 1024); }
                    SB0
                }
            } else {
#pragma unroll
                for (int ks = 0; ks < 14; ++ks) {
                    const v4u x0 = xa[ks % 3], x1 = xb[ks % 3];
                    acc[0][0] = mfma(x0, wt[0][ks], acc[0][0]); acc[1][0] = mfma(x1, wt[0][ks], acc[1][0]);
                    acc[0][1] = mfma(x0, wt[1][ks], acc[0][1]); acc[1][1] = mfma(x1, wt[1][ks], acc[1][1]);
                    if (ks + 3 < 14) { xa[ks % 3] = *(const v4u*)(p0 + (ks + 3) * 1024); xb[ks % 3] = *(const v4u*)(p1 + (ks + 3) * 1024); }
                    SB0
                }
            }
        }
        const unsigned so = __builtin_amdgcn_readfirstlane((2 * pr + sp) * 458752);
        st_sc1(pack8(acc[0][0], acc[1][0]), rs, obase, so);
        st_sc1(pack8(acc[0][1], acc[1][1]), rs, obase + ostep, so);
        const bool drain = (pr < 4) || (pr == 31);
        if (drain) asm volatile("s_waitcnt vmcnt(0)" ::: "memory");
        else asm volatile("s_waitcnt vmcnt(2)" ::: "memory");
        bar();
        if (tid == 448) __builtin_amdgcn_raw_buffer_store_b32((unsigned)(drain ? 2 * pr + 2 : 2 * pr), rs, (unsigned)CTRL_OFF + C_GU + 128u * gw, 0, 16);
    }
}

DI void a0_prep(unsigned char* ws, const int u, const u16* __restrict__ wqkv, const u16* __restrict__ wphi, const u16* __restrict__ awo) {
    const int tid = threadIdx.x, lane = tid & 63, w = __builtin_amdgcn_readfirstlane(tid >> 6);
    const int rho = lane & 15, g = lane >> 4;
    __amdgpu_buffer_rsrc_t rs = mkrs(ws, 0x7fffffff);
    {
        const int which = w >> 2, h2 = u;
        v4u bf[2];
#pragma unroll
        for (int t = 0; t < 2; ++t) {
            const int f = perm32(t, rho);
            unsigned e[8];
#pragma unroll
            for (int jj = 0; jj < 8; ++jj) { const int d = 8 * g + jj; e[jj] = (d < 28 && f < 28) ? wphi[(h2 * 28 + d) * 28 + f] : 0u; }
            bf[t] = v4u{e[0] | (e[1] << 16), e[2] | (e[3] << 16), e[4] | (e[5] << 16), e[6] | (e[7] << 16)};
        }
        v4u af[4][2];
#pragma unroll
        for (int i = 0; i < 4; ++i) {
            const int ks = min((w & 3) + 4 * i, 13);
#pragma unroll
            for (int t = 0; t < 2; ++t) {
                const u16* p = wqkv + (size_t)(32 * ks + perm32(t, rho)) * 1344 + which * 448 + h2 * 28 + g * 8;
                v2u lo = *(const v2u*)p, hi = {0, 0};
                if (g < 3) hi = *(const v2u*)(p + 4);
                af[i][t] = v4u{lo.x, lo.y, hi.x, hi.y};
            }
        }
#pragma unroll
        for (int i = 0; i < 4; ++i) {
            const int ks = (w & 3) + 4 * i;
            if (ks < 14) {
#pragma unroll
                for (int t = 0; t < 2; ++t) {
                    f4 d0 = mfma(af[i][0], bf[t], f4{0, 0, 0, 0}), d1 = mfma(af[i][1], bf[t], f4{0, 0, 0, 0});
                    st_sc1(pack8(d0 * LOG2E, d1 * LOG2E), rs, (unsigned)(WQF_OFF + which * WQF_ONE + ((size_t)(h2 * 2 + t) * 14 + ks) * 1024) + lane * 16, 0);
                }
            }
        }
    }
#pragma unroll
    for (int i = 0; i < 4; ++i) {
        const int fi = w + 8 * i;
        if (fi < 28) {
            const int t = fi / 14, ks = fi % 14;
            const int d = 16 * t + rho;
            unsigned e[8], e2[8];
            const int cb = fi >> 1, t2 = fi & 1; const int c = 32 * cb + perm32(t2, rho);
#pragma unroll
            for (int jj = 0; jj < 8; ++jj) {
                e[jj] = d < 28 ? wqkv[(size_t)(32 * ks + 8 * g + jj) * 1344 + 896 + u * 28 + d] : 0u;
                const int sl = 8 * g + jj; e2[jj] = sl < 28 ? awo[(size_t)(u * 28 + sl) * 448 + c] : 0u;
            }
            st_sc1(v4u{e[0] | (e[1] << 16), e[2] | (e[3] << 16), e[4] | (e[5] << 16), e[6] | (e[7] << 16)}, rs, (unsigned)(WQF_OFF + 2 * WQF_ONE + (size_t)(u * 28 + fi) * 1024) + lane * 16, 0);
            st_sc1(v4u{e2[0] | (e2[1] << 16), e2[2] | (e2[3] << 16), e2[4] | (e2[5] << 16), e2[6] | (e2[7] << 16)}, rs, (unsigned)(WOF_OFF + (size_t)((cb * 2 + t2) * 16 + u) * 1024) + lane * 16, 0);
        }
    }
}
DI void phi_pair(f4 d0, f4 d1, int g, v4u& pos, v4u& neg) {
    float v[8] = {d0[0], d0[1], d0[2], d0[3], d1[0], d1[1], d1[2], d1[3]};
    const bool tail = (g == 3);
    float mx = fmaxf(fmaxf(v[0], v[1]), fmaxf(v[2], v[3])), mn = fminf(fminf(v[0], v[1]), fminf(v[2], v[3]));
    float mx2 = fmaxf(fmaxf(v[4], v[5]), fmaxf(v[6], v[7])), mn2 = fminf(fminf(v[4], v[5]), fminf(v[6], v[7]));
    mx = tail ? mx : fmaxf(mx, mx2); mn = tail ? mn : fminf(mn, mn2);
    mx = xmax32(xmax16(mx)); mn = -xmax32(xmax16(-mn));
    float ep[8], en[8], sp = 0.f, sn = 0.f;
#pragma unroll
    for (int k = 0; k < 8; ++k) {
        ep[k] = __builtin_amdgcn_exp2f(v[k] - mx); en[k] = __builtin_amdgcn_exp2f(mn - v[k]);
        if (k >= 4) { ep[k] = tail ? 0.f : ep[k]; en[k] = tail ? 0.f : en[k]; }
        sp += ep[k]; sn += en[k];
    }
    sp = xsum32(xsum16(sp)); sn = xsum32(xsum16(sn));
    const float rp = __builtin_amdgcn_rcpf(sp), rn = __builtin_amdgcn_rcpf(sn);
    pos = v4u{pk2(ep[0] * rp, ep[1] * rp), pk2(ep[2] * rp, ep[3] * rp), pk2(ep[4] * rp, ep[5] * rp), pk2(ep[6] * rp, ep[7] * rp)};
    neg = v4u{pk2(en[0] * rn, en[1] * rn), pk2(en[2] * rn, en[3] * rn), pk2(en[4] * rn, en[5] * rn), pk2(en[6] * rn, en[7] * rn)};
}
DI void stage_x64(const u16* __restrict__ ax, unsigned char* lds, int blk64) {
    int tid = threadIdx.x; asm volatile("" : "+v"(tid));
    const int lane = tid & 63, w = tid >> 6;
    v4u stg[7];
#pragma unroll
    for (int i = 0; i < 7; ++i) {
        const int f = i * 8 + w; const int q = f / 14, ks = f % 14;
        stg[i] = *(const v4u*)(ax + (size_t)(blk64 * 64 + 32 * (q >> 1) + perm32(q & 1, lane & 15)) * D + ks * 32 + (lane >> 4) * 8);
    }
#pragma unroll
    for (int i = 0; i < 7; ++i) *(v4u*)(lds + (i * 512 + tid) * 16) = stg[i];
}

DI void a1_unit(unsigned char* ws, unsigned char* lds, const int u, const u16* __restrict__ ax) {
    const int tid = threadIdx.x, lane = tid & 63, w = __builtin_amdgcn_readfirstlane(tid >> 6);
    const int rho = lane & 15, g = lane >> 4;
    __amdgpu_buffer_rsrc_t rs = mkrs(ws, 0x7fffffff);
    unsigned char* T = lds + 57344 + w * 9216;
    constexpr int TS = 144;
    f4 kv[2][4][2];
#pragma unroll
    for (int a = 0; a < 2; ++a)
#pragma unroll
        for (int b = 0; b < 4; ++b) { kv[a][b][0] = f4{0,0,0,0}; kv[a][b][1] = f4{0,0,0,0}; }
    for (int pass = 0; pass < 2; ++pass) {
        __syncthreads();
        stage_x64(ax, lds, 2 * u + pass);
        __syncthreads();
#pragma unroll
        for (int hi = 0; hi < 2; ++hi) {
            const int h = 2 * w + hi;
            const unsigned wk = (unsigned)(WQF_OFF + WQF_ONE + (size_t)h * 28672) + lane * 16;
            const unsigned wv = (unsigned)(WQF_OFF + 2 * WQF_ONE + (size_t)h * 28672) + lane * 16;
            f4 ak[2][4], av[2][4];
#pragma unroll
            for (int t = 0; t < 2; ++t)
#pragma unroll
                for (int q = 0; q < 4; ++q) { ak[t][q] = f4{0,0,0,0}; av[t][q] = f4{0,0,0,0}; }
#pragma unroll 2
            for (int ks = 0; ks < 14; ++ks) {
                v4u k0 = ld_sc1(rs, wk + ks * 1024, 0), k1 = ld_sc1(rs, wk + (14 + ks) * 1024, 0);
                v4u v0 = ld_sc1(rs, wv + ks * 1024, 0), v1 = ld_sc1(rs, wv + (14 + ks) * 1024, 0);
#pragma unroll
                for (int q = 0; q < 4; ++q) {
                    v4u x = *(const v4u*)(lds + (q * 14 + ks) * 1024 + lane * 16);
                    ak[0][q] = mfma(k0, x, ak[0][q]); ak[1][q] = mfma(k1, x, ak[1][q]);
                    av[0][q] = mfma(x, v0, av[0][q]); av[1][q] = mfma(x, v1, av[1][q]);
                }
            }
#pragma unroll
            for (int q = 0; q < 4; ++q) {
                v4u pos, neg; phi_pair(ak[0][q], ak[1][q], g, pos, neg);
                const int tok = 32 * (q >> 1) + perm32(q & 1, rho);
                *(v4u*)(T + tok * TS + g * 16) = pos;
                *(v4u*)(T + tok * TS + 64 + g * 16) = neg;
            }
            asm volatile("s_waitcnt lgkmcnt(0)" ::: "memory");
            v4u vf[2][2];
#pragma unroll
            for (int bk = 0; bk < 2; ++bk)
#pragma unroll
                for (int t = 0; t < 2; ++t) {
                    vf[bk][t] = pack8(av[t][2 * bk], av[t][2 * bk + 1]);
                    if (t == 1 && rho == 12) vf[bk][t] = v4u{0x3F803F80u, 0x3F803F80u, 0x3F803F80u, 0x3F803F80u};
                }
            const unsigned trb = (8 * g + (rho >> 2)) * TS + (lane & 3) * 8;
#pragma unroll
            for (int bk = 0; bk < 2; ++bk)
#pragma unroll
                for (int ft = 0; ft < 4; ++ft) {
                    v2u lo = trread(T + trb + (32 * bk) * TS + ft * 32), hi2 = trread(T + trb + (32 * bk + 4) * TS + ft * 32);
                    v4u pa = {lo.x, lo.y, hi2.x, hi2.y};
                    kv[hi][ft][0] = mfma(pa, vf[bk][0], kv[hi][ft][0]);
                    kv[hi][ft][1] = mfma(pa, vf[bk][1], kv[hi][ft][1]);
                }
            asm volatile("s_waitcnt lgkmcnt(0)" ::: "memory");
        }
    }
#pragma unroll
    for (int hi = 0; hi < 2; ++hi)
#pragma unroll
        for (int ft = 0; ft < 4; ++ft)
#pragma unroll
            for (int dt = 0; dt < 2; ++dt)
                st_sc1(__builtin_bit_cast(v4u, kv[hi][ft][dt]), rs, (unsigned)(KVP_OFF + ((size_t)(u * 16 + 2 * w + hi) * 8 + ft * 2 + dt) * 1024) + lane * 16, 0);
}
DI void a15_unit(unsigned char* ws, unsigned char* lds, const int h) {
    const int tid = threadIdx.x, lane = tid & 63, w = tid >> 6;
    __amdgpu_buffer_rsrc_t rs = mkrs(ws, 0x7fffffff);
    f4 s = {0, 0, 0, 0};
    v4u t[16];
#pragma unroll
    for (int u = 0; u < 16; ++u) t[u] = ld_sc1(rs, (unsigned)(KVP_OFF + ((size_t)(u * 16 + h) * 8) * 1024) + tid * 16, 0);
#pragma unroll
    for (int u = 0; u < 16; ++u) s += __builtin_bit_cast(f4, t[u]);
    const int ft = w >> 1, dt = w & 1, rho = lane & 15, g = lane >> 4;
    __syncthreads();
    v2u o; o.x = pk2(s[0], s[1]); o.y = pk2(s[2], s[3]);
    *(v2u*)(lds + (16 * dt + rho) * 128 + (16 * ft + 4 * g) * 2) = o;
    __syncthreads();
    if (tid < 256) {
        const int fr = tid >> 6;
        const int tt = fr >> 1, kb = fr & 1;
        v4u v = *(const v4u*)(lds + perm32(tt, rho) * 128 + (32 * kb + 8 * g) * 2);
        st_sc1(v, rs, (unsigned)(KVF_OFF + (size_t)(h * 4 + fr) * 1024) + lane * 16, 0);
    }
}
DI void a2_unit(unsigned char* ws, unsigned char* lds, const int u, u16* __restrict__ out, const u16* __restrict__ ax) {
    int tid = threadIdx.x; asm volatile("" : "+v"(tid));
    const int lane = tid & 63, w = __builtin_amdgcn_readfirstlane(tid >> 6);
    const int rho = lane & 15, g = lane >> 4;
    __amdgpu_buffer_rsrc_t rs = mkrs(ws, 0x7fffffff);
    unsigned char* O = lds + 57344;
    __syncthreads();
    stage_x64(ax, lds, u);
    wg_wait(rs, C_A0, 16, 4);
    v4u pos[2][4], neg[2][4];
#pragma unroll
    for (int hi = 0; hi < 2; ++hi) {
        const int h = 2 * w + hi;
        const unsigned wq = (unsigned)(WQF_OFF + (size_t)h * 28672) + lane * 16;
        f4 aq[2][4];
#pragma unroll
        for (int t = 0; t < 2; ++t)
#pragma unroll
            for (int q = 0; q < 4; ++q) aq[t][q] = f4{0,0,0,0};
#pragma unroll 2
        for (int ks = 0; ks < 14; ++ks) {
            v4u k0 = ld_sc1(rs, wq + ks * 1024, 0), k1 = ld_sc1(rs, wq + (14 + ks) * 1024, 0);
#pragma unroll
            for (int q = 0; q < 4; ++q) {
                v4u x = *(const v4u*)(lds + (q * 14 + ks) * 1024 + lane * 16);
                aq[0][q] = mfma(k0, x, aq[0][q]); aq[1][q] = mfma(k1, x, aq[1][q]);
            }
        }
#pragma unroll
        for (int q = 0; q < 4; ++q) phi_pair(aq[0][q], aq[1][q], g, pos[hi][q], neg[hi][q]);
    }
    wg_wait(rs, C_KV, 16, 4);
#pragma unroll
    for (int hi = 0; hi < 2; ++hi) {
        const int h = 2 * w + hi;
        v4u kf[4];
#pragma unroll
        for (int fr = 0; fr < 4; ++fr) kf[fr] = ld_sc1(rs, (unsigned)(KVF_OFF + (size_t)(h * 4 + fr) * 1024) + lane * 16, 0);
#pragma unroll
        for (int q = 0; q < 4; ++q) {
            f4 n0 = mfma(kf[0], pos[hi][q], f4{0,0,0,0}); n0 = mfma(kf[1], neg[hi][q], n0);
            f4 n1 = mfma(kf[2], pos[hi][q], f4{0,0,0,0}); n1 = mfma(kf[3], neg[hi][q], n1);
            const float den = asf((unsigned)__builtin_amdgcn_ds_bpermute((48 + rho) * 4, (int)asu(n1[0])));
            const float rd = 1.0f / (den + 1e-6f);
            if (g == 3) n1 = f4{0, 0, 0, 0};
            *(v4u*)(O + (h * 4 + q) * 1024 + lane * 16) = pack8(n0 * rd, n1 * rd);
        }
    }
    __syncthreads();
    for (int cb = w; cb < 14; cb += 8) {
        f4 acc[2][4];
#pragma unroll
        for (int t = 0; t < 2; ++t)
#pragma unroll
            for (int q = 0; q < 4; ++q) acc[t][q] = f4{0,0,0,0};
        const unsigned wo = (unsigned)(WOF_OFF + (size_t)cb * 32768) + lane * 16;
#pragma unroll 4
        for (int h = 0; h < 16; ++h) {
            v4u w0 = ld_sc1(rs, wo + h * 1024, 0), w1 = ld_sc1(rs, wo + (16 + h) * 1024, 0);
#pragma unroll
            for (int q = 0; q < 4; ++q) {
                v4u o = *(const v4u*)(O + (h * 4 + q) * 1024 + lane * 16);
                acc[0][q] = mfma(w0, o, acc[0][q]); acc[1][q] = mfma(w1, o, acc[1][q]);
            }
        }
#pragma unroll
        for (int q = 0; q < 4; ++q) {
            const int tok = u * 64 + 32 * (q >> 1) + perm32(q & 1, rho);
            *(v4u*)(out + (size_t)tok * OUTW + 32 * cb + 8 * g) = pack8(acc[0][q], acc[1][q]);
        }
    }
}

DI void p2_unit(unsigned char* ws, unsigned char* lds, const int unit, u16* __restrict__ out) {
    const int seg = unit >> 1, chalf = unit & 1;
    int tid = threadIdx.x; asm volatile("" : "+v"(tid));
    const int lane = tid & 63, w = __builtin_amdgcn_readfirstlane(tid >> 6);
    const int rho = lane & 15, g = lane >> 4;
    __amdgpu_buffer_rsrc_t rs = mkrs(ws, 0x7fffffff);
    unsigned char* xl = lds + 40960;
    __syncthreads();
    for (int i = tid; i < 1792; i += 512) *(v4u*)(xl + i * 16) = *(const v4u*)(ws + XF_OFF + (size_t)seg * 28672 + i * 16);
    if (tid < 64 && seg > 8) wait_flags(rs, C_REC, seg - 8, 16);
    if (tid < 128) wait_flags(rs, C_GU + 128u * (unsigned)min(tid, 111), seg + 1, 8);
    __syncthreads();
    f4 sx[2][2][2];
#pragma unroll
    for (int hi = 0; hi < 2; ++hi) {
#pragma unroll
        for (int a = 0; a < 2; ++a) { sx[hi][a][0] = f4{0,0,0,0}; sx[hi][a][1] = f4{0,0,0,0}; }
        const unsigned gb = (unsigned)(G_OFF + (size_t)(seg * 16 + 2 * w + hi) * 28672) + lane * 16;
#pragma unroll 7
        for (int ks = 0; ks < 14; ++ks) {
            v4u g0 = ld_sc1(rs, gb + ks * 1024, 0), g1 = ld_sc1(rs, gb + (14 + ks) * 1024, 0);
            v4u x0 = *(const v4u*)(xl + ks * 1024 + lane * 16), x1 = *(const v4u*)(xl + (14 + ks) * 1024 + lane * 16);
            sx[hi][0][0] = mfma(g0, x0, sx[hi][0][0]); sx[hi][0][1] = mfma(g0, x1, sx[hi][0][1]);
            sx[hi][1][0] = mfma(g1, x0, sx[hi][1][0]); sx[hi][1][1] = mfma(g1, x1, sx[hi][1][1]);
        }
    }
    f4* sxl = (f4*)(lds + 69632) + tid;
#pragma unroll
    for (int hi = 0; hi < 2; ++hi)
#pragma unroll
        for (int a = 0; a < 2; ++a)
#pragma unroll
            for (int b2 = 0; b2 < 2; ++b2) sxl[((hi * 2 + a) * 2 + b2) * 512] = sx[hi][a][b2];
    SB0
    const int cb = 7 * chalf + (w < 7 ? w : 6);
    v4u uf[2][16];
    unsigned uo[2], vo[2];
#pragma unroll
    for (int t = 0; t < 2; ++t) {
        const int c = 32 * cb + perm32(t, rho); const int j = c >> 3, c8 = c & 7;
        uo[t] = (unsigned)(U_OFF + (size_t)(seg * 56 + j) * 8192) + (c8 + 16 * g) * 16;
        vo[t] = (unsigned)(VP_OFF + (size_t)(seg * 56 + j) * 2048) + g * 128 + c8 * 16;
#pragma unroll
        for (int h = 0; h < 16; ++h) uf[t][h] = ld_sc1(rs, uo[t] + (h >> 1) * 1024 + (h & 1) * 128, 0);
    }
    if (seg < 61) { if (tid < 64) wait_flags(rs, C_REC, seg + 1, 8); }
    else if (tid < 128) { wait_flags(rs, C_REC, 59, 8); wait_flags(rs, C_DN2 + 4u * (unsigned)min(tid, 111), 1, 1); }
    __syncthreads();
    f4 sm[2] = {f4{0,0,0,0}, f4{0,0,0,0}};
    v4u vf[2][4];
    {
        const unsigned gmb = (unsigned)(GM_OFF + (size_t)(seg * 16 + 2 * w + (rho >> 3)) * 7168) + g * 128 + (rho & 7) * 16;
        v4u gm[14];
#pragma unroll
        for (int ks = 0; ks < 14; ++ks) gm[ks] = ld_sc1(rs, gmb + ks * 512, 0);
#pragma unroll
        for (int t = 0; t < 2; ++t)
#pragma unroll
            for (int w4 = 0; w4 < 4; ++w4) vf[t][w4] = ld_sc1(rs, vo[t] + w4 * 512, 0);
        SB0
#pragma unroll
        for (int ks = 0; ks < 14; ++ks) {
            v4u x0 = *(const v4u*)(xl + ks * 1024 + lane * 16), x1 = *(const v4u*)(xl + (14 + ks) * 1024 + lane * 16);
            sm[0] = mfma(gm[ks], x0, sm[0]); sm[1] = mfma(gm[ks], x1, sm[1]);
            SB0
        }
    }
#pragma unroll
    for (int hi = 0; hi < 2; ++hi) {
        const int h = 2 * w + hi;
        const bool own = ((g >> 1) == hi);
#pragma unroll
        for (int tp = 0; tp < 2; ++tp) {
            const int tq = perm32(tp, rho);
            float v[8];
#pragma unroll
            for (int tau = 0; tau < 2; ++tau) {
                const f4 sv = sxl[((hi * 2 + tau) * 2 + tp) * 512];
#pragma unroll
                for (int r = 0; r < 4; ++r) { const int key = 16 * tau + 4 * g + r; v[4 * tau + r] = (key <= tq) ? sv[r] : -3.0e38f; }
            }
            float mx = fmaxf(fmaxf(fmaxf(v[0], v[1]), fmaxf(v[2], v[3])), fmaxf(fmaxf(v[4], v[5]), fmaxf(v[6], v[7])));
            const float mo = fmaxf(fmaxf(sm[tp][0], sm[tp][1]), fmaxf(sm[tp][2], sm[tp][3]));
            mx = own ? fmaxf(mx, mo) : mx;
            mx = xmax32(xmax16(mx));
            float sum = 0.f, em[4];
#pragma unroll
            for (int k = 0; k < 8; ++k) { v[k] = __builtin_amdgcn_exp2f(v[k] - mx); sum += v[k]; }
#pragma unroll
            for (int r = 0; r < 4; ++r) { em[r] = own ? __builtin_amdgcn_exp2f(sm[tp][r] - mx) : 0.f; sum += em[r]; }
            sum = xsum32(xsum16(sum));
            const float rz = __builtin_amdgcn_rcpf(sum);
            v2u p0; p0.x = pk2(v[0] * rz, v[1] * rz); p0.y = pk2(v[2] * rz, v[3] * rz);
            v2u p1; p1.x = pk2(v[4] * rz, v[5] * rz); p1.y = pk2(v[6] * rz, v[7] * rz);
            *(v2u*)(lds + (h * 2 + tp) * 1024 + (rho + 16 * (g >> 1)) * 16 + (g & 1) * 8) = p0;
            *(v2u*)(lds + (h * 2 + tp) * 1024 + (rho + 16 * (2 + (g >> 1))) * 16 + (g & 1) * 8) = p1;
            if (own) {
                v2u pm; pm.x = pk2(em[0] * rz, em[1] * rz); pm.y = pk2(em[2] * rz, em[3] * rz);
                *(v2u*)(lds + ((16 + (h >> 2)) * 2 + tp) * 1024 + (rho + 16 * (h & 3)) * 16 + (g & 1) * 8) = pm;
            }
        }
    }
    __syncthreads();
    if (w < 7) {
        f4 acc[2][2] = {{f4{0,0,0,0}, f4{0,0,0,0}}, {f4{0,0,0,0}, f4{0,0,0,0}}};
#pragma unroll
        for (int h = 0; h < 16; ++h) {
            v4u p0 = *(const v4u*)(lds + (h * 2) * 1024 + lane * 16), p1 = *(const v4u*)(lds + (h * 2 + 1) * 1024 + lane * 16);
#pragma unroll
            for (int t = 0; t < 2; ++t) { acc[t][0] = mfma(uf[t][h], p0, acc[t][0]); acc[t][1] = mfma(uf[t][h], p1, acc[t][1]); }
        }
#pragma unroll
        for (int w4 = 0; w4 < 4; ++w4) {
            v4u p0 = *(const v4u*)(lds + ((16 + w4) * 2) * 1024 + lane * 16), p1 = *(const v4u*)(lds + ((16 + w4) * 2 + 1) * 1024 + lane * 16);
#pragma unroll
            for (int t = 0; t < 2; ++t) { acc[t][0] = mfma(vf[t][w4], p0, acc[t][0]); acc[t][1] = mfma(vf[t][w4], p1, acc[t][1]); }
        }
#pragma unroll
        for (int tp = 0; tp < 2; ++tp) {
            const int tok = seg * 32 + perm32(tp, rho);
            *(v4u*)(out + (size_t)tok * OUTW + 448 + 32 * cb + 8 * g) = pack8(acc[0][tp], acc[1][tp]);
        }
    }
}

constexpr int LDS_MAIN = 135168 + 64;
__global__ void __launch_bounds__(512, 2) k_main(unsigned char* ws, u16* __restrict__ out, const u16* __restrict__ ax, const u16* __restrict__ wqkv, const u16* __restrict__ wphi, const u16* __restrict__ awo, int dS, int dV, int dVm, int pad_) {
    extern __shared__ __attribute__((aligned(16))) unsigned char lds[];
    const int bidx = blockIdx.x;
    const int idx = (bidx >> 3) * 4 + (bidx & 3);
#define PT(i)
    if ((bidx & 7) < 4) { rec_role(ws, lds, idx, dS, dV, dVm); return; }
    __amdgpu_buffer_rsrc_t rs = mkrs(ws, 0x7fffffff);
    if (idx < 112) gu_role(ws, lds, idx);
    else {
        const int u = idx - 112;
        a0_prep(ws, u, wqkv, wphi, awo);
        signal_add(ws, C_A0);
        wg_wait(rs, C_A0, 16, 2);
        a1_unit(ws, lds, u, ax);
        signal_add(ws, C_A1);
        wg_wait(rs, C_A1, 16, 4);
        a15_unit(ws, lds, u);
        signal_add(ws, C_KV);
    }
    PT(0)
    for (;;) {
        __syncthreads();
        if (threadIdx.x == 0) LDSV(LDS_MAIN - 16) = __hip_atomic_fetch_add((unsigned*)(ws + CTRL_OFF + C_TICKET), 1u, __ATOMIC_RELAXED, __HIP_MEMORY_SCOPE_AGENT);
        __syncthreads();
        const int t = __builtin_amdgcn_readfirstlane(LDSV(LDS_MAIN - 16));
        if (t >= 160) break;
        if (t < 32) a2_unit(ws, lds, t, out, ax);
        else p2_unit(ws, lds, t - 32, out);
        if (t < 32) { PT(1) } else { PT(2) }
    }
}

#define REC_DS (89 | (50 << 8))
#define REC_DV (82 | (1 << 8))
#define REC_DVM (4 | (8 << 8) | (0 << 16))
constexpr int g_dS = REC_DS, g_dV = REC_DV, g_dVm = REC_DVM;

extern "C" void kernel_launch(void* const* d_in, const int* in_sizes, int n_in, void* d_out, int out_size, void* d_ws, size_t ws_size, hipStream_t stream) {
    const u16* A_x = (const u16*)d_in[0]; const u16* A_wqkv = (const u16*)d_in[1]; const u16* A_wphi = (const u16*)d_in[2]; const u16* A_wo = (const u16*)d_in[3];
    const u16* B_x = (const u16*)d_in[4]; const u16* B_mem0 = (const u16*)d_in[5];
    const u16* Wq = (const u16*)d_in[6]; const u16* Wk = (const u16*)d_in[7]; const u16* Wv = (const u16*)d_in[8]; const u16* Wo = (const u16*)d_in[9];
    u16* out = (u16*)d_out;
    unsigned char* ws = (unsigned char*)(((uintptr_t)d_ws + 2 * MB - 1) & ~(uintptr_t)(2 * MB - 1));
    (void)hipFuncSetAttribute((const void*)k_pre, hipFuncAttributeMaxDynamicSharedMemorySize, 61440);
    (void)hipFuncSetAttribute((const void*)k_main, hipFuncAttributeMaxDynamicSharedMemorySize, LDS_MAIN);
    k_pre<<<256, 448, 61440, stream>>>(ws, Wq, Wk, Wv, Wo, B_x, B_mem0);
    k_main<<<256, 512, LDS_MAIN, stream>>>(ws, out, A_x, A_wqkv, A_wphi, A_wo, g_dS, g_dV, g_dVm, 0);
}
```

```cpp
#include <hip/hip_runtime.h>
#include <hip/hip_bf16.h>
#include <stdint.h>

typedef unsigned short u16;
typedef unsigned v4u __attribute__((ext_vector_type(4)));
typedef unsigned v2u __attribute__((ext_vector_type(2)));
typedef float f4 __attribute__((ext_vector_type(4)));
typedef float f2 __attribute__((ext_vector_type(2)));
typedef short s8 __attribute__((ext_vector_type(8)));
typedef __bf16 bf2 __attribute__((ext_vector_type(2)));
#define DI __device__ __forceinline__

constexpr int D = 448, H = 16, DH = 256, MEM = 8, SEG = 32, NSEG = 64, S_TOK = 2048;
constexpr int NKS = 14;
constexpr int OUTW = 896;
constexpr unsigned SENT = 0xFFFFFFFFu;
constexpr float LOG2E = 1.4426950408889634f;

constexpr size_t MB = 1024 * 1024;
constexpr int KCOPY = 4;
constexpr size_t COMM_OFF = 0;
constexpr size_t COMM_BYTES = 2 * MB;
constexpr size_t CTRL_OFF = COMM_BYTES;
constexpr size_t AS_OFF = CTRL_OFF + 262144;
constexpr size_t AS_BYTES = (size_t)16 * 14 * 2 * 14 * 1024;
constexpr size_t BS_OFF = AS_OFF + AS_BYTES;
constexpr size_t BS_BYTES = (size_t)56 * 8 * 14 * 1024;
constexpr size_t XF_OFF = BS_OFF + BS_BYTES;
constexpr size_t XF_BYTES = (size_t)64 * 28 * 1024;
constexpr size_t G_OFF = XF_OFF + XF_BYTES;
constexpr size_t G_BYTES = (size_t)64 * 16 * 28 * 1024;
constexpr size_t U_OFF = G_OFF + G_BYTES;
constexpr size_t U_BYTES = (size_t)64 * 56 * 8 * 1024;
constexpr size_t GM_OFF = U_OFF + U_BYTES;
constexpr size_t GM_BYTES = (size_t)64 * 16 * 14 * 512;
constexpr size_t VP_OFF = GM_OFF + GM_BYTES;
constexpr size_t VP_BYTES = (size_t)64 * 56 * 4 * 512;
constexpr size_t AXF_OFF = VP_OFF + VP_BYTES;
constexpr size_t WQF_OFF = AXF_OFF + XF_BYTES;
constexpr size_t WQF_ONE = (size_t)16 * 2 * 14 * 1024;
constexpr size_t WOF_OFF = WQF_OFF + 3 * WQF_ONE;
constexpr size_t KVP_OFF = WOF_OFF + WQF_ONE;
constexpr size_t KVF_OFF = KVP_OFF + (size_t)16 * 16 * 8192;
constexpr size_t WS_END = KVF_OFF + 65536;
constexpr unsigned C_POL = 0, C_REC = 128, C_DONE = 256, C_A1 = 384, C_KV = 512, C_TICKET = 640, C_A0 = 768, C_GU = 1024;
constexpr unsigned C_DN2 = 16384;
constexpr unsigned C_DBG = 32768;

DI unsigned pk2(float a, float b) { f2 v = {a, b}; return __builtin_bit_cast(unsigned, __builtin_convertvector(v, bf2)); }
DI float asf(unsigned x) { return __builtin_bit_cast(float, x); }
DI unsigned asu(float x) { return __builtin_bit_cast(unsigned, x); }
DI float bflo(unsigned x) { return __builtin_bit_cast(float, x << 16); }
DI float bfhi(unsigned x) { return __builtin_bit_cast(float, x & 0xffff0000u); }
DI float bf2f(u16 x) { return __builtin_bit_cast(float, (unsigned)x << 16); }
DI f4 mfma(v4u a, v4u b, f4 c) { return __builtin_amdgcn_mfma_f32_16x16x32_bf16(__builtin_bit_cast(s8, a), __builtin_bit_cast(s8, b), c, 0, 0, 0); }
DI v4u pack8(f4 a, f4 b) { v4u r; r.x = pk2(a[0], a[1]); r.y = pk2(a[2], a[3]); r.z = pk2(b[0], b[1]); r.w = pk2(b[2], b[3]); return r; }
DI v4u nosent(v4u v) { v.x = min(v.x, 0xFFFFFFFEu); v.y = min(v.y, 0xFFFFFFFEu); v.z = min(v.z, 0xFFFFFFFEu); v.w = min(v.w, 0xFFFFFFFEu); return v; }
DI int perm32(int t, int rho) { return 8 * (rho >> 2) + 4 * t + (rho & 3); }
DI __amdgpu_buffer_rsrc_t mkrs(void* p, unsigned bytes) { return __builtin_amdgcn_make_buffer_rsrc(p, 0, bytes, 0x00020000); }
DI v4u ld_sc1(__amdgpu_buffer_rsrc_t rs, unsigned voff, unsigned soff) { return __builtin_amdgcn_raw_buffer_load_b128(rs, voff, soff, 16); }
DI v2u ld2_sc1(__amdgpu_buffer_rsrc_t rs, unsigned voff, unsigned soff) { return __builtin_amdgcn_raw_buffer_load_b64(rs, voff, soff, 16); }
DI void st_sc1(v4u v, __amdgpu_buffer_rsrc_t rs, unsigned voff, unsigned soff) { __builtin_amdgcn_raw_buffer_store_b128(v, rs, voff, soff, 16); }
DI void st2_sc1(v2u v, __amdgpu_buffer_rsrc_t rs, unsigned voff, unsigned soff) { __builtin_amdgcn_raw_buffer_store_b64(v, rs, voff, soff, 16); }
DI unsigned long long mtime() { unsigned long long t; asm volatile("s_memtime %0\n\ts_waitcnt lgkmcnt(0)" : "=s"(t) :: "memory"); return t; }

typedef short s4t __attribute__((ext_vector_type(4)));
DI v2u trread(const unsigned char* p) { return __builtin_bit_cast(v2u, __builtin_amdgcn_ds_read_tr16_b64_v4i16((__attribute__((address_space(3))) s4t*)p)); }
DI unsigned comm_step_base(int step, int copy) { return __builtin_amdgcn_readfirstlane((unsigned)(step & 7) * 262144u + (unsigned)copy * 65536u); }
DI unsigned comm_off(int chunk0, unsigned o, unsigned pol) { return (unsigned)(chunk0 + (o >> 13)) * 16384u + pol * 8192u + (o & 8191u); }

__global__ void __launch_bounds__(448) k_pre(unsigned char* ws, const u16* __restrict__ Wq, const u16* __restrict__ Wk, const u16* __restrict__ Wv, const u16* __restrict__ Wo,
                                             const u16* __restrict__ bx, const u16* __restrict__ mem0) {
    extern __shared__ __attribute__((aligned(16))) unsigned char lds[];
    const int tid = threadIdx.x, lane = tid & 63, w = __builtin_amdgcn_readfirstlane(tid >> 6);
    const int wg = blockIdx.x;
#define PP(i)
    const int h = (wg & 7) * 2 + ((wg >> 3) & 1), rest = wg >> 4;
    const int mat = rest >> 3, rh = (rest >> 2) & 1, q = rest & 3;
    const int rho = lane & 15, g = lane >> 4;
    const int rb = 7 * rh + w;
    __amdgpu_buffer_rsrc_t rs = mkrs(ws, 0x7fffffff);
    const int gt = wg * 448 + tid; constexpr int GN = 256 * 448;
    constexpr int WST = 240;
    v4u bt[8];
    if (mat == 0) {
        const int ct = 7 * q + w; const int d = 32 * (ct >> 1) + perm32(ct & 1, rho);
#pragma unroll
        for (int es = 0; es < 8; ++es) bt[es] = *(const v4u*)(Wq + ((size_t)d * H + h) * DH + es * 32 + g * 8);
    } else {
#pragma unroll
        for (int it = 0; it < 8; ++it) {
            const int idx = it * 448 + tid; const int e = idx / 14, c8 = idx % 14;
            bt[it] = *(const v4u*)(Wo + ((size_t)h * DH + e) * D + 112 * q + c8 * 8);
        }
    }
    const u16* Wr = mat ? Wv : Wk;
    v4u a[2][8];
#pragma unroll
    for (int t = 0; t < 2; ++t) {
        const int row = 32 * rb + perm32(t, rho);
#pragma unroll
        for (int es = 0; es < 8; ++es) a[t][es] = *(const v4u*)(Wr + ((size_t)row * H + h) * DH + es * 32 + g * 8);
    }
    v4u xfv;
    {
        const int f = gt >> 6; const int ks = f % 14; const int st = f / 14; const int tau = st & 1, seg = st >> 1;
        xfv = *(const v4u*)(bx + (size_t)(seg * 32 + perm32(tau, rho)) * D + ks * 32 + g * 8);
    }
    __builtin_amdgcn_sched_barrier(0);
    if (wg == 1 && tid < 128) __builtin_amdgcn_raw_buffer_store_b32(0u, rs, (unsigned)CTRL_OFF + tid * 128, 0, 16);
    if (wg == 2 && tid < 128) __builtin_amdgcn_raw_buffer_store_b32(0u, rs, (unsigned)CTRL_OFF + C_DN2 + tid * 4, 0, 16);
    for (unsigned i = gt; i < COMM_BYTES / 16; i += GN) {
        unsigned off = i * 16;
        unsigned within = off & 65535u, slot = off >> 18;
        unsigned chunk = (within >> 14) & 3, o = within & 8191u;
        if (chunk == 0 && (o >= 7184 || (slot == 0 && o < 7168))) continue;
        st_sc1(v4u{SENT, SENT, SENT, SENT}, rs, off, 0);
    }
    PP(0)
    if (mat == 0) {
#pragma unroll
        for (int es = 0; es < 8; ++es) *(v4u*)(lds + (w * 8 + es) * 1024 + lane * 16) = bt[es];
    } else {
#pragma unroll
        for (int it = 0; it < 8; ++it) { const int idx = it * 448 + tid; const int e = idx / 14, c8 = idx % 14; *(v4u*)(lds + e * WST + c8 * 16) = bt[it]; }
    }
    PP(3)
    __syncthreads();
    PP(4)
    f4 acc[2][7];
#pragma unroll
    for (int t = 0; t < 2; ++t)
#pragma unroll
        for (int c = 0; c < 7; ++c) acc[t][c] = f4{0, 0, 0, 0};
    if (mat == 0) {
#pragma unroll
        for (int es = 0; es < 8; ++es)
#pragma unroll
            for (int c = 0; c < 7; ++c) {
                v4u bfr = *(const v4u*)(lds + (c * 8 + es) * 1024 + lane * 16);
                acc[0][c] = mfma(a[0][es], bfr, acc[0][c]);
                acc[1][c] = mfma(a[1][es], bfr, acc[1][c]);
            }
        const float sc = LOG2E * 0.0625f;
#pragma unroll
        for (int c = 0; c < 7; ++c) {
            const int ct = 7 * q + c;
            st_sc1(pack8(acc[0][c] * sc, acc[1][c] * sc), rs, (unsigned)(AS_OFF + ((size_t)((h * 14 + (ct >> 1)) * 2 + (ct & 1)) * 14 + rb) * 1024) + lane * 16, 0);
        }
    } else {
        const unsigned trb = (8 * g + (rho >> 2)) * WST + (lane & 3) * 8;
#pragma unroll
        for (int es = 0; es < 8; ++es)
#pragma unroll
            for (int c = 0; c < 7; ++c) {
                v2u lo = trread(lds + trb + (32 * es) * WST + c * 32), hi = trread(lds + trb + (32 * es + 4) * WST + c * 32);
                v4u bfr = {lo.x, lo.y, hi.x, hi.y};
                acc[0][c] = mfma(a[0][es], bfr, acc[0][c]);
                acc[1][c] = mfma(a[1][es], bfr, acc[1][c]);
            }
#pragma unroll
        for (int c = 0; c < 7; ++c) {
            const int ct = 7 * q + c;
            const int j = 2 * ct + (rho >> 3);
            st_sc1(pack8(acc[0][c], acc[1][c]), rs, (unsigned)(BS_OFF + ((size_t)(j * 8 + (h >> 1)) * 14 + rb) * 1024) + (((h & 1) * 8 + (rho & 7)) + 16 * g) * 16, 0);
        }
    }
    st_sc1(xfv, rs, (unsigned)XF_OFF + gt * 16, 0);
    if (wg < 8) {
        const int j = tid >> 3, irow = tid & 7;
        st_sc1(nosent(*(const v4u*)(mem0 + irow * D + j * 8)), rs, (wg >> 1) * 65536u + (wg & 1) * 8192u + tid * 16, 0);
    }
    PP(5)
}

DI unsigned umax3(unsigned a, unsigned b, unsigned c) { return max(max(a, b), c); }
DI unsigned vmax4(unsigned t, v4u v) { return umax3(umax3(t, v.x, v.y), v.z, v.w); }
DI float dpp_xor8(float v) { return asf((unsigned)__builtin_amdgcn_update_dpp(0, (int)asu(v), 0x128, 0xf, 0xf, false)); }
DI unsigned dpp_xor8u(unsigned v) { return (unsigned)__builtin_amdgcn_update_dpp(0, (int)v, 0x128, 0xf, 0xf, false); }
constexpr unsigned SPIN_MAX = 200000;
DI float dpp_x1(float v) { return asf((unsigned)__builtin_amdgcn_update_dpp(0, (int)asu(v), 0xB1, 0xf, 0xf, false)); }
DI float dpp_x2(float v) { return asf((unsigned)__builtin_amdgcn_update_dpp(0, (int)asu(v), 0x4E, 0xf, 0xf, false)); }
DI f4 quadsum(f4 v) {
#pragma unroll
    for (int r = 0; r < 4; ++r) v[r] += dpp_x1(v[r]);
#pragma unroll
    for (int r = 0; r < 4; ++r) v[r] += dpp_x2(v[r]);
    return v;
}
DI float xmax16(float x) { auto r = __builtin_amdgcn_permlane16_swap(asu(x), asu(x), false, false); unsigned a = r[0], b = r[1]; return fmaxf(asf(a), asf(b)); }
DI float xmax32(float x) { auto r = __builtin_amdgcn_permlane32_swap(asu(x), asu(x), false, false); unsigned a = r[0], b = r[1]; return fmaxf(asf(a), asf(b)); }
DI float xsum16(float x) { auto r = __builtin_amdgcn_permlane16_swap(asu(x), asu(x), false, false); unsigned a = r[0], b = r[1]; return asf(a) + asf(b); }
DI float xsum32(float x) { auto r = __builtin_amdgcn_permlane32_swap(asu(x), asu(x), false, false); unsigned a = r[0], b = r[1]; return asf(a) + asf(b); }
#define LOADM(mf) v4u mf[14]; _Pragma("unroll") for (int ks = 0; ks < 14; ++ks) mf[ks] = *(const v4u*)(ldsm + ks * 512 + mfrag_off); __builtin_amdgcn_sched_barrier(0);
DI void bar() { asm volatile("s_waitcnt lgkmcnt(0)" ::: "memory"); __builtin_amdgcn_s_barrier(); asm volatile("" ::: "memory"); }
DI unsigned now32() { return (unsigned)mtime(); }
DI void wait_until(unsigned target) {
    int rem = (int)(target - now32());
    while (rem > 80) { __builtin_amdgcn_s_sleep(1); rem -= 64; }
    while (rem > 0) { asm volatile("s_nop 7" ::: "memory"); rem -= 16; }
}

DI unsigned rt32() { unsigned long long t; asm volatile("s_memrealtime %0\n\ts_waitcnt lgkmcnt(0)" : "=s"(t) :: "memory"); return (unsigned)t; }
DI unsigned approach_rt(unsigned target, int lead) {
    int rem;
    for (;;) {
        rem = (int)(target - rt32());
        if (rem <= lead + 4) break;
        int n = (rem - lead) >> 2;
        while (n-- > 0) __builtin_amdgcn_s_sleep(1);
    }
    return now32() + (unsigned)(rem * 23 - 110);
}
DI void clock_ref(unsigned& re, unsigned& me) {
    unsigned long long m0, m1, r[12];
    asm volatile("s_memtime %0\n\t"
        "s_memrealtime %1\n\ts_nop 1\n\ts_memrealtime %2\n\ts_nop 1\n\ts_memrealtime %3\n\ts_nop 1\n\ts_memrealtime %4\n\ts_nop 1\n\t"
        "s_memrealtime %5\n\ts_nop 1\n\ts_memrealtime %6\n\ts_nop 1\n\ts_memrealtime %7\n\ts_nop 1\n\ts_memrealtime %8\n\ts_nop 1\n\t"
        "s_memrealtime %9\n\ts_nop 1\n\ts_memrealtime %10\n\ts_nop 1\n\ts_memrealtime %11\n\ts_nop 1\n\ts_memrealtime %12\n\ts_nop 1\n\t"
        "s_memtime %13\n\ts_waitcnt lgkmcnt(0)"
        : "=&s"(m0), "=&s"(r[0]), "=&s"(r[1]), "=&s"(r[2]), "=&s"(r[3]), "=&s"(r[4]), "=&s"(r[5]), "=&s"(r[6]), "=&s"(r[7]), "=&s"(r[8]), "=&s"(r[9]), "=&s"(r[10]), "=&s"(r[11]), "=&s"(m1) :: "memory");
    unsigned cnt = 0;
#pragma unroll
    for (int i = 1; i < 12; ++i) cnt += ((unsigned)r[i] == (unsigned)r[0]) ? 1u : 0u;
    re = (unsigned)r[0] + 4u;
    me = (unsigned)m0 + ((((unsigned)m1 - (unsigned)m0) * (2u * cnt + 1u) * 43u) >> 10);
}
DI void sleep_to(unsigned tc) {
    for (;;) {
        const int rem = (int)(tc - now32());
        if (rem <= 0) return;
        if (rem > 260) { int n = (rem - 160) / 96; while (n-- > 0) __builtin_amdgcn_s_sleep(1); }
    }
}
DI void spin_to(unsigned tc) { while ((int)(tc - now32()) > 0) {} }
DI void wait_rt(unsigned target) { spin_to(approach_rt(target, 10)); }
template <int NP, bool CTRL>
DI unsigned gather_m(__amdgpu_buffer_rsrc_t rs, unsigned sb, unsigned char* ldsm, int p0, int lane, int skipline = -1) {
    const unsigned o0 = p0 * 1024 + lane * 16;
    for (unsigned spin = 0;; ++spin) {
        asm volatile("" ::: "memory");
        v4u a[NP]; v2u c = {0, 0};
        bool sk[NP];
#pragma unroll
        for (int k = 0; k < NP; ++k) { sk[k] = (int)((o0 + k * 1024) >> 7) == skipline; a[k] = ld_sc1(rs, (o0 + k * 1024) ^ (sk[k] ? 128u : 0u), sb); }
        if (CTRL) c = ld2_sc1(rs, 7168u, sb);
#pragma unroll
        for (int k = 0; k < NP; ++k) *(v4u*)(ldsm + (sk[k] ? 7424u + (lane & 7) * 16 : o0 + k * 1024)) = a[k];
        if (CTRL && lane == 0) *(v2u*)(ldsm + 7168) = c;
        unsigned t;
        if (NP == 2) t = max(vmax4(0, a[0]), vmax4(0, a[1])); else t = vmax4(0, a[0]);
        if (CTRL) t = umax3(t, c.x, c.y);
        if (__all(t != SENT) || spin > SPIN_MAX) return spin;
        __builtin_amdgcn_s_sleep(1);
    }
}
template <int NP, bool CTRL>
DI unsigned gather_fast(__amdgpu_buffer_rsrc_t rs, unsigned sb, unsigned char* ldsm, const unsigned (&vo)[2], const unsigned (&lo)[2], int p0, int lane, int skipline) {
    asm volatile("; gather %0 %1" :: "i"(NP), "i"((int)CTRL) : "memory");
    v4u a0 = ld_sc1(rs, vo[0], sb), a1; v2u c;
    if (NP == 2) a1 = ld_sc1(rs, vo[1], sb);
    if (CTRL) c = ld2_sc1(rs, 7168u, sb);
    __builtin_amdgcn_sched_barrier(0);
    *(v4u*)(ldsm + lo[0]) = a0;
    unsigned t = vmax4(0, a0);
    if (NP == 2) { *(v4u*)(ldsm + lo[1]) = a1; t = vmax4(t, a1); }
    if (CTRL) { *(v2u*)(ldsm + lo[1]) = c; t = umax3(t, c.x, c.y); }
    if (__builtin_expect(__all(t != SENT), 1)) return 0;
    gather_m<NP, CTRL>(rs, sb, ldsm, p0, lane, skipline);
    return 1;
}
#define GADDR(vo, lo, p0, np, skl) { _Pragma("unroll") for (int k_ = 0; k_ < 2; ++k_) { const unsigned o_ = ((p0) + k_) * 1024 + lane * 16; const bool sk_ = (int)(o_ >> 7) == (skl); \
        vo[k_] = o_ ^ (sk_ ? 128u : 0u); lo[k_] = sk_ ? 7424u + (lane & 7) * 16 : o_; } if ((np) == 1) lo[1] = lane == 0 ? 7168u : 7552u + (lane & 7) * 8; }
#define LOADP(mf) v4u mf[7]; _Pragma("unroll") for (int q_ = 0; q_ < 7; ++q_) mf[q_] = *(const v4u*)(ldsm + q_ * 1024 + pfrag_off); __builtin_amdgcn_sched_barrier(0);
DI unsigned reduce8(const float* rd, int lane) {
    const float* q = rd + (lane >> 3) * 100 + (lane & 7) * 12;
    f4 a = *(const f4*)q, b = *(const f4*)(q + 4);
    a += b;
    float v = (a[0] + a[1]) + (a[2] + a[3]);
    float o = dpp_x1(v);
    unsigned r = (lane & 1) ? pk2(o, v) : pk2(v, o);
    return min(r, 0xFFFFFFFEu);
}
#define READCTRL() { SAVER1 v2u c_ = *(const v2u*)(ldsm + 7168); R2 = __builtin_amdgcn_readfirstlane(c_.x); R1n = __builtin_amdgcn_readfirstlane(c_.y); }

#define SAVER1
typedef short s4 __attribute__((ext_vector_type(4)));
DI f4 mfma16(v2u a, v2u b, f4 c) { return __builtin_amdgcn_mfma_f32_16x16x16bf16_1k(__builtin_bit_cast(s4, a), __builtin_bit_cast(s4, b), c, 0, 0, 0); }
DI float vmax(float a, float b) { float r; asm("s_nop 1\n\tv_max_f32 %0, %1, %2" : "=v"(r) : "v"(a), "v"(b)); return r; }
DI float vmax3(float a, float b, float c) { float r; asm("v_max3_f32 %0, %1, %2, %3" : "=v"(r) : "v"(a), "v"(b), "v"(c)); return r; }
DI float vmax3_mfma(float a, float b, float c) { float r; asm("s_nop 3\n\tv_max3_f32 %0, %1, %2, %3" : "=v"(r) : "v"(a), "v"(b), "v"(c)); return r; }
DI float rmax16(float x) { auto r = __builtin_amdgcn_permlane16_swap(asu(x), asu(x), false, false); unsigned a = r[0], b = r[1]; return vmax(asf(a), asf(b)); }
DI float rmax32(float x) { auto r = __builtin_amdgcn_permlane32_swap(asu(x), asu(x), false, false); unsigned a = r[0], b = r[1]; return vmax(asf(a), asf(b)); }
constexpr int HS = 1408;
constexpr int ST_OFF = 16 * HS;
constexpr int ZL_OFF = ST_OFF + 1024;
constexpr int L_M = 0, L_RED = 8192, L_RED2 = 12800, L_GM = 16128, L_VP = 17152, L_FL = 19200, L_UF = 20480;

DI unsigned gather_ctrl(__amdgpu_buffer_rsrc_t rs, unsigned sb, unsigned char* ldsm, int lane) {
    for (unsigned spin = 0;; ++spin) {
        asm volatile("" ::: "memory");
        v2u c = ld2_sc1(rs, 7168u, sb);
        if (lane == 0) *(v2u*)(ldsm + 7168) = c;
        if (__all(max(c.x, c.y) != SENT) || spin > SPIN_MAX) return spin;
        __builtin_amdgcn_s_sleep(1);
    }
}
#define REREF 0
#define RSLEEP 0
#define XCK 100
#define ASLEEP 2
#define HSLEEP 2
#define SB0 __builtin_amdgcn_sched_barrier(0);

DI void glds16(const unsigned char* gsrc, unsigned lds_dst) {
    unsigned keep;
    asm volatile("s_mov_b32 %0, m0\n\ts_mov_b32 m0, %2\n\ts_nop 0\n\tglobal_load_lds_dwordx4 %1, off sc1\n\ts_mov_b32 m0, %0" : "=&s"(keep) : "v"(gsrc), "s"(lds_dst) : "memory");
}
DI void glds16x4(const unsigned char* gsrc, unsigned lds_dst) {
    unsigned keep;
    asm volatile("s_mov_b32 %0, m0\n\ts_mov_b32 m0, %2\n\ts_nop 0\n\t"
                 "global_load_lds_dwordx4 %1, off sc1\n\tglobal_load_lds_dwordx4 %1, off offset:1024 sc1\n\t"
                 "global_load_lds_dwordx4 %1, off offset:2048 sc1\n\tglobal_load_lds_dwordx4 %1, off offset:3072 sc1\n\t"
                 "s_mov_b32 m0, %0" : "=&s"(keep) : "v"(gsrc), "s"(lds_dst) : "memory");
}
DI void glds4(const unsigned char* gsrc, unsigned lds_dst) {
    unsigned keep;
    asm volatile("s_mov_b32 %0, m0\n\ts_mov_b32 m0, %2\n\ts_nop 0\n\tglobal_load_lds_dword %1, off sc1\n\ts_mov_b32 m0, %0" : "=&s"(keep) : "v"(gsrc), "s"(lds_dst) : "memory");
}
DI unsigned ldflag(__amdgpu_buffer_rsrc_t rs, unsigned off) { return __builtin_amdgcn_raw_buffer_load_b32(rs, (unsigned)CTRL_OFF + off, 0, 16); }
DI unsigned wait_flags(__amdgpu_buffer_rsrc_t rs, unsigned off, unsigned need, int slp) {
    unsigned v;
    for (unsigned spin = 0;; ++spin) {
        asm volatile("" ::: "memory");
        v = ldflag(rs, off);
        if (__all(v >= need) || spin > 4 * SPIN_MAX) break;
        for (int i = 0; i < slp; ++i) __builtin_amdgcn_s_sleep(4);
    }
#pragma unroll
    for (int o = 32; o >= 1; o >>= 1) v = min(v, (unsigned)__shfl_xor((int)v, o));
    return __builtin_amdgcn_readfirstlane(v);
}

#define LDSV(off) (*(volatile __attribute__((address_space(3))) unsigned*)((__attribute__((address_space(3))) unsigned char*)lds + (off)))
DI void rec_role(unsigned char* ws, unsigned char* lds, const int role, int dS, int dV, int dVm) {
    const int tid = threadIdx.x, lane = tid & 63, w = __builtin_amdgcn_readfirstlane(tid >> 6);
    const int rho = lane & 15, g = lane >> 4;
    __amdgpu_buffer_rsrc_t rs = mkrs(ws, 0x7fffffff);
    unsigned char* ldsm = lds + L_M;
    float* red = (float*)(lds + L_RED);
    float* red2 = (float*)(lds + L_RED2);
    if (role == 112) {
        unsigned best = 0xffffffffu;
        for (int r = 0; r < 10; ++r) {
            const v4u z = {SENT, SENT, SENT, SENT};
            const unsigned off = (unsigned)((w >> 1) + 4 * (r & 1)) * 262144u + ((r >> 1) & 3) * 65536u + 7680u + ((r >> 3) + 2 * (r & 1)) * 128u + (w & 1) * 8192u;
            const unsigned t0 = now32();
            if (lane < 8) st_sc1(z, rs, off + lane * 16, 0);
            asm volatile("s_waitcnt vmcnt(0)" ::: "memory");
            const unsigned dt = now32() - t0;
            if (r > 0) best = min(best, dt);
        }
        if (lane == 0) ((unsigned*)lds)[w] = best;
        __syncthreads();
        if (tid == 0) {
            const unsigned* bl = (const unsigned*)lds;
            const unsigned b0 = (bl[0] + bl[2]) + (bl[4] + bl[6]), b1 = (bl[1] + bl[3]) + (bl[5] + bl[7]);
            __builtin_amdgcn_raw_buffer_store_b32(b1 < b0 ? 2u : 1u, rs, (unsigned)CTRL_OFF + C_POL, 0, 16);
        }
        __syncthreads();
    }
#define POLINIT() const unsigned polo = (wait_flags(rs, C_POL, 1, 1) - 1u) * 8192u; const unsigned myc = (unsigned)(role & 3) * 65536u + polo;
    if (role < 112) { for (int i = 0; i < RSLEEP; ++i) __builtin_amdgcn_s_sleep(16); }
    unsigned nfail = 0, nfail2 = 0;
    unsigned long long* dbg = (unsigned long long*)(ws + CTRL_OFF + C_DBG);
#define STAMP(cond, idx)
#define TL(idx)
#define GT(idx, ref)
#define BT(i) {}
    constexpr unsigned TOFF = 70, TOFF2 = 30;
#define CLKUPD() { unsigned r1_, m1_; clock_ref(r1_, m1_); const unsigned dr = r1_ - r0, dm = m1_ - m0; \
        if (dr > 60u && dr < TA + TB + 44u) { const int err = (int)(dm * 256u) - (int)(cq * dr); cq = (unsigned)min(max((int)cq + (err >> 8), 4096), 6400); } r0 = r1_; m0 = m1_; }
#define WAITR1() { unsigned tg_ = m0 - TOFF + (unsigned)(((int)(R1n - r0) * (int)cq) >> 8); \
        if (REREF && (int)(tg_ - now32()) > 700) { sleep_to(tg_ - 520u); unsigned ra_, ma_; clock_ref(ra_, ma_); tg_ = ma_ - TOFF + (unsigned)(((int)(R1n - ra_) * (int)cq) >> 8); } \
        sleep_to(tg_); }
    const unsigned pfrag_off = (rho >> 3) * 512 + g * 128 + (rho & 7) * 16;
    const unsigned TA = dS & 255, TB = dV & 255, TE = dVm & 255, TLATE = (dS >> 8) & 255, XUP = (dV >> 8) & 255, XDN = (dVm >> 8) & 255, X0 = (dVm >> 16) & 255;
    unsigned R2 = 0, R1n = 0;
    const bool is_mx = role >= 112;
    const bool is_v = !is_mx && (((role >> 2) & 1) == 0);
    const int j = (role >> 3) * 4 + (role & 3);
    const int h = role / 7, p = role % 7;
    const int hp = (lane >> 3) & 1, i8 = lane & 7;
    const v4u SENT4 = {SENT, SENT, SENT, SENT};

    if (is_mx) {
        const int hx = role - 112;
        POLINIT()
        if (w >= 4) {
            const unsigned orst = comm_off(1, hx * HS + 896 + (lane & 31) * 16, 0) + (lane >> 5) * 65536u + polo;
            const unsigned orst2 = comm_off(1, ST_OFF + (hx >> 1) * 128 + (hx & 1) * 64 + (lane & 1) * 16, 0) + ((lane >> 1) & 3) * 65536u + polo;
            const unsigned gfl2 = C_GU + 128u * (unsigned)min((14 * hx) / 4 + (lane & 7), (14 * hx + 13) / 4);
            unsigned kn = 0, gvo[2], glo[2];
            GADDR(gvo, glo, 2 * (w - 4), (w < 7 ? 2 : 1), -1)
            unsigned r0 = 0, m0 = 0, cq = 5900u;
            if (w == 7) clock_ref(r0, m0);
            if (w == 5 && lane == 0) LDSV(L_UF) = 0u;
            for (int step = 0; step < NSEG; ++step) {
                const unsigned sbr = comm_step_base(step, 0) + myc;
                if (step > 0 && w == 7) WAITR1()
                bar();
                if (w < 7) nfail += gather_fast<2, false>(rs, sbr, ldsm, gvo, glo, 2 * (w - 4), lane, -1);
                else nfail += gather_fast<1, true>(rs, sbr, ldsm, gvo, glo, 6, lane, -1);
                bar();
                READCTRL()
                bar();
                if (w == 4 && step >= 4) {
                    __builtin_amdgcn_s_sleep(4);
                    const unsigned sbz = comm_step_base(step + 4, 0);
                    st_sc1(SENT4, rs, orst, sbz); st_sc1(SENT4, rs, orst + 131072u, sbz);
                    if (lane < 8) st_sc1(SENT4, rs, orst2, sbz);
                }
                if (w == 7) CLKUPD()
                if (w == 5 && kn < 64u) {
                    __builtin_amdgcn_s_sleep(4);
                    kn = wait_flags(rs, gfl2, 0, 1);
                    if (lane == 0) LDSV(L_UF) = kn;
                }
            }
        } else {
            __builtin_amdgcn_s_setprio(2);
            const int tau = w & 1, kh = w >> 1;
            v4u wa[8], wb[8];
            const unsigned gbase = (unsigned)(G_OFF + ((size_t)hx * 2 + tau) * 14 * 1024 + kh * 8192) + lane * 16;
            const unsigned g67 = gbase + (kh ? 4096u : 6144u);
#define GLOAD(wt, sb_) { _Pragma("unroll") for (int ks = 0; ks < 6; ++ks) wt[ks] = ld_sc1(rs, gbase + ks * 1024, sb_); \
                     wt[6] = ld_sc1(rs, g67, sb_); wt[7] = ld_sc1(rs, g67 + 1024, sb_); }
            const unsigned gfl = C_GU + 128u * (unsigned)min((14 * hx) / 4 + (lane & 7), (14 * hx + 13) / 4);
            unsigned known = wait_flags(rs, gfl, 2, 1);
            GLOAD(wa, 0)
            GLOAD(wb, 458752)
            float* sc = red;
            const int tq = lane & 7, ir = lane >> 3;
            const unsigned oes = comm_off(1, hx * HS + 896 + lane * 8, 0) + w * 65536u + polo;
            const unsigned oss = comm_off(1, ST_OFF + (hx >> 1) * 128 + (hx & 1) * 64 + ir * 4, 0) + w * 65536u + polo;
#define MXGT
#define MXSTEP(wt, step) { \
                const bool last = ((step) == NSEG - 1); \
                const unsigned sbs = comm_step_base(step, 0); \
                __builtin_amdgcn_s_barrier(); \
                bar(); \
                f4 aE = {0, 0, 0, 0}, aO = {0, 0, 0, 0}; \
                v4u mf[4]; \
                _Pragma("unroll") for (int q = 0; q < 3; ++q) mf[q] = *(const v4u*)(ldsm + (4 * kh + q) * 1024 + pfrag_off); \
                mf[3] = *(const v4u*)(ldsm + 3 * 1024 + pfrag_off); \
                SB0 \
                _Pragma("unroll") for (int q = 0; q < 3; ++q) { aE = mfma(wt[2 * q], mf[q], aE); aO = mfma(wt[2 * q + 1], mf[q], aO); } \
                if (kh == 0) { aE = mfma(wt[6], mf[3], aE); aO = mfma(wt[7], mf[3], aO); } \
                f4 xs = hp ? aO : aE; \
                *(f4*)(sc + ((2 * kh + hp) * 8 + i8) * 36 + 16 * tau + 4 * g) = xs; \
                bar(); \
                const float* sp = sc + ir * 36 + 4 * tq; \
                f4 a0 = (*(const f4*)(sp) + *(const f4*)(sp + 288)) + (*(const f4*)(sp + 576) + *(const f4*)(sp + 864)); \
                float mx; \
                asm("v_max3_f32 %0, %1, %2, %3\n\tv_max_f32 %0, %0, %4\n\ts_nop 1\n\t" \
                    "v_max_f32_dpp %0, %0, %0 quad_perm:[1,0,3,2] row_mask:0xf bank_mask:0xf\n\ts_nop 1\n\t" \
                    "v_max_f32_dpp %0, %0, %0 quad_perm:[2,3,0,1] row_mask:0xf bank_mask:0xf\n\ts_nop 1\n\t" \
                    "v_max_f32_dpp %0, %0, %0 row_half_mirror row_mask:0xf bank_mask:0xf" \
                    : "=&v"(mx) : "v"(a0[0]), "v"(a0[1]), "v"(a0[2]), "v"(a0[3])); \
                if (!last) { \
                    unsigned e0 = pk2(__builtin_amdgcn_exp2f(a0[0] - mx), __builtin_amdgcn_exp2f(a0[1] - mx)); \
                    unsigned e1 = pk2(__builtin_amdgcn_exp2f(a0[2] - mx), __builtin_amdgcn_exp2f(a0[3] - mx)); \
                    v2u eo; eo.x = min(e0, 0xFFFFFFFEu); eo.y = min(e1, 0xFFFFFFFEu); \
                    st2_sc1(eo, rs, oes, sbs); \
                    if (tq == 1) __builtin_amdgcn_raw_buffer_store_b32(min(asu(mx), 0xFFFFFFFEu), rs, oss, sbs, 16); \
                    MXGT \
                } \
                { const int sn = min((step) + 2, NSEG - 1); \
                  known = max(known, (unsigned)__builtin_amdgcn_readfirstlane(LDSV(L_UF))); \
                  if (known < (unsigned)sn + 1) known = wait_flags(rs, gfl, sn + 1, 1); \
                  const unsigned sb = __builtin_amdgcn_readfirstlane(sn * 458752); \
                  GLOAD(wt, sb) } }
            for (int step = 0; step < NSEG; step += 2) {
                MXSTEP(wa, step)
                MXSTEP(wb, step + 1)
            }
        }
    } else if (w >= 4) {
        const int wv = w - 4;
        v4u wtv[2][14];
        if (is_v) {
            const unsigned char* src = ws + BS_OFF + ((size_t)j * 8 + 2 * wv) * 14 * 1024;
#pragma unroll
            for (int t = 0; t < 2; ++t)
#pragma unroll
                for (int f = 0; f < 14; ++f) wtv[t][f] = *(const v4u*)(src + (t * 14 + f) * 1024 + lane * 16);
        } else {
#pragma unroll
            for (int t = 0; t < 2; ++t)
#pragma unroll
                for (int f = 0; f < 14; ++f) wtv[t][f] = v4u{0, 0, 0, 0};
        }
        POLINIT()
        __builtin_amdgcn_s_waitcnt(0x0F70);
        const bool cond = is_v && j == 0;
        const bool slow = w >= 6;
        const unsigned ufl = C_GU + 128u * (56 + j);
        const unsigned char* usrc = ws + U_OFF + ((size_t)j * 8 + 4 * (wv & 1)) * 1024 + lane * 16;
        const unsigned udst = (unsigned)(size_t)(__attribute__((address_space(3))) unsigned char*)lds + L_UF + (wv & 1) * 4096;
#define UDMA(sg) { const unsigned char* p_ = usrc + (size_t)(sg) * 458752; const unsigned d_ = __builtin_amdgcn_readfirstlane(udst + ((sg) & 3) * 8192); \
            glds16x4(p_, d_); }
#define VPRIME() { \
                f4 e0 = {0, 0, 0, 0}, o0 = {0, 0, 0, 0}, e1 = {0, 0, 0, 0}, o1 = {0, 0, 0, 0}; \
                _Pragma("unroll") for (int q = 0; q < 7; ++q) { \
                    e0 = mfma(mf[q], wtv[0][2 * q], e0); o0 = mfma(mf[q], wtv[0][2 * q + 1], o0); \
                    e1 = mfma(mf[q], wtv[1][2 * q], e1); o1 = mfma(mf[q], wtv[1][2 * q + 1], o1); } \
                f4 cs; \
                _Pragma("unroll") for (int r = 0; r < 4; ++r) { \
                    auto sw = __builtin_amdgcn_permlane32_swap(asu(o0[r]), asu(e1[r]), false, false); \
                    unsigned d0 = sw[0], d1 = sw[1]; \
                    cs[r] = g < 2 ? e0[r] + asf(d1) : o1[r] + asf(d0); } \
                v2u vp; vp.x = pk2(cs[0], cs[1]); vp.y = pk2(cs[2], cs[3]); \
                *(v2u*)(lds + L_VP + wv * 512 + (2 * (g >> 1) + hp) * 128 + i8 * 16 + (g & 1) * 8) = vp; }
#define BYPROD(st_) { \
            if (w == 7) { v4u gmv = *(const v4u*)(lds + L_GM + lane * 16); \
                st_sc1(gmv, rs, (unsigned)(GM_OFF + ((size_t)h * 14 + 2 * p) * 512) + lane * 16, __builtin_amdgcn_readfirstlane((st_) * 16 * 14 * 512)); } \
            if (w == 6 && is_v) { v4u v0 = *(const v4u*)(lds + L_VP + lane * 16), v1 = *(const v4u*)(lds + L_VP + 1024 + lane * 16); \
                const unsigned sbv = __builtin_amdgcn_readfirstlane((st_) * 56 * 4 * 512); \
                st_sc1(v0, rs, (unsigned)(VP_OFF + (size_t)j * 2048) + lane * 16, sbv); \
                st_sc1(v1, rs, (unsigned)(VP_OFF + (size_t)j * 2048 + 1024) + lane * 16, sbv); } }
#define RESETS() if (w == 7 && step >= 4) { \
                    if (dorst) st_sc1(SENT4, rs, orst, comm_step_base(step + 4, 0)); \
                    if (cond && lane < 4) st_sc1(SENT4, rs, polo + 7168u + lane * 65536u, comm_step_base(step + 4, 0)); }
        const unsigned orst = (lane < 32 ? comm_off(1, h * HS + p * 128 + (lane & 7) * 16, 0) : (unsigned)(j * 128 + (lane & 7) * 16)) + ((lane >> 3) & 3) * 65536u + polo;
        const bool dorst = lane < 32 || is_v;
#define CSTORES() { \
                    if (cond && lane < 4) st_sc1(SENT4, rs, polo + 7168u + lane * 65536u, comm_step_base(step + 4, 0)); \
                    if (dorst) st_sc1(SENT4, rs, orst, comm_step_base(step + 4, 0)); \
                    v4u gmv = *(const v4u*)(lds + L_GM + lane * 16); \
                    st_sc1(gmv, rs, (unsigned)(GM_OFF + ((size_t)h * 14 + 2 * p) * 512) + lane * 16, __builtin_amdgcn_readfirstlane(step * 16 * 14 * 512)); }
        if (w == 7) {
            unsigned known = 0;
            unsigned r0, m0, cq = 5900u; clock_ref(r0, m0);
            if (is_v) { known = wait_flags(rs, ufl, 2, 1); UDMA(0) UDMA(1) asm volatile("s_waitcnt vmcnt(0)" ::: "memory"); }
            for (int step = 0; step < NSEG; ++step) {
                if (step > 0) WAITR1()
                bar();
                BT(0)
                CLKUPD()
                if (lane == 0) *(v4u*)(lds + L_FL + 512) = v4u{r0, m0 - TOFF2, cq, 0u};
                bar();
                __builtin_amdgcn_s_sleep(HSLEEP);
                READCTRL()
                LOADP(mf)
                bar();
                if (is_v) VPRIME()
                bar();
                asm volatile("s_waitcnt vmcnt(0)" ::: "memory");
                if (is_v && step + 3 < NSEG) UDMA(step + 2)
                if (is_v) bar();
                else CSTORES()
            }
            bar();
            asm volatile("s_waitcnt vmcnt(0)" ::: "memory");
            __builtin_amdgcn_s_barrier();
        } else if (w == 6) {
            unsigned known = 0;
            if (is_v) { known = wait_flags(rs, ufl, 2, 1); UDMA(0) UDMA(1) asm volatile("s_waitcnt vmcnt(0)" ::: "memory"); }
            const bool reporter = (role == 4);
            const unsigned char* fsrc = ws + CTRL_OFF + ufl;
            const unsigned fdst = __builtin_amdgcn_readfirstlane((unsigned)(size_t)(__attribute__((address_space(3))) unsigned char*)lds + L_FL);
            if (lane == 0) LDSV(L_FL) = 0u;
#define VPSTORE(st_) { v4u v0 = *(const v4u*)(lds + L_VP + lane * 16), v1 = *(const v4u*)(lds + L_VP + 1024 + lane * 16); \
                const unsigned sbv = __builtin_amdgcn_readfirstlane((st_) * 56 * 4 * 512); \
                st_sc1(v0, rs, (unsigned)(VP_OFF + (size_t)j * 2048) + lane * 16, sbv); \
                st_sc1(v1, rs, (unsigned)(VP_OFF + (size_t)j * 2048 + 1024) + lane * 16, sbv); }
            for (int step = 0; step < NSEG; ++step) {
                bar();
                if (is_v) {
                    asm volatile("s_waitcnt vmcnt(2)" ::: "memory");
                    __builtin_amdgcn_s_sleep(ASLEEP);
                    if (step + 3 < NSEG) {
                        if (known < (unsigned)step + 3) known = max(known, (unsigned)__builtin_amdgcn_readfirstlane(LDSV(L_FL)));
                        if (known < (unsigned)step + 3) known = wait_flags(rs, ufl, step + 3, 1);
                        UDMA(step + 2)
                    }
                    if (step > 0) VPSTORE(step - 1)
                    if (known < (unsigned)step + 6 && known < 64u && lane == 0) glds4(fsrc, fdst);
                }
                bar();
                __builtin_amdgcn_s_sleep(HSLEEP);
                LOADP(mf)
                bar();
                if (is_v) VPRIME()
                bar();
                if (reporter && lane == 0 && step >= 3) __builtin_amdgcn_raw_buffer_store_b32((unsigned)(step - 2), rs, (unsigned)CTRL_OFF + C_REC, 0, 16);
                if (is_v) { CSTORES() bar(); }
            }
            bar();
            if (is_v) VPSTORE(NSEG - 1)
            asm volatile("s_waitcnt vmcnt(0)" ::: "memory");
            __builtin_amdgcn_s_barrier();
        } else {
            unsigned extra = X0, calm = 0, ont = 0, gvo[2] = {0, 0}, glo[2] = {0, 0};
            if (cond && w == 5) {
                wait_flags(rs, ufl, 2, 1);
                unsigned t = rt32() + 120u;
                v2u c; c.x = min(t, 0xFFFFFFFEu); c.y = min(t + TB, 0xFFFFFFFEu);
                if (lane < KCOPY) st2_sc1(c, rs, polo + 7168u + lane * 65536u, 0);
                if (lane == 0) *(v2u*)(ldsm + 7168) = c;
            }
            const unsigned opub = polo + j * 128 + (lane >> 1) * 4 + (lane & 1) * 65536u;
            const unsigned opubS = comm_off(1, h * HS + p * 128 + (lane >> 1) * 4, 0) + (lane & 1) * 65536u + polo;
            for (int step = 0; step < NSEG; ++step) {
                const bool last = (step == NSEG - 1);
                const unsigned sbs = comm_step_base(step, 0);
                const unsigned sbr = sbs + myc;
                const unsigned sbn = comm_step_base(step + 1, 0);
                const int skl = (is_v && step > 0) ? j : -1;
                if (step < 2) GADDR(gvo, glo, w + 1, 1, skl)
                bar();
                if (w == 4 || cond) nfail += gather_fast<1, false>(rs, sbr, ldsm, gvo, glo, w + 1, lane, skl);
                else nfail += gather_fast<1, true>(rs, sbr, ldsm, gvo, glo, 6, lane, skl);
                bar();
                if (w == 5) BT(1)
                READCTRL()
                __builtin_amdgcn_s_sleep(HSLEEP);
                LOADP(mf)
                if (w == 4) __builtin_amdgcn_s_setprio(3);
                bar();
                if (w == 5) BT(2)
                if (w == 4) {
                    if (!last) {
                        unsigned o = reduce8(red, lane);
                        __builtin_amdgcn_raw_buffer_store_b32(o, rs, opubS, sbs, 16);
                        __builtin_amdgcn_raw_buffer_store_b32(o, rs, opubS + 131072u, sbs, 16);
                        BT(5)
                        if ((lane & 1) == 0) *(unsigned*)(lds + L_FL + 640 + (lane >> 1) * 4) = o;
                    }
                    __builtin_amdgcn_s_setprio(0);
                }
                if (is_v) {
                    VPRIME()
                    bar();
                    if (w == 5) BT(3)
                    unsigned xclk = 0;
                    if (cond && w == 5) {
                        const int cqv = (int)__builtin_amdgcn_readfirstlane(LDSV(L_FL + 520));
                        xclk = (unsigned)max((5950 - cqv) * XCK, 0) >> 13;
                    }
                    if (w == 4) __builtin_amdgcn_s_setprio(3);
                    bar();
                    if (w == 5 && !cond) BT(4)
                    if (!last) {
                        if (w == 4) {
                            unsigned o = reduce8(red2, lane);
                            __builtin_amdgcn_raw_buffer_store_b32(o, rs, opub, sbn, 16);
                            __builtin_amdgcn_raw_buffer_store_b32(o, rs, opub + 131072u, sbn, 16);
                            BT(6)
                            if ((lane & 1) == 0) *(unsigned*)(ldsm + j * 128 + (lane >> 1) * 4) = o;
                            __builtin_amdgcn_s_setprio(0);
                        }
                        if (cond && w == 5) {
                            const unsigned tp = rt32();
                            unsigned base = R1n;
                            if ((int)(tp + 4u - R1n) > 0) { base = tp + TLATE; if (ont) extra = min(extra + XUP, 10u); ont = 0; calm = 0; }
                            else { ont = 1; if (++calm >= XDN) { calm = 0; extra = extra ? extra - 1u : 0u; } }
                            const unsigned ex = extra + xclk;
                            v2u c; c.x = min(base + TA + ex, 0xFFFFFFFEu); c.y = min(base + TA + TB + 2u * ex, 0xFFFFFFFEu);
                            if (lane < KCOPY) st2_sc1(c, rs, polo + 7168u + lane * 65536u, sbn);
                            if (lane == 0) *(v2u*)(ldsm + 7168) = c;
                        }
                    }
                } else bar();
            }
            bar();
            __builtin_amdgcn_s_barrier();
            if (w == 4 && lane == 0) __builtin_amdgcn_raw_buffer_store_b32(1u, rs, (unsigned)CTRL_OFF + C_DN2 + role * 4, 0, 16);
        }
    } else {
        __builtin_amdgcn_s_setprio(2);
        const int b = 2 * p + (w >> 1), t = w & 1;
        v4u wt[14];
        {
            const unsigned char* src = ws + AS_OFF + ((size_t)(h * 14 + b) * 2 + t) * 14 * 1024;
#pragma unroll
            for (int ks = 0; ks < 14; ++ks) wt[ks] = *(const v4u*)(src + ks * 1024 + lane * 16);
        }
        POLINIT()
        __builtin_amdgcn_s_waitcnt(0x0F70);
        unsigned op[2][2], oe[2], ost, gvo[2] = {0, 0}, glo[2] = {0, 0};
        v4u pf[2][2], ef[2]; unsigned stt;
#pragma unroll
        for (int a = 0; a < 2; ++a) {
            const int hd = 4 * w + 2 * a + hp;
#pragma unroll
            for (int u = 0; u < 2; ++u) {
                const int q = 4 * u + g;
                const bool z = (q == 7) || (hd == h && q == p);
                op[a][u] = z ? 0x80000000u : comm_off(1, hd * HS + q * 128 + i8 * 16, 0);
            }
            oe[a] = comm_off(1, hd * HS + 896 + i8 * 64 + g * 16, 0);
        }
        const int hl = 4 * w + 2 * (g >> 1) + hp;
        ost = comm_off(1, ST_OFF + (hl >> 1) * 128 + (hl & 1) * 64 + i8 * 4, 0);
        const bool ownhead = (hl == h);
        v4u selLo = {0, 0, 0, 0}, selHi = {0, 0, 0, 0}, oneLo, oneHi;
        {
            const unsigned one = 0x3F80u << (16 * (rho & 1));
            const int wd = (rho & 7) >> 1;
            v4u sv; sv.x = wd == 0 ? one : 0; sv.y = wd == 1 ? one : 0; sv.z = wd == 2 ? one : 0; sv.w = wd == 3 ? one : 0;
            if (rho < 8) selLo = sv; else selHi = sv;
            const unsigned o2 = 0x3F803F80u;
            oneLo = rho < 8 ? v4u{o2, o2, o2, o2} : v4u{0, 0, 0, 0};
            oneHi = rho < 8 ? v4u{0, 0, 0, 0} : v4u{o2, o2, o2, o2};
        }
        for (int step = 0; step < NSEG; ++step) {
            const bool last = (step == NSEG - 1);
            const unsigned sbs = comm_step_base(step, 0);
            const unsigned sbr = sbs + myc;
            STAMP(step == 1, 0); STAMP(step == 63, 1);
            if (step < 2) GADDR(gvo, glo, (w == 0 ? 0 : w + 1), 2, ((is_v && step > 0) ? j : -1))
            bar();
            TL(0)
            const int skl = (is_v && step > 0) ? j : -1;
            if (w == 0) nfail += gather_fast<2, false>(rs, sbr, ldsm, gvo, glo, 0, lane, skl);
            else nfail += gather_fast<1, false>(rs, sbr, ldsm, gvo, glo, w + 1, lane, skl);
            bar();
            TL(1)
            f4 aE = {0, 0, 0, 0}, aO = {0, 0, 0, 0};
            v4u mf[7];
#pragma unroll
            for (int q_ = 0; q_ < 7; ++q_) mf[q_] = *(const v4u*)(ldsm + q_ * 1024 + pfrag_off);
            v2u mb = *(const v2u*)(ldsm + b * 512 + g * 128 + i8 * 16 + t * 8);
            const unsigned cwx = *(const unsigned*)(ldsm + 7168);
            const v4u ckr = *(const v4u*)(lds + L_FL + 512);
            SB0
#pragma unroll
            for (int q = 0; q < 7; ++q) { aE = mfma(wt[2 * q], mf[q], aE); aO = mfma(wt[2 * q + 1], mf[q], aO); }
            f4 xs = hp ? aO : aE;
            v2u gh; gh.x = pk2(xs[0], xs[1]); gh.y = pk2(xs[2], xs[3]);
            f4 sp = mfma16(gh, mb, f4{0, 0, 0, 0});
            if (rho < 8) {
                float* rq = red + rho * 100 + (4 * (g & 1)) * 12 + 2 * w + (g >> 1);
                rq[0] = sp[0]; rq[12] = sp[1]; rq[24] = sp[2]; rq[36] = sp[3];
            }
            SB0
            TL(7)
            bar();
#define GMSTAGE() { f4 fs; _Pragma("unroll") for (int r = 0; r < 4; ++r) fs[r] = xs[r] + dpp_xor8(xs[r]); \
                v2u gm; gm.x = pk2(fs[0], fs[1]); gm.y = pk2(fs[2], fs[3]); \
                if (rho < 8) *(v2u*)(lds + L_GM + (w >> 1) * 512 + g * 128 + rho * 16 + t * 8) = gm; }
            if (!is_v) { GMSTAGE() bar(); continue; }
            if (last) { GMSTAGE() bar(); bar(); continue; }
#define E_LOADS(sb_) { stt = __builtin_amdgcn_raw_buffer_load_b32(rs, ost, sb_, 16); ef[0] = ld_sc1(rs, oe[0], sb_); ef[1] = ld_sc1(rs, oe[1], sb_); SB0 }
#define P_LOADS(sb_) { pf[0][0] = ld_sc1(rs, op[0][0], sb_); SB0 pf[1][0] = ld_sc1(rs, op[1][0], sb_); SB0 pf[0][1] = ld_sc1(rs, op[0][1], sb_); SB0 pf[1][1] = ld_sc1(rs, op[1][1], sb_); SB0 }
            const unsigned tc2 = (unsigned)__builtin_amdgcn_readfirstlane(ckr.y) + (unsigned)(((int)(__builtin_amdgcn_readfirstlane(cwx) - __builtin_amdgcn_readfirstlane(ckr.x)) * (int)__builtin_amdgcn_readfirstlane(ckr.z)) >> 8);
            spin_to(tc2 - TE * 23u);
            E_LOADS(sbr)
            asm volatile("" ::: "memory");
            spin_to(tc2);
            TL(3)
            P_LOADS(sbr)
            asm volatile("" ::: "memory");
            SB0
            if (w == 1) BT(7)
            GMSTAGE()
            v4u uf[2];
            uf[0] = *(const v4u*)(lds + L_UF + (step & 3) * 8192 + w * 2048 + lane * 16); uf[1] = *(const v4u*)(lds + L_UF + (step & 3) * 8192 + w * 2048 + 1024 + lane * 16);
            bar();
            v2u vpA, vpB;
            f4 sini = {0, 0, 0, 0};
            {
                v2u vp = *(const v2u*)(lds + L_VP + w * 512 + (2 * (g >> 1) + hp) * 128 + i8 * 16 + (g & 1) * 8);
                v2u ow = *(const v2u*)(lds + L_FL + 640 + i8 * 16 + (g & 1) * 8);
                vpA = g < 2 ? vp : v2u{0, 0};
                vpB = g < 2 ? v2u{0, 0} : vp;
                if (ownhead) { sini[0] = bflo(ow.x); sini[1] = bfhi(ow.x); sini[2] = bflo(ow.y); sini[3] = bfhi(ow.y); }
            }
            asm volatile("" ::: "memory");
            SB0
            TL(4)
            bool okall;
            auto combine = [&]() __attribute__((always_inline)) {
                f4 ls = mfma(oneLo, ef[0], f4{0, 0, 0, 0});
                ls = mfma(oneHi, ef[1], ls);
                f4 x0 = mfma(uf[0], ef[0], f4{0, 0, 0, 0});
                f4 x1 = mfma(uf[1], ef[1], f4{0, 0, 0, 0});
                unsigned t2 = vmax4(vmax4(stt, ef[0]), ef[1]);
                float lsv = ls[0];
                asm volatile("" : "+v"(t2), "+v"(lsv));
                SB0
                f4 sacc = mfma(selLo, pf[0][0], sini);
                sacc = mfma(selHi, pf[1][0], sacc);
                sacc = mfma(selLo, pf[0][1], sacc);
                sacc = mfma(selHi, pf[1][1], sacc);
                SB0
                unsigned t0 = vmax4(vmax4(t2, pf[0][0]), pf[0][1]), t1 = vmax4(vmax4(0, pf[1][0]), pf[1][1]);
                asm volatile("" : "+v"(t0), "+v"(t1));
                SB0
                const float mloc = asf(stt);
                float mx = vmax3(vmax3_mfma(sacc[0], sacc[1], sacc[2]), sacc[3], mloc);
                mx = rmax16(mx);
                float p0 = __builtin_amdgcn_exp2f(sacc[0] - mx), p1 = __builtin_amdgcn_exp2f(sacc[1] - mx), p2 = __builtin_amdgcn_exp2f(sacc[2] - mx), p3 = __builtin_amdgcn_exp2f(sacc[3] - mx);
                float f = __builtin_amdgcn_exp2f(mloc - mx);
                v2u pb; pb.x = pk2(p0, p1); pb.y = pk2(p2, p3);
                float zq = (p0 + p1) + (p2 + p3);
                SB0
                f4 cmA = mfma16(vpA, pb, f4{0, 0, 0, 0});
                f4 cmB = mfma16(vpB, pb, f4{0, 0, 0, 0});
                SB0
                float z = xsum16(zq);
                auto fs = __builtin_amdgcn_permlane32_swap(asu(f), asu(f), false, false);
                unsigned fa = fs[0], fb = fs[1];
                z += f * lsv;
                float rz = __builtin_amdgcn_rcpf(z);
                okall = __all(max(t0, t1) != SENT);
                f4 ya = x0 * asf(fa) + cmA, yb = x1 * asf(fb) + cmB;
                auto rs2 = __builtin_amdgcn_permlane32_swap(asu(rz), asu(rz), false, false);
                unsigned ra = rs2[0], rb = rs2[1];
                f4 cx = ya * asf(ra) + yb * asf(rb);
                if (hp == (g >> 1)) {
                    float* rq = red2 + i8 * 100 + (4 * (g & 1)) * 12 + 2 * w + hp;
                    rq[0] = cx[0]; rq[12] = cx[1]; rq[24] = cx[2]; rq[36] = cx[3];
                }
            };
            combine();
            if (!okall) {
                nfail2++;
                for (unsigned spin = 0; spin < SPIN_MAX && !okall; ++spin) {
                    __builtin_amdgcn_s_sleep(1);
                    asm volatile("" ::: "memory");
                    E_LOADS(sbr)
                    P_LOADS(sbr)
                    combine();
                }
            }
            TL(6)
            bar();
        }
        bar();
        __builtin_amdgcn_s_barrier();
    }
}

DI void signal_add(unsigned char* ws, unsigned off) {
    asm volatile("s_waitcnt vmcnt(0)" ::: "memory");
    __syncthreads();
    if (threadIdx.x == 0) __hip_atomic_fetch_add((unsigned*)(ws + CTRL_OFF + off), 1u, __ATOMIC_RELAXED, __HIP_MEMORY_SCOPE_AGENT);
}
DI void wg_wait(__amdgpu_buffer_rsrc_t rs, unsigned off_lane, unsigned need, int slp) {
    if (threadIdx.x < 64) wait_flags(rs, off_lane, need, slp);
    __syncthreads();
}

DI void gldsx16(const unsigned char* gsrc, unsigned lds_dst) {
    unsigned keep;
    asm volatile("s_mov_b32 %0, m0\n\ts_mov_b32 m0, %2\n\ts_nop 0\n\tglobal_load_lds_dwordx4 %1, off\n\ts_mov_b32 m0, %0" : "=&s"(keep) : "v"(gsrc), "s"(lds_dst) : "memory");
}
DI void gu_role(unsigned char* ws, unsigned char* lds, const int gw) {
    const int tid = threadIdx.x, lane = tid & 63, w8 = __builtin_amdgcn_readfirstlane(tid >> 6);
    const int w = w8 & 3, sp = w8 >> 2;
    const bool isG = gw < 56;
    const unsigned ldsb = __builtin_amdgcn_readfirstlane((unsigned)(size_t)(__attribute__((address_space(3))) unsigned char*)lds);
    const int nat0 = (((lane & 15) >> 2) & 1) * 14464 + ((4 * ((lane & 15) >> 3) + (lane & 3)) + 16 * (lane >> 4)) * 16;
    const unsigned char* xsrc = ws + XF_OFF + w8 * 1024 + lane * 16;
#define XDMA(pr_) { _Pragma("unroll") for (int i = 0; i < 7; ++i) gldsx16(xsrc + (size_t)(pr_) * 57344 + i * 8192, ldsb + ((pr_) & 1) * 57856 + (i * 8 + w8) * 1024 + ((i * 8 + w8) / 14) * 128); }
    XDMA(0)
    v4u wt[2][14];
    const unsigned char* wsrc = isG ? ws + AS_OFF + (size_t)(4 * gw + w) * 28 * 1024 : ws + BS_OFF + ((size_t)(gw - 56) * 8 + 2 * w) * 14 * 1024;
    unsigned char* xch = lds + 57856;
    v4u tmp[14];
#pragma unroll
    for (int ks = 0; ks < 14; ++ks) tmp[ks] = *(const v4u*)(wsrc + (sp * 14 + ks) * 1024 + lane * 16);
    SB0
#pragma unroll
    for (int half = 0; half < 2; ++half) {
        if (half) bar();
#pragma unroll
        for (int ks = 0; ks < 7; ++ks) *(v4u*)(xch + (w8 * 7 + ks) * 1024 + lane * 16) = tmp[half * 7 + ks];
        bar();
#pragma unroll
        for (int t = 0; t < 2; ++t)
#pragma unroll
            for (int ks = 0; ks < 7; ++ks) wt[t][half * 7 + ks] = *(const v4u*)(xch + ((w + 4 * t) * 7 + ks) * 1024 + lane * 16);
    }
    __amdgpu_buffer_rsrc_t rs = mkrs(ws, 0x7fffffff);
    asm volatile("s_waitcnt vmcnt(0)" ::: "memory");
    bar();
    const int bb = 4 * gw + w, hh = bb / 14, bq = bb % 14;
    const unsigned obase = isG ? (unsigned)(G_OFF + ((size_t)(hh * 2) * 14 + bq) * 1024) + lane * 16 : (unsigned)(U_OFF + ((size_t)(gw - 56) * 8 + 2 * w) * 1024) + lane * 16;
    const unsigned ostep = isG ? 14336u : 1024u;
    for (int pr = 0; pr < 32; ++pr) {
        const unsigned char* cur = lds + (pr & 1) * 57856 + sp * 28928;
        if (pr + 1 < 32) XDMA(pr + 1)
        f4 acc[2][2] = {{f4{0,0,0,0}, f4{0,0,0,0}}, {f4{0,0,0,0}, f4{0,0,0,0}}};
        {
            const unsigned char* p0 = cur + (isG ? nat0 : lane * 16);
            const unsigned char* p1 = p0 + (isG ? 128 : 14464);
            v4u xa[3], xb[3];
#pragma unroll
            for (int k = 0; k < 3; ++k) { xa[k] = *(const v4u*)(p0 + k * 1024); xb[k] = *(const v4u*)(p1 + k * 1024); }
            if (isG) {
#pragma unroll
                for (int ks = 0; ks < 14; ++ks) {
                    const v4u x0 = xa[ks % 3], x1 = xb[ks % 3];
                    acc[0][0] = mfma(wt[0][ks], x0, acc[0][0]); acc[1][0] = mfma(wt[1][ks], x0, acc[1][0]);
                    acc[0][1] = mfma(wt[0][ks], x1, acc[0][1]); acc[1][1] = mfma(wt[1][ks], x1, acc[1][1]);
                    if (ks + 3 < 14) { xa[ks % 3] = *(const v4u*)(p0 + (ks + 3) * 1024); xb[ks % 3] = *(const v4u*)(p1 + (ks + 3) * 1024); }
                    SB0
                }
            } else {
#pragma unroll
                for (int ks = 0; ks < 14; ++ks) {
                    const v4u x0 = xa[ks % 3], x1 = xb[ks % 3];
                    acc[0][0] = mfma(x0, wt[0][ks], acc[0][0]); acc[1][0] = mfma(x1, wt[0][ks], acc[1][0]);
                    acc[0][1] = mfma(x0, wt[1][ks], acc[0][1]); acc[1][1] = mfma(x1, wt[1][ks], acc[1][1]);
                    if (ks + 3 < 14) { xa[ks % 3] = *(const v4u*)(p0 + (ks + 3) * 1024); xb[ks % 3] = *(const v4u*)(p1 + (ks + 3) * 1024); }
                    SB0
                }
            }
        }
        const unsigned so = __builtin_amdgcn_readfirstlane((2 * pr + sp) * 458752);
        st_sc1(pack8(acc[0][0], acc[1][0]), rs, obase, so);
        st_sc1(pack8(acc[0][1], acc[1][1]), rs, obase + ostep, so);
        const bool drain = (pr < 4) || (pr == 31);
        if (drain) asm volatile("s_waitcnt vmcnt(0)" ::: "memory");
        else asm volatile("s_waitcnt vmcnt(2)" ::: "memory");
        bar();
        if (tid == 448) __builtin_amdgcn_raw_buffer_store_b32((unsigned)(drain ? 2 * pr + 2 : 2 * pr), rs, (unsigned)CTRL_OFF + C_GU + 128u * gw, 0, 16);
    }
}

DI void a0_prep(unsigned char* ws, const int u, const u16* __restrict__ wqkv, const u16* __restrict__ wphi, const u16* __restrict__ awo) {
    const int tid = threadIdx.x, lane = tid & 63, w = __builtin_amdgcn_readfirstlane(tid >> 6);
    const int rho = lane & 15, g = lane >> 4;
    __amdgpu_buffer_rsrc_t rs = mkrs(ws, 0x7fffffff);
    {
        const int which = w >> 2, h2 = u;
        v4u bf[2];
#pragma unroll
        for (int t = 0; t < 2; ++t) {
            const int f = perm32(t, rho);
            unsigned e[8];
#pragma unroll
            for (int jj = 0; jj < 8; ++jj) { const int d = 8 * g + jj; e[jj] = (d < 28 && f < 28) ? wphi[(h2 * 28 + d) * 28 + f] : 0u; }
            bf[t] = v4u{e[0] | (e[1] << 16), e[2] | (e[3] << 16), e[4] | (e[5] << 16), e[6] | (e[7] << 16)};
        }
        v4u af[4][2];
#pragma unroll
        for (int i = 0; i < 4; ++i) {
            const int ks = min((w & 3) + 4 * i, 13);
#pragma unroll
            for (int t = 0; t < 2; ++t) {
                const u16* p = wqkv + (size_t)(32 * ks + perm32(t, rho)) * 1344 + which * 448 + h2 * 28 + g * 8;
                v2u lo = *(const v2u*)p, hi = {0, 0};
                if (g < 3) hi = *(const v2u*)(p + 4);
                af[i][t] = v4u{lo.x, lo.y, hi.x, hi.y};
            }
        }
#pragma unroll
        for (int i = 0; i < 4; ++i) {
            const int ks = (w & 3) + 4 * i;
            if (ks < 14) {
#pragma unroll
                for (int t = 0; t < 2; ++t) {
                    f4 d0 = mfma(af[i][0], bf[t], f4{0, 0, 0, 0}), d1 = mfma(af[i][1], bf[t], f4{0, 0, 0, 0});
                    st_sc1(pack8(d0 * LOG2E, d1 * LOG2E), rs, (unsigned)(WQF_OFF + which * WQF_ONE + ((size_t)(h2 * 2 + t) * 14 + ks) * 1024) + lane * 16, 0);
                }
            }
        }
    }
#pragma unroll
    for (int i = 0; i < 4; ++i) {
        const int fi = w + 8 * i;
        if (fi < 28) {
            const int t = fi / 14, ks = fi % 14;
            const int d = 16 * t + rho;
            unsigned e[8], e2[8];
            const int cb = fi >> 1, t2 = fi & 1; const int c = 32 * cb + perm32(t2, rho);
#pragma unroll
            for (int jj = 0; jj < 8; ++jj) {
                e[jj] = d < 28 ? wqkv[(size_t)(32 * ks + 8 * g + jj) * 1344 + 896 + u * 28 + d] : 0u;
                const int sl = 8 * g + jj; e2[jj] = sl < 28 ? awo[(size_t)(u * 28 + sl) * 448 + c] : 0u;
            }
            st_sc1(v4u{e[0] | (e[1] << 16), e[2] | (e[3] << 16), e[4] | (e[5] << 16), e[6] | (e[7] << 16)}, rs, (unsigned)(WQF_OFF + 2 * WQF_ONE + (size_t)(u * 28 + fi) * 1024) + lane * 16, 0);
            st_sc1(v4u{e2[0] | (e2[1] << 16), e2[2] | (e2[3] << 16), e2[4] | (e2[5] << 16), e2[6] | (e2[7] << 16)}, rs, (unsigned)(WOF_OFF + (size_t)((cb * 2 + t2) * 16 + u) * 1024) + lane * 16, 0);
        }
    }
}
DI void phi_pair(f4 d0, f4 d1, int g, v4u& pos, v4u& neg) {
    float v[8] = {d0[0], d0[1], d0[2], d0[3], d1[0], d1[1], d1[2], d1[3]};
    const bool tail = (g == 3);
    float mx = fmaxf(fmaxf(v[0], v[1]), fmaxf(v[2], v[3])), mn = fminf(fminf(v[0], v[1]), fminf(v[2], v[3]));
    float mx2 = fmaxf(fmaxf(v[4], v[5]), fmaxf(v[6], v[7])), mn2 = fminf(fminf(v[4], v[5]), fminf(v[6], v[7]));
    mx = tail ? mx : fmaxf(mx, mx2); mn = tail ? mn : fminf(mn, mn2);
    mx = xmax32(xmax16(mx)); mn = -xmax32(xmax16(-mn));
    float ep[8], en[8], sp = 0.f, sn = 0.f;
#pragma unroll
    for (int k = 0; k < 8; ++k) {
        ep[k] = __builtin_amdgcn_exp2f(v[k] - mx); en[k] = __builtin_amdgcn_exp2f(mn - v[k]);
        if (k >= 4) { ep[k] = tail ? 0.f : ep[k]; en[k] = tail ? 0.f : en[k]; }
        sp += ep[k]; sn += en[k];
    }
    sp = xsum32(xsum16(sp)); sn = xsum32(xsum16(sn));
    const float rp = __builtin_amdgcn_rcpf(sp), rn = __builtin_amdgcn_rcpf(sn);
    pos = v4u{pk2(ep[0] * rp, ep[1] * rp), pk2(ep[2] * rp, ep[3] * rp), pk2(ep[4] * rp, ep[5] * rp), pk2(ep[6] * rp, ep[7] * rp)};
    neg = v4u{pk2(en[0] * rn, en[1] * rn), pk2(en[2] * rn, en[3] * rn), pk2(en[4] * rn, en[5] * rn), pk2(en[6] * rn, en[7] * rn)};
}
DI void stage_x64(const u16* __restrict__ ax, unsigned char* lds, int blk64) {
    int tid = threadIdx.x; asm volatile("" : "+v"(tid));
    const int lane = tid & 63, w = tid >> 6;
    v4u stg[7];
#pragma unroll
    for (int i = 0; i < 7; ++i) {
        const int f = i * 8 + w; const int q = f / 14, ks = f % 14;
        stg[i] = *(const v4u*)(ax + (size_t)(blk64 * 64 + 32 * (q >> 1) + perm32(q & 1, lane & 15)) * D + ks * 32 + (lane >> 4) * 8);
    }
#pragma unroll
    for (int i = 0; i < 7; ++i) *(v4u*)(lds + (i * 512 + tid) * 16) = stg[i];
}

DI void a1_unit(unsigned char* ws, unsigned char* lds, const int u, const u16* __restrict__ ax) {
    const int tid = threadIdx.x, lane = tid & 63, w = __builtin_amdgcn_readfirstlane(tid >> 6);
    const int rho = lane & 15, g = lane >> 4;
    __amdgpu_buffer_rsrc_t rs = mkrs(ws, 0x7fffffff);
    unsigned char* T = lds + 57344 + w * 9216;
    constexpr int TS = 144;
    f4 kv[2][4][2];
#pragma unroll
    for (int a = 0; a < 2; ++a)
#pragma unroll
        for (int b = 0; b < 4; ++b) { kv[a][b][0] = f4{0,0,0,0}; kv[a][b][1] = f4{0,0,0,0}; }
    for (int pass = 0; pass < 2; ++pass) {
        __syncthreads();
        stage_x64(ax, lds, 2 * u + pass);
        __syncthreads();
#pragma unroll
        for (int hi = 0; hi < 2; ++hi) {
            const int h = 2 * w + hi;
            const unsigned wk = (unsigned)(WQF_OFF + WQF_ONE + (size_t)h * 28672) + lane * 16;
            const unsigned wv = (unsigned)(WQF_OFF + 2 * WQF_ONE + (size_t)h * 28672) + lane * 16;
            f4 ak[2][4], av[2][4];
#pragma unroll
            for (int t = 0; t < 2; ++t)
#pragma unroll
                for (int q = 0; q < 4; ++q) { ak[t][q] = f4{0,0,0,0}; av[t][q] = f4{0,0,0,0}; }
#pragma unroll 2
            for (int ks = 0; ks < 14; ++ks) {
                v4u k0 = ld_sc1(rs, wk + ks * 1024, 0), k1 = ld_sc1(rs, wk + (14 + ks) * 1024, 0);
                v4u v0 = ld_sc1(rs, wv + ks * 1024, 0), v1 = ld_sc1(rs, wv + (14 + ks) * 1024, 0);
#pragma unroll
                for (int q = 0; q < 4; ++q) {
                    v4u x = *(const v4u*)(lds + (q * 14 + ks) * 1024 + lane * 16);
                    ak[0][q] = mfma(k0, x, ak[0][q]); ak[1][q] = mfma(k1, x, ak[1][q]);
                    av[0][q] = mfma(x, v0, av[0][q]); av[1][q] = mfma(x, v1, av[1][q]);
                }
            }
#pragma unroll
            for (int q = 0; q < 4; ++q) {
                v4u pos, neg; phi_pair(ak[0][q], ak[1][q], g, pos, neg);
                const int tok = 32 * (q >> 1) + perm32(q & 1, rho);
                *(v4u*)(T + tok * TS + g * 16) = pos;
                *(v4u*)(T + tok * TS + 64 + g * 16) = neg;
            }
            asm volatile("s_waitcnt lgkmcnt(0)" ::: "memory");
            v4u vf[2][2];
#pragma unroll
            for (int bk = 0; bk < 2; ++bk)
#pragma unroll
                for (int t = 0; t < 2; ++t) {
                    vf[bk][t] = pack8(av[t][2 * bk], av[t][2 * bk + 1]);
                    if (t == 1 && rho == 12) vf[bk][t] = v4u{0x3F803F80u, 0x3F803F80u, 0x3F803F80u, 0x3F803F80u};
                }
            const unsigned trb = (8 * g + (rho >> 2)) * TS + (lane & 3) * 8;
#pragma unroll
            for (int bk = 0; bk < 2; ++bk)
#pragma unroll
                for (int ft = 0; ft < 4; ++ft) {
                    v2u lo = trread(T + trb + (32 * bk) * TS + ft * 32), hi2 = trread(T + trb + (32 * bk + 4) * TS + ft * 32);
                    v4u pa = {lo.x, lo.y, hi2.x, hi2.y};
                    kv[hi][ft][0] = mfma(pa, vf[bk][0], kv[hi][ft][0]);
                    kv[hi][ft][1] = mfma(pa, vf[bk][1], kv[hi][ft][1]);
                }
            asm volatile("s_waitcnt lgkmcnt(0)" ::: "memory");
        }
    }
#pragma unroll
    for (int hi = 0; hi < 2; ++hi)
#pragma unroll
        for (int ft = 0; ft < 4; ++ft)
#pragma unroll
            for (int dt = 0; dt < 2; ++dt)
                st_sc1(__builtin_bit_cast(v4u, kv[hi][ft][dt]), rs, (unsigned)(KVP_OFF + ((size_t)(u * 16 + 2 * w + hi) * 8 + ft * 2 + dt) * 1024) + lane * 16, 0);
}
DI void a15_unit(unsigned char* ws, unsigned char* lds, const int h) {
    const int tid = threadIdx.x, lane = tid & 63, w = tid >> 6;
    __amdgpu_buffer_rsrc_t rs = mkrs(ws, 0x7fffffff);
    f4 s = {0, 0, 0, 0};
    v4u t[16];
#pragma unroll
    for (int u = 0; u < 16; ++u) t[u] = ld_sc1(rs, (unsigned)(KVP_OFF + ((size_t)(u * 16 + h) * 8) * 1024) + tid * 16, 0);
#pragma unroll
    for (int u = 0; u < 16; ++u) s += __builtin_bit_cast(f4, t[u]);
    const int ft = w >> 1, dt = w & 1, rho = lane & 15, g = lane >> 4;
    __syncthreads();
    v2u o; o.x = pk2(s[0], s[1]); o.y = pk2(s[2], s[3]);
    *(v2u*)(lds + (16 * dt + rho) * 128 + (16 * ft + 4 * g) * 2) = o;
    __syncthreads();
    if (tid < 256) {
        const int fr = tid >> 6;
        const int tt = fr >> 1, kb = fr & 1;
        v4u v = *(const v4u*)(lds + perm32(tt, rho) * 128 + (32 * kb + 8 * g) * 2);
        st_sc1(v, rs, (unsigned)(KVF_OFF + (size_t)(h * 4 + fr) * 1024) + lane * 16, 0);
    }
}
DI void a2_unit(unsigned char* ws, unsigned char* lds, const int u, u16* __restrict__ out, const u16* __restrict__ ax) {
    int tid = threadIdx.x; asm volatile("" : "+v"(tid));
    const int lane = tid & 63, w = __builtin_amdgcn_readfirstlane(tid >> 6);
    const int rho = lane & 15, g = lane >> 4;
    __amdgpu_buffer_rsrc_t rs = mkrs(ws, 0x7fffffff);
    unsigned char* O = lds + 57344;
    __syncthreads();
    stage_x64(ax, lds, u);
    wg_wait(rs, C_A0, 16, 4);
    v4u pos[2][4], neg[2][4];
#pragma unroll
    for (int hi = 0; hi < 2; ++hi) {
        const int h = 2 * w + hi;
        const unsigned wq = (unsigned)(WQF_OFF + (size_t)h * 28672) + lane * 16;
        f4 aq[2][4];
#pragma unroll
        for (int t = 0; t < 2; ++t)
#pragma unroll
            for (int q = 0; q < 4; ++q) aq[t][q] = f4{0,0,0,0};
#pragma unroll 2
        for (int ks = 0; ks < 14; ++ks) {
            v4u k0 = ld_sc1(rs, wq + ks * 1024, 0), k1 = ld_sc1(rs, wq + (14 + ks) * 1024, 0);
#pragma unroll
            for (int q = 0; q < 4; ++q) {
                v4u x = *(const v4u*)(lds + (q * 14 + ks) * 1024 + lane * 16);
                aq[0][q] = mfma(k0, x, aq[0][q]); aq[1][q] = mfma(k1, x, aq[1][q]);
            }
        }
#pragma unroll
        for (int q = 0; q < 4; ++q) phi_pair(aq[0][q], aq[1][q], g, pos[hi][q], neg[hi][q]);
    }
    wg_wait(rs, C_KV, 16, 4);
#pragma unroll
    for (int hi = 0; hi < 2; ++hi) {
        const int h = 2 * w + hi;
        v4u kf[4];
#pragma unroll
        for (int fr = 0; fr < 4; ++fr) kf[fr] = ld_sc1(rs, (unsigned)(KVF_OFF + (size_t)(h * 4 + fr) * 1024) + lane * 16, 0);
#pragma unroll
        for (int q = 0; q < 4; ++q) {
            f4 n0 = mfma(kf[0], pos[hi][q], f4{0,0,0,0}); n0 = mfma(kf[1], neg[hi][q], n0);
            f4 n1 = mfma(kf[2], pos[hi][q], f4{0,0,0,0}); n1 = mfma(kf[3], neg[hi][q], n1);
            const float den = asf((unsigned)__builtin_amdgcn_ds_bpermute((48 + rho) * 4, (int)asu(n1[0])));
            const float rd = 1.0f / (den + 1e-6f);
            if (g == 3) n1 = f4{0, 0, 0, 0};
            *(v4u*)(O + (h * 4 + q) * 1024 + lane * 16) = pack8(n0 * rd, n1 * rd);
        }
    }
    __syncthreads();
    for (int cb = w; cb < 14; cb += 8) {
        f4 acc[2][4];
#pragma unroll
        for (int t = 0; t < 2; ++t)
#pragma unroll
            for (int q = 0; q < 4; ++q) acc[t][q] = f4{0,0,0,0};
        const unsigned wo = (unsigned)(WOF_OFF + (size_t)cb * 32768) + lane * 16;
#pragma unroll 4
        for (int h = 0; h < 16; ++h) {
            v4u w0 = ld_sc1(rs, wo + h * 1024, 0), w1 = ld_sc1(rs, wo + (16 + h) * 1024, 0);
#pragma unroll
            for (int q = 0; q < 4; ++q) {
                v4u o = *(const v4u*)(O + (h * 4 + q) * 1024 + lane * 16);
                acc[0][q] = mfma(w0, o, acc[0][q]); acc[1][q] = mfma(w1, o, acc[1][q]);
            }
        }
#pragma unroll
        for (int q = 0; q < 4; ++q) {
            const int tok = u * 64 + 32 * (q >> 1) + perm32(q & 1, rho);
            *(v4u*)(out + (size_t)tok * OUTW + 32 * cb + 8 * g) = pack8(acc[0][q], acc[1][q]);
        }
    }
}

DI void p2_unit(unsigned char* ws, unsigned char* lds, const int unit, u16* __restrict__ out) {
    const int seg = unit >> 1, chalf = unit & 1;
    int tid = threadIdx.x; asm volatile("" : "+v"(tid));
    const int lane = tid & 63, w = __builtin_amdgcn_readfirstlane(tid >> 6);
    const int rho = lane & 15, g = lane >> 4;
    __amdgpu_buffer_rsrc_t rs = mkrs(ws, 0x7fffffff);
    unsigned char* xl = lds + 40960;
    __syncthreads();
    for (int i = tid; i < 1792; i += 512) *(v4u*)(xl + i * 16) = *(const v4u*)(ws + XF_OFF + (size_t)seg * 28672 + i * 16);
    if (tid < 64 && seg > 8) wait_flags(rs, C_REC, seg - 8, 16);
    if (tid < 128) wait_flags(rs, C_GU + 128u * (unsigned)min(tid, 111), seg + 1, 8);
    __syncthreads();
    f4 sx[2][2][2];
#pragma unroll
    for (int hi = 0; hi < 2; ++hi) {
#pragma unroll
        for (int a = 0; a < 2; ++a) { sx[hi][a][0] = f4{0,0,0,0}; sx[hi][a][1] = f4{0,0,0,0}; }
        const unsigned gb = (unsigned)(G_OFF + (size_t)(seg * 16 + 2 * w + hi) * 28672) + lane * 16;
#pragma unroll 7
        for (int ks = 0; ks < 14; ++ks) {
            v4u g0 = ld_sc1(rs, gb + ks * 1024, 0), g1 = ld_sc1(rs, gb + (14 + ks) * 1024, 0);
            v4u x0 = *(const v4u*)(xl + ks * 1024 + lane * 16), x1 = *(const v4u*)(xl + (14 + ks) * 1024 + lane * 16);
            sx[hi][0][0] = mfma(g0, x0, sx[hi][0][0]); sx[hi][0][1] = mfma(g0, x1, sx[hi][0][1]);
            sx[hi][1][0] = mfma(g1, x0, sx[hi][1][0]); sx[hi][1][1] = mfma(g1, x1, sx[hi][1][1]);
        }
    }
    f4* sxl = (f4*)(lds + 69632) + tid;
#pragma unroll
    for (int hi = 0; hi < 2; ++hi)
#pragma unroll
        for (int a = 0; a < 2; ++a)
#pragma unroll
            for (int b2 = 0; b2 < 2; ++b2) sxl[((hi * 2 + a) * 2 + b2) * 512] = sx[hi][a][b2];
    SB0
    const int cb = 7 * chalf + (w < 7 ? w : 6);
    v4u uf[2][16];
    unsigned uo[2], vo[2];
#pragma unroll
    for (int t = 0; t < 2; ++t) {
        const int c = 32 * cb + perm32(t, rho); const int j = c >> 3, c8 = c & 7;
        uo[t] = (unsigned)(U_OFF + (size_t)(seg * 56 + j) * 8192) + (c8 + 16 * g) * 16;
        vo[t] = (unsigned)(VP_OFF + (size_t)(seg * 56 + j) * 2048) + g * 128 + c8 * 16;
#pragma unroll
        for (int h = 0; h < 16; ++h) uf[t][h] = ld_sc1(rs, uo[t] + (h >> 1) * 1024 + (h & 1) * 128, 0);
    }
    if (seg < 61) { if (tid < 64) wait_flags(rs, C_REC, seg + 1, 8); }
    else if (tid < 128) { wait_flags(rs, C_REC, 59, 8); wait_flags(rs, C_DN2 + 4u * (unsigned)min(tid, 111), 1, 1); }
    __syncthreads();
    f4 sm[2] = {f4{0,0,0,0}, f4{0,0,0,0}};
    v4u vf[2][4];
    {
        const unsigned gmb = (unsigned)(GM_OFF + (size_t)(seg * 16 + 2 * w + (rho >> 3)) * 7168) + g * 128 + (rho & 7) * 16;
        v4u gm[14];
#pragma unroll
        for (int ks = 0; ks < 14; ++ks) gm[ks] = ld_sc1(rs, gmb + ks * 512, 0);
#pragma unroll
        for (int t = 0; t < 2; ++t)
#pragma unroll
            for (int w4 = 0; w4 < 4; ++w4) vf[t][w4] = ld_sc1(rs, vo[t] + w4 * 512, 0);
        SB0
#pragma unroll
        for (int ks = 0; ks < 14; ++ks) {
            v4u x0 = *(const v4u*)(xl + ks * 1024 + lane * 16), x1 = *(const v4u*)(xl + (14 + ks) * 1024 + lane * 16);
            sm[0] = mfma(gm[ks], x0, sm[0]); sm[1] = mfma(gm[ks], x1, sm[1]);
            SB0
        }
    }
#pragma unroll
    for (int hi = 0; hi < 2; ++hi) {
        const int h = 2 * w + hi;
        const bool own = ((g >> 1) == hi);
#pragma unroll
        for (int tp = 0; tp < 2; ++tp) {
            const int tq = perm32(tp, rho);
            float v[8];
#pragma unroll
            for (int tau = 0; tau < 2; ++tau) {
                const f4 sv = sxl[((hi * 2 + tau) * 2 + tp) * 512];
#pragma unroll
                for (int r = 0; r < 4; ++r) { const int key = 16 * tau + 4 * g + r; v[4 * tau + r] = (key <= tq) ? sv[r] : -3.0e38f; }
            }
            float mx = fmaxf(fmaxf(fmaxf(v[0], v[1]), fmaxf(v[2], v[3])), fmaxf(fmaxf(v[4], v[5]), fmaxf(v[6], v[7])));
            const float mo = fmaxf(fmaxf(sm[tp][0], sm[tp][1]), fmaxf(sm[tp][2], sm[tp][3]));
            mx = own ? fmaxf(mx, mo) : mx;
            mx = xmax32(xmax16(mx));
            float sum = 0.f, em[4];
#pragma unroll
            for (int k = 0; k < 8; ++k) { v[k] = __builtin_amdgcn_exp2f(v[k] - mx); sum += v[k]; }
#pragma unroll
            for (int r = 0; r < 4; ++r) { em[r] = own ? __builtin_amdgcn_exp2f(sm[tp][r] - mx) : 0.f; sum += em[r]; }
            sum = xsum32(xsum16(sum));
            const float rz = __builtin_amdgcn_rcpf(sum);
            v2u p0; p0.x = pk2(v[0] * rz, v[1] * rz); p0.y = pk2(v[2] * rz, v[3] * rz);
            v2u p1; p1.x = pk2(v[4] * rz, v[5] * rz); p1.y = pk2(v[6] * rz, v[7] * rz);
            *(v2u*)(lds + (h * 2 + tp) * 1024 + (rho + 16 * (g >> 1)) * 16 + (g & 1) * 8) = p0;
            *(v2u*)(lds + (h * 2 + tp) * 1024 + (rho + 16 * (2 + (g >> 1))) * 16 + (g & 1) * 8) = p1;
            if (own) {
                v2u pm; pm.x = pk2(em[0] * rz, em[1] * rz); pm.y = pk2(em[2] * rz, em[3] * rz);
                *(v2u*)(lds + ((16 + (h >> 2)) * 2 + tp) * 1024 + (rho + 16 * (h & 3)) * 16 + (g & 1) * 8) = pm;
            }
        }
    }
    __syncthreads();
    if (w < 7) {
        f4 acc[2][2] = {{f4{0,0,0,0}, f4{0,0,0,0}}, {f4{0,0,0,0}, f4{0,0,0,0}}};
#pragma unroll
        for (int h = 0; h < 16; ++h) {
            v4u p0 = *(const v4u*)(lds + (h * 2) * 1024 + lane * 16), p1 = *(const v4u*)(lds + (h * 2 + 1) * 1024 + lane * 16);
#pragma unroll
            for (int t = 0; t < 2; ++t) { acc[t][0] = mfma(uf[t][h], p0, acc[t][0]); acc[t][1] = mfma(uf[t][h], p1, acc[t][1]); }
        }
#pragma unroll
        for (int w4 = 0; w4 < 4; ++w4) {
            v4u p0 = *(const v4u*)(lds + ((16 + w4) * 2) * 1024 + lane * 16), p1 = *(const v4u*)(lds + ((16 + w4) * 2 + 1) * 1024 + lane * 16);
#pragma unroll
            for (int t = 0; t < 2; ++t) { acc[t][0] = mfma(vf[t][w4], p0, acc[t][0]); acc[t][1] = mfma(vf[t][w4], p1, acc[t][1]); }
        }
#pragma unroll
        for (int tp = 0; tp < 2; ++tp) {
            const int tok = seg * 32 + perm32(tp, rho);
            *(v4u*)(out + (size_t)tok * OUTW + 448 + 32 * cb + 8 * g) = pack8(acc[0][tp], acc[1][tp]);
        }
    }
}

constexpr int LDS_MAIN = 135168 + 64;
__global__ void __launch_bounds__(512, 2) k_main(unsigned char* ws, u16* __restrict__ out, const u16* __restrict__ ax, const u16* __restrict__ wqkv, const u16* __restrict__ wphi, const u16* __restrict__ awo, int dS, int dV, int dVm, int pad_) {
    extern __shared__ __attribute__((aligned(16))) unsigned char lds[];
    const int bidx = blockIdx.x;
    const int idx = (bidx >> 3) * 4 + (bidx & 3);
#define PT(i)
    if ((bidx & 7) < 4) { rec_role(ws, lds, idx, dS, dV, dVm); return; }
    __amdgpu_buffer_rsrc_t rs = mkrs(ws, 0x7fffffff);
    if (idx < 112) gu_role(ws, lds, idx);
    else {
        const int u = idx - 112;
        a0_prep(ws, u, wqkv, wphi, awo);
        signal_add(ws, C_A0);
        wg_wait(rs, C_A0, 16, 2);
        a1_unit(ws, lds, u, ax);
        signal_add(ws, C_A1);
        wg_wait(rs, C_A1, 16, 4);
        a15_unit(ws, lds, u);
        signal_add(ws, C_KV);
    }
    PT(0)
    for (;;) {
        __syncthreads();
        if (threadIdx.x == 0) LDSV(LDS_MAIN - 16) = __hip_atomic_fetch_add((unsigned*)(ws + CTRL_OFF + C_TICKET), 1u, __ATOMIC_RELAXED, __HIP_MEMORY_SCOPE_AGENT);
        __syncthreads();
        const int t = __builtin_amdgcn_readfirstlane(LDSV(LDS_MAIN - 16));
        if (t >= 160) break;
        if (t < 32) a2_unit(ws, lds, t, out, ax);
        else p2_unit(ws, lds, t - 32, out);
        if (t < 32) { PT(1) } else { PT(2) }
    }
}

#define REC_DS (83 | (50 << 8))
#define REC_DV (78 | (1 << 8))
#define REC_DVM (4 | (8 << 8) | (0 << 16))
constexpr int g_dS = REC_DS, g_dV = REC_DV, g_dVm = REC_DVM;

extern "C" void kernel_launch(void* const* d_in, const int* in_sizes, int n_in, void* d_out, int out_size, void* d_ws, size_t ws_size, hipStream_t stream) {
    const u16* A_x = (const u16*)d_in[0]; const u16* A_wqkv = (const u16*)d_in[1]; const u16* A_wphi = (const u16*)d_in[2]; const u16* A_wo = (const u16*)d_in[3];
    const u16* B_x = (const u16*)d_in[4]; const u16* B_mem0 = (const u16*)d_in[5];
    const u16* Wq = (const u16*)d_in[6]; const u16* Wk = (const u16*)d_in[7]; const u16* Wv = (const u16*)d_in[8]; const u16* Wo = (const u16*)d_in[9];
    u16* out = (u16*)d_out;
    unsigned char* ws = (unsigned char*)(((uintptr_t)d_ws + 2 * MB - 1) & ~(uintptr_t)(2 * MB - 1));
    (void)hipFuncSetAttribute((const void*)k_pre, hipFuncAttributeMaxDynamicSharedMemorySize, 61440);
    (void)hipFuncSetAttribute((const void*)k_main, hipFuncAttributeMaxDynamicSharedMemorySize, LDS_MAIN);
    k_pre<<<256, 448, 61440, stream>>>(ws, Wq, Wk, Wv, Wo, B_x, B_mem0);
    k_main<<<256, 512, LDS_MAIN, stream>>>(ws, out, A_x, A_wqkv, A_wphi, A_wo, g_dS, g_dV, g_dVm, 0);
}
```

```cpp
#include <hip/hip_runtime.h>
#include <hip/hip_bf16.h>
#include <stdint.h>

typedef unsigned short u16;
typedef unsigned v4u __attribute__((ext_vector_type(4)));
typedef unsigned v2u __attribute__((ext_vector_type(2)));
typedef float f4 __attribute__((ext_vector_type(4)));
typedef float f2 __attribute__((ext_vector_type(2)));
typedef short s8 __attribute__((ext_vector_type(8)));
typedef __bf16 bf2 __attribute__((ext_vector_type(2)));
#define DI __device__ __forceinline__

constexpr int D = 448, H = 16, DH = 256, MEM = 8, SEG = 32, NSEG = 64, S_TOK = 2048;
constexpr int NKS = 14;
constexpr int OUTW = 896;
constexpr unsigned SENT = 0xFFFFFFFFu;
constexpr float LOG2E = 1.4426950408889634f;

constexpr size_t MB = 1024 * 1024;
constexpr int KCOPY = 4;
#define FARX 8192u
constexpr size_t COMM_OFF = 0;
constexpr size_t COMM_BYTES = 2 * MB;
constexpr size_t CTRL_OFF = COMM_BYTES;
constexpr size_t AS_OFF = CTRL_OFF + 262144;
constexpr size_t AS_BYTES = (size_t)16 * 14 * 2 * 14 * 1024;
constexpr size_t BS_OFF = AS_OFF + AS_BYTES;
constexpr size_t BS_BYTES = (size_t)56 * 8 * 14 * 1024;
constexpr size_t XF_OFF = BS_OFF + BS_BYTES;
constexpr size_t XF_BYTES = (size_t)64 * 28 * 1024;
constexpr size_t GU_BASE = 40 * MB;
constexpr size_t GM_OFF = XF_OFF + XF_BYTES;
constexpr size_t GM_BYTES = (size_t)64 * 16 * 14 * 512;
constexpr size_t VP_OFF = GM_OFF + GM_BYTES;
constexpr size_t VP_BYTES = (size_t)64 * 56 * 4 * 512;
constexpr size_t AXF_OFF = VP_OFF + VP_BYTES;
constexpr size_t WQF_OFF = AXF_OFF + XF_BYTES;
constexpr size_t WQF_ONE = (size_t)16 * 2 * 14 * 1024;
constexpr size_t WOF_OFF = WQF_OFF + 3 * WQF_ONE;
constexpr size_t KVP_OFF = WOF_OFF + WQF_ONE;
constexpr size_t KVF_OFF = KVP_OFF + (size_t)16 * 16 * 8192;
constexpr size_t WS_END = KVF_OFF + 65536;
static_assert(WS_END + MB <= GU_BASE && GU_BASE % (4 * MB) == 0 && GU_BASE + 128 * MB + 2 * MB <= 256 * MB, "workspace layout");
constexpr unsigned C_POL = 0, C_REC = 128, C_DONE = 256, C_A1 = 384, C_KV = 512, C_TICKET = 640, C_A0 = 768, C_GU = 1024;
constexpr unsigned C_DN2 = 16384;
constexpr unsigned C_DBG = 32768;

DI unsigned pk2(float a, float b) { f2 v = {a, b}; return __builtin_bit_cast(unsigned, __builtin_convertvector(v, bf2)); }
DI float asf(unsigned x) { return __builtin_bit_cast(float, x); }
DI unsigned asu(float x) { return __builtin_bit_cast(unsigned, x); }
DI float bflo(unsigned x) { return __builtin_bit_cast(float, x << 16); }
DI float bfhi(unsigned x) { return __builtin_bit_cast(float, x & 0xffff0000u); }
DI float bf2f(u16 x) { return __builtin_bit_cast(float, (unsigned)x << 16); }
DI f4 mfma(v4u a, v4u b, f4 c) { return __builtin_amdgcn_mfma_f32_16x16x32_bf16(__builtin_bit_cast(s8, a), __builtin_bit_cast(s8, b), c, 0, 0, 0); }
DI v4u pack8(f4 a, f4 b) { v4u r; r.x = pk2(a[0], a[1]); r.y = pk2(a[2], a[3]); r.z = pk2(b[0], b[1]); r.w = pk2(b[2], b[3]); return r; }
DI v4u nosent(v4u v) { v.x = min(v.x, 0xFFFFFFFEu); v.y = min(v.y, 0xFFFFFFFEu); v.z = min(v.z, 0xFFFFFFFEu); v.w = min(v.w, 0xFFFFFFFEu); return v; }
DI int perm32(int t, int rho) { return 8 * (rho >> 2) + 4 * t + (rho & 3); }
DI __amdgpu_buffer_rsrc_t mkrs(void* p, unsigned bytes) { return __builtin_amdgcn_make_buffer_rsrc(p, 0, bytes, 0x00020000); }
DI v4u ld_sc1(__amdgpu_buffer_rsrc_t rs, unsigned voff, unsigned soff) { return __builtin_amdgcn_raw_buffer_load_b128(rs, voff, soff, 16); }
DI v2u ld2_sc1(__amdgpu_buffer_rsrc_t rs, unsigned voff, unsigned soff) { return __builtin_amdgcn_raw_buffer_load_b64(rs, voff, soff, 16); }
DI void st_sc1(v4u v, __amdgpu_buffer_rsrc_t rs, unsigned voff, unsigned soff) { __builtin_amdgcn_raw_buffer_store_b128(v, rs, voff, soff, 16); }
DI void st2_sc1(v2u v, __amdgpu_buffer_rsrc_t rs, unsigned voff, unsigned soff) { __builtin_amdgcn_raw_buffer_store_b64(v, rs, voff, soff, 16); }
DI unsigned long long mtime() { unsigned long long t; asm volatile("s_memtime %0\n\ts_waitcnt lgkmcnt(0)" : "=s"(t) :: "memory"); return t; }

typedef short s4t __attribute__((ext_vector_type(4)));
DI v2u trread(const unsigned char* p) { return __builtin_bit_cast(v2u, __builtin_amdgcn_ds_read_tr16_b64_v4i16((__attribute__((address_space(3))) s4t*)p)); }
DI unsigned comm_step_base(int step, int copy) { return __builtin_amdgcn_readfirstlane((unsigned)(step & 7) * 262144u + (unsigned)copy * 65536u); }
DI unsigned segb(int sg, unsigned polo) { return __builtin_amdgcn_readfirstlane((unsigned)GU_BASE + (unsigned)sg * 2097152u + ((polo ^ FARX ^ ((unsigned)(sg & 1) << 13)) & 8192u)); }
DI unsigned comm_off(int chunk0, unsigned o, unsigned pol) { return (unsigned)(chunk0 + (o >> 13)) * 16384u + pol * 8192u + (o & 8191u); }

__global__ void __launch_bounds__(448) k_pre(unsigned char* ws, const u16* __restrict__ Wq, const u16* __restrict__ Wk, const u16* __restrict__ Wv, const u16* __restrict__ Wo,
                                             const u16* __restrict__ bx, const u16* __restrict__ mem0) {
    extern __shared__ __attribute__((aligned(16))) unsigned char lds[];
    const int tid = threadIdx.x, lane = tid & 63, w = __builtin_amdgcn_readfirstlane(tid >> 6);
    const int wg = blockIdx.x;
#define PP(i)
    const int h = (wg & 7) * 2 + ((wg >> 3) & 1), rest = wg >> 4;
    const int mat = rest >> 3, rh = (rest >> 2) & 1, q = rest & 3;
    const int rho = lane & 15, g = lane >> 4;
    const int rb = 7 * rh + w;
    __amdgpu_buffer_rsrc_t rs = mkrs(ws, 0x7fffffff);
    const int gt = wg * 448 + tid; constexpr int GN = 256 * 448;
    constexpr int WST = 240;
    v4u bt[8];
    if (mat == 0) {
        const int ct = 7 * q + w; const int d = 32 * (ct >> 1) + perm32(ct & 1, rho);
#pragma unroll
        for (int es = 0; es < 8; ++es) bt[es] = *(const v4u*)(Wq + ((size_t)d * H + h) * DH + es * 32 + g * 8);
    } else {
#pragma unroll
        for (int it = 0; it < 8; ++it) {
            const int idx = it * 448 + tid; const int e = idx / 14, c8 = idx % 14;
            bt[it] = *(const v4u*)(Wo + ((size_t)h * DH + e) * D + 112 * q + c8 * 8);
        }
    }
    const u16* Wr = mat ? Wv : Wk;
    v4u a[2][8];
#pragma unroll
    for (int t = 0; t < 2; ++t) {
        const int row = 32 * rb + perm32(t, rho);
#pragma unroll
        for (int es = 0; es < 8; ++es) a[t][es] = *(const v4u*)(Wr + ((size_t)row * H + h) * DH + es * 32 + g * 8);
    }
    v4u xfv;
    {
        const int f = gt >> 6; const int ks = f % 14; const int st = f / 14; const int tau = st & 1, seg = st >> 1;
        xfv = *(const v4u*)(bx + (size_t)(seg * 32 + perm32(tau, rho)) * D + ks * 32 + g * 8);
    }
    __builtin_amdgcn_sched_barrier(0);
    if (wg == 1 && tid < 128) __builtin_amdgcn_raw_buffer_store_b32(0u, rs, (unsigned)CTRL_OFF + tid * 128, 0, 16);
    if (wg == 2 && tid < 128) __builtin_amdgcn_raw_buffer_store_b32(0u, rs, (unsigned)CTRL_OFF + C_DN2 + tid * 4, 0, 16);
    for (unsigned i = gt; i < COMM_BYTES / 16; i += GN) {
        unsigned off = i * 16;
        unsigned within = off & 65535u, slot = off >> 18;
        unsigned chunk = (within >> 14) & 3, o = within & 8191u;
        if (chunk == 0 && (o >= 7184 || (slot == 0 && o < 7168))) continue;
        st_sc1(v4u{SENT, SENT, SENT, SENT}, rs, off, 0);
    }
    PP(0)
    if (mat == 0) {
#pragma unroll
        for (int es = 0; es < 8; ++es) *(v4u*)(lds + (w * 8 + es) * 1024 + lane * 16) = bt[es];
    } else {
#pragma unroll
        for (int it = 0; it < 8; ++it) { const int idx = it * 448 + tid; const int e = idx / 14, c8 = idx % 14; *(v4u*)(lds + e * WST + c8 * 16) = bt[it]; }
    }
    PP(3)
    __syncthreads();
    PP(4)
    f4 acc[2][7];
#pragma unroll
    for (int t = 0; t < 2; ++t)
#pragma unroll
        for (int c = 0; c < 7; ++c) acc[t][c] = f4{0, 0, 0, 0};
    if (mat == 0) {
#pragma unroll
        for (int es = 0; es < 8; ++es)
#pragma unroll
            for (int c = 0; c < 7; ++c) {
                v4u bfr = *(const v4u*)(lds + (c * 8 + es) * 1024 + lane * 16);
                acc[0][c] = mfma(a[0][es], bfr, acc[0][c]);
                acc[1][c] = mfma(a[1][es], bfr, acc[1][c]);
            }
        const float sc = LOG2E * 0.0625f;
#pragma unroll
        for (int c = 0; c < 7; ++c) {
            const int ct = 7 * q + c;
            st_sc1(pack8(acc[0][c] * sc, acc[1][c] * sc), rs, (unsigned)(AS_OFF + ((size_t)((h * 14 + (ct >> 1)) * 2 + (ct & 1)) * 14 + rb) * 1024) + lane * 16, 0);
        }
    } else {
        const unsigned trb = (8 * g + (rho >> 2)) * WST + (lane & 3) * 8;
#pragma unroll
        for (int es = 0; es < 8; ++es)
#pragma unroll
            for (int c = 0; c < 7; ++c) {
                v2u lo = trread(lds + trb + (32 * es) * WST + c * 32), hi = trread(lds + trb + (32 * es + 4) * WST + c * 32);
                v4u bfr = {lo.x, lo.y, hi.x, hi.y};
                acc[0][c] = mfma(a[0][es], bfr, acc[0][c]);
                acc[1][c] = mfma(a[1][es], bfr, acc[1][c]);
            }
#pragma unroll
        for (int c = 0; c < 7; ++c) {
            const int ct = 7 * q + c;
            const int j = 2 * ct + (rho >> 3);
            st_sc1(pack8(acc[0][c], acc[1][c]), rs, (unsigned)(BS_OFF + ((size_t)(j * 8 + (h >> 1)) * 14 + rb) * 1024) + (((h & 1) * 8 + (rho & 7)) + 16 * g) * 16, 0);
        }
    }
    st_sc1(xfv, rs, (unsigned)XF_OFF + gt * 16, 0);
    if (wg < 8) {
        const int j = tid >> 3, irow = tid & 7;
        st_sc1(nosent(*(const v4u*)(mem0 + irow * D + j * 8)), rs, (wg >> 1) * 65536u + (wg & 1) * 8192u + tid * 16, 0);
    }
    PP(5)
}

DI unsigned umax3(unsigned a, unsigned b, unsigned c) { return max(max(a, b), c); }
DI unsigned vmax4(unsigned t, v4u v) { return umax3(umax3(t, v.x, v.y), v.z, v.w); }
DI float dpp_xor8(float v) { return asf((unsigned)__builtin_amdgcn_update_dpp(0, (int)asu(v), 0x128, 0xf, 0xf, false)); }
DI unsigned dpp_xor8u(unsigned v) { return (unsigned)__builtin_amdgcn_update_dpp(0, (int)v, 0x128, 0xf, 0xf, false); }
constexpr unsigned SPIN_MAX = 200000;
DI float dpp_x1(float v) { return asf((unsigned)__builtin_amdgcn_update_dpp(0, (int)asu(v), 0xB1, 0xf, 0xf, false)); }
DI float dpp_x2(float v) { return asf((unsigned)__builtin_amdgcn_update_dpp(0, (int)asu(v), 0x4E, 0xf, 0xf, false)); }
DI f4 quadsum(f4 v) {
#pragma unroll
    for (int r = 0; r < 4; ++r) v[r] += dpp_x1(v[r]);
#pragma unroll
    for (int r = 0; r < 4; ++r) v[r] += dpp_x2(v[r]);
    return v;
}
DI float xmax16(float x) { auto r = __builtin_amdgcn_permlane16_swap(asu(x), asu(x), false, false); unsigned a = r[0], b = r[1]; return fmaxf(asf(a), asf(b)); }
DI float xmax32(float x) { auto r = __builtin_amdgcn_permlane32_swap(asu(x), asu(x), false, false); unsigned a = r[0], b = r[1]; return fmaxf(asf(a), asf(b)); }
DI float xsum16(float x) { auto r = __builtin_amdgcn_permlane16_swap(asu(x), asu(x), false, false); unsigned a = r[0], b = r[1]; return asf(a) + asf(b); }
DI float xsum32(float x) { auto r = __builtin_amdgcn_permlane32_swap(asu(x), asu(x), false, false); unsigned a = r[0], b = r[1]; return asf(a) + asf(b); }
#define LOADM(mf) v4u mf[14]; _Pragma("unroll") for (int ks = 0; ks < 14; ++ks) mf[ks] = *(const v4u*)(ldsm + ks * 512 + mfrag_off); __builtin_amdgcn_sched_barrier(0);
DI void bar() { asm volatile("s_waitcnt lgkmcnt(0)" ::: "memory"); __builtin_amdgcn_s_barrier(); asm volatile("" ::: "memory"); }
DI unsigned now32() { return (unsigned)mtime(); }
DI void wait_until(unsigned target) {
    int rem = (int)(target - now32());
    while (rem > 80) { __builtin_amdgcn_s_sleep(1); rem -= 64; }
    while (rem > 0) { asm volatile("s_nop 7" ::: "memory"); rem -= 16; }
}

DI unsigned rt32() { unsigned long long t; asm volatile("s_memrealtime %0\n\ts_waitcnt lgkmcnt(0)" : "=s"(t) :: "memory"); return (unsigned)t; }
DI unsigned approach_rt(unsigned target, int lead) {
    int rem;
    for (;;) {
        rem = (int)(target - rt32());
        if (rem <= lead + 4) break;
        int n = (rem - lead) >> 2;
        while (n-- > 0) __builtin_amdgcn_s_sleep(1);
    }
    return now32() + (unsigned)(rem * 23 - 110);
}
DI void clock_ref(unsigned& re, unsigned& me) {
    unsigned long long m0, m1, r[12];
    asm volatile("s_memtime %0\n\t"
        "s_memrealtime %1\n\ts_nop 1\n\ts_memrealtime %2\n\ts_nop 1\n\ts_memrealtime %3\n\ts_nop 1\n\ts_memrealtime %4\n\ts_nop 1\n\t"
        "s_memrealtime %5\n\ts_nop 1\n\ts_memrealtime %6\n\ts_nop 1\n\ts_memrealtime %7\n\ts_nop 1\n\ts_memrealtime %8\n\ts_nop 1\n\t"
        "s_memrealtime %9\n\ts_nop 1\n\ts_memrealtime %10\n\ts_nop 1\n\ts_memrealtime %11\n\ts_nop 1\n\ts_memrealtime %12\n\ts_nop 1\n\t"
        "s_memtime %13\n\ts_waitcnt lgkmcnt(0)"
        : "=&s"(m0), "=&s"(r[0]), "=&s"(r[1]), "=&s"(r[2]), "=&s"(r[3]), "=&s"(r[4]), "=&s"(r[5]), "=&s"(r[6]), "=&s"(r[7]), "=&s"(r[8]), "=&s"(r[9]), "=&s"(r[10]), "=&s"(r[11]), "=&s"(m1) :: "memory");
    unsigned cnt = 0;
#pragma unroll
    for (int i = 1; i < 12; ++i) cnt += ((unsigned)r[i] == (unsigned)r[0]) ? 1u : 0u;
    re = (unsigned)r[0] + 4u;
    me = (unsigned)m0 + ((((unsigned)m1 - (unsigned)m0) * (2u * cnt + 1u) * 43u) >> 10);
}
DI void sleep_to(unsigned tc) {
    for (;;) {
        const int rem = (int)(tc - now32());
        if (rem <= 0) return;
        if (rem > 260) { int n = (rem - 160) / 96; while (n-- > 0) __builtin_amdgcn_s_sleep(1); }
    }
}
DI void spin_to(unsigned tc) { while ((int)(tc - now32()) > 0) {} }
DI void wait_rt(unsigned target) { spin_to(approach_rt(target, 10)); }
template <int NP, bool CTRL>
DI unsigned gather_m(__amdgpu_buffer_rsrc_t rs, unsigned sb, unsigned char* ldsm, int p0, int lane, int skipline = -1) {
    const unsigned o0 = p0 * 1024 + lane * 16;
    for (unsigned spin = 0;; ++spin) {
        asm volatile("" ::: "memory");
        v4u a[NP]; v2u c = {0, 0};
        bool sk[NP];
#pragma unroll
        for (int k = 0; k < NP; ++k) { sk[k] = (int)((o0 + k * 1024) >> 7) == skipline; a[k] = ld_sc1(rs, (o0 + k * 1024) ^ (sk[k] ? 128u : 0u), sb); }
        if (CTRL) c = ld2_sc1(rs, 7168u, sb);
#pragma unroll
        for (int k = 0; k < NP; ++k) *(v4u*)(ldsm + (sk[k] ? 7424u + (lane & 7) * 16 : o0 + k * 1024)) = a[k];
        if (CTRL && lane == 0) *(v2u*)(ldsm + 7168) = c;
        unsigned t;
        if (NP == 2) t = max(vmax4(0, a[0]), vmax4(0, a[1])); else t = vmax4(0, a[0]);
        if (CTRL) t = umax3(t, c.x, c.y);
        if (__all(t != SENT) || spin > SPIN_MAX) return spin;
        __builtin_amdgcn_s_sleep(1);
    }
}
template <int NP, bool CTRL>
DI unsigned gather_fast(__amdgpu_buffer_rsrc_t rs, unsigned sb, unsigned char* ldsm, const unsigned (&vo)[2], const unsigned (&lo)[2], int p0, int lane, int skipline) {
    asm volatile("; gather %0 %1" :: "i"(NP), "i"((int)CTRL) : "memory");
    v4u a0 = ld_sc1(rs, vo[0], sb), a1; v2u c;
    if (NP == 2) a1 = ld_sc1(rs, vo[1], sb);
    if (CTRL) c = ld2_sc1(rs, 7168u, sb);
    __builtin_amdgcn_sched_barrier(0);
    *(v4u*)(ldsm + lo[0]) = a0;
    unsigned t = vmax4(0, a0);
    if (NP == 2) { *(v4u*)(ldsm + lo[1]) = a1; t = vmax4(t, a1); }
    if (CTRL) { *(v2u*)(ldsm + lo[1]) = c; t = umax3(t, c.x, c.y); }
    if (__builtin_expect(__all(t != SENT), 1)) return 0;
    gather_m<NP, CTRL>(rs, sb, ldsm, p0, lane, skipline);
    return 1;
}
#define GADDR(vo, lo, p0, np, skl) { _Pragma("unroll") for (int k_ = 0; k_ < 2; ++k_) { const unsigned o_ = ((p0) + k_) * 1024 + lane * 16; const bool sk_ = (int)(o_ >> 7) == (skl); \
        vo[k_] = o_ ^ (sk_ ? 128u : 0u); lo[k_] = sk_ ? 7424u + (lane & 7) * 16 : o_; } if ((np) == 1) lo[1] = lane == 0 ? 7168u : 7552u + (lane & 7) * 8; }
#define LOADP(mf) v4u mf[7]; _Pragma("unroll") for (int q_ = 0; q_ < 7; ++q_) mf[q_] = *(const v4u*)(ldsm + q_ * 1024 + pfrag_off); __builtin_amdgcn_sched_barrier(0);
DI unsigned reduce8(const float* rd, int lane) {
    const float* q = rd + (lane >> 3) * 100 + (lane & 7) * 12;
    f4 a = *(const f4*)q, b = *(const f4*)(q + 4);
    a += b;
    float v = (a[0] + a[1]) + (a[2] + a[3]);
    float o = dpp_x1(v);
    unsigned r = (lane & 1) ? pk2(o, v) : pk2(v, o);
    return min(r, 0xFFFFFFFEu);
}
#define READCTRL() { SAVER1 v2u c_ = *(const v2u*)(ldsm + 7168); R2 = __builtin_amdgcn_readfirstlane(c_.x); R1n = __builtin_amdgcn_readfirstlane(c_.y); }

#define SAVER1
typedef short s4 __attribute__((ext_vector_type(4)));
DI f4 mfma16(v2u a, v2u b, f4 c) { return __builtin_amdgcn_mfma_f32_16x16x16bf16_1k(__builtin_bit_cast(s4, a), __builtin_bit_cast(s4, b), c, 0, 0, 0); }
DI float vmax(float a, float b) { float r; asm("s_nop 1\n\tv_max_f32 %0, %1, %2" : "=v"(r) : "v"(a), "v"(b)); return r; }
DI float vmax3(float a, float b, float c) { float r; asm("v_max3_f32 %0, %1, %2, %3" : "=v"(r) : "v"(a), "v"(b), "v"(c)); return r; }
DI float vmax3_mfma(float a, float b, float c) { float r; asm("s_nop 3\n\tv_max3_f32 %0, %1, %2, %3" : "=v"(r) : "v"(a), "v"(b), "v"(c)); return r; }
DI float rmax16(float x) { auto r = __builtin_amdgcn_permlane16_swap(asu(x), asu(x), false, false); unsigned a = r[0], b = r[1]; return vmax(asf(a), asf(b)); }
DI float rmax32(float x) { auto r = __builtin_amdgcn_permlane32_swap(asu(x), asu(x), false, false); unsigned a = r[0], b = r[1]; return vmax(asf(a), asf(b)); }
constexpr int HS = 1408;
constexpr int ST_OFF = 16 * HS;
constexpr int ZL_OFF = ST_OFF + 1024;
constexpr int L_M = 0, L_RED = 8192, L_RED2 = 12800, L_GM = 16128, L_VP = 17152, L_FL = 19200, L_UF = 20480;

DI unsigned gather_ctrl(__amdgpu_buffer_rsrc_t rs, unsigned sb, unsigned char* ldsm, int lane) {
    for (unsigned spin = 0;; ++spin) {
        asm volatile("" ::: "memory");
        v2u c = ld2_sc1(rs, 7168u, sb);
        if (lane == 0) *(v2u*)(ldsm + 7168) = c;
        if (__all(max(c.x, c.y) != SENT) || spin > SPIN_MAX) return spin;
        __builtin_amdgcn_s_sleep(1);
    }
}
#define REREF 0
#define RSLEEP 0
#define XCK 100
#define FIRSTN 2
#define ASLEEP 2
#define HSLEEP 2
#define SB0 __builtin_amdgcn_sched_barrier(0);

DI void glds16(const unsigned char* gsrc, unsigned lds_dst) {
    unsigned keep;
    asm volatile("s_mov_b32 %0, m0\n\ts_mov_b32 m0, %2\n\ts_nop 0\n\tglobal_load_lds_dwordx4 %1, off sc1\n\ts_mov_b32 m0, %0" : "=&s"(keep) : "v"(gsrc), "s"(lds_dst) : "memory");
}
DI void glds16x4(const unsigned char* gsrc, unsigned lds_dst) {
    unsigned keep;
    asm volatile("s_mov_b32 %0, m0\n\ts_mov_b32 m0, %2\n\ts_nop 0\n\t"
                 "global_load_lds_dwordx4 %1, off sc1\n\tglobal_load_lds_dwordx4 %1, off offset:1024 sc1\n\t"
                 "global_load_lds_dwordx4 %1, off offset:2048 sc1\n\tglobal_load_lds_dwordx4 %1, off offset:3072 sc1\n\t"
                 "s_mov_b32 m0, %0" : "=&s"(keep) : "v"(gsrc), "s"(lds_dst) : "memory");
}
DI void glds4(const unsigned char* gsrc, unsigned lds_dst) {
    unsigned keep;
    asm volatile("s_mov_b32 %0, m0\n\ts_mov_b32 m0, %2\n\ts_nop 0\n\tglobal_load_lds_dword %1, off sc1\n\ts_mov_b32 m0, %0" : "=&s"(keep) : "v"(gsrc), "s"(lds_dst) : "memory");
}
DI unsigned ldflag(__amdgpu_buffer_rsrc_t rs, unsigned off) { return __builtin_amdgcn_raw_buffer_load_b32(rs, (unsigned)CTRL_OFF + off, 0, 16); }
DI unsigned wait_flags(__amdgpu_buffer_rsrc_t rs, unsigned off, unsigned need, int slp) {
    unsigned v;
    for (unsigned spin = 0;; ++spin) {
        asm volatile("" ::: "memory");
        v = ldflag(rs, off);
        if (__all(v >= need) || spin > 4 * SPIN_MAX) break;
        for (int i = 0; i < slp; ++i) __builtin_amdgcn_s_sleep(4);
    }
#pragma unroll
    for (int o = 32; o >= 1; o >>= 1) v = min(v, (unsigned)__shfl_xor((int)v, o));
    return __builtin_amdgcn_readfirstlane(v);
}

#define LDSV(off) (*(volatile __attribute__((address_space(3))) unsigned*)((__attribute__((address_space(3))) unsigned char*)lds + (off)))
DI void rec_role(unsigned char* ws, unsigned char* lds, const int role, int dS, int dV, int dVm) {
    const int tid = threadIdx.x, lane = tid & 63, w = __builtin_amdgcn_readfirstlane(tid >> 6);
    const int rho = lane & 15, g = lane >> 4;
    __amdgpu_buffer_rsrc_t rs = mkrs(ws, 0x7fffffff);
    unsigned char* ldsm = lds + L_M;
    float* red = (float*)(lds + L_RED);
    float* red2 = (float*)(lds + L_RED2);
    if (role == 112) {
        unsigned best = 0xffffffffu;
        for (int r = 0; r < 10; ++r) {
            const v4u z = {SENT, SENT, SENT, SENT};
            const unsigned off = (unsigned)((w >> 1) + 4 * (r & 1)) * 262144u + ((r >> 1) & 3) * 65536u + 7680u + ((r >> 3) + 2 * (r & 1)) * 128u + (w & 1) * 8192u;
            const unsigned t0 = now32();
            if (lane < 8) st_sc1(z, rs, off + lane * 16, 0);
            asm volatile("s_waitcnt vmcnt(0)" ::: "memory");
            const unsigned dt = now32() - t0;
            if (r > 0) best = min(best, dt);
        }
        if (lane == 0) ((unsigned*)lds)[w] = best;
        __syncthreads();
        if (tid == 0) {
            const unsigned* bl = (const unsigned*)lds;
            const unsigned b0 = (bl[0] + bl[2]) + (bl[4] + bl[6]), b1 = (bl[1] + bl[3]) + (bl[5] + bl[7]);
            __builtin_amdgcn_raw_buffer_store_b32(b1 < b0 ? 2u : 1u, rs, (unsigned)CTRL_OFF + C_POL, 0, 16);
        }
        __syncthreads();
    }
#define POLINIT() const unsigned polo = (wait_flags(rs, C_POL, 1, 1) - 1u) * 8192u; const unsigned myc = (unsigned)(role & 3) * 65536u + polo;
    if (role < 112) { for (int i = 0; i < RSLEEP; ++i) __builtin_amdgcn_s_sleep(16); }
    unsigned nfail = 0, nfail2 = 0;
    unsigned long long* dbg = (unsigned long long*)(ws + CTRL_OFF + C_DBG);
#define STAMP(cond, idx)
#define TL(idx)
#define GT(idx, ref)
#define BT(i) {}
    constexpr unsigned TOFF = 70, TOFF2 = 30;
#define CLKUPD() { unsigned r1_, m1_; clock_ref(r1_, m1_); const unsigned dr = r1_ - r0, dm = m1_ - m0; \
        if (dr > 60u && dr < TA + TB + 44u) { const int err = (int)(dm * 256u) - (int)(cq * dr); cq = (unsigned)min(max((int)cq + (err >> 8), 4096), 6400); } r0 = r1_; m0 = m1_; }
#define CLKINIT() { for (int i_ = 0; i_ < 6; ++i_) __builtin_amdgcn_s_sleep(8); unsigned r1_, m1_; clock_ref(r1_, m1_); const unsigned dr = r1_ - r0, dm = m1_ - m0; \
        if (dr > 60u) cq = __builtin_amdgcn_readfirstlane(min(max((unsigned)((float)dm * 256.0f / (float)dr), 4096u), 6400u)); r0 = r1_; m0 = m1_; }
#define WAITR1() { unsigned tg_ = m0 - TOFF + (unsigned)(((int)(R1n - r0) * (int)cq) >> 8); \
        if (REREF && (int)(tg_ - now32()) > 700) { sleep_to(tg_ - 520u); unsigned ra_, ma_; clock_ref(ra_, ma_); tg_ = ma_ - TOFF + (unsigned)(((int)(R1n - ra_) * (int)cq) >> 8); } \
        sleep_to(tg_); }
    const unsigned pfrag_off = (rho >> 3) * 512 + g * 128 + (rho & 7) * 16;
    const unsigned TA = dS & 255, TB = dV & 255, TE = dVm & 255, TLATE = (dS >> 8) & 255, XUP = (dV >> 8) & 255, XDN = (dVm >> 8) & 255, X0 = (dVm >> 16) & 255;
    unsigned R2 = 0, R1n = 0;
    const bool is_mx = role >= 112;
    const bool is_v = !is_mx && (((role >> 2) & 1) == 0);
    const int j = (role >> 3) * 4 + (role & 3);
    const int h = role / 7, p = role % 7;
    const int hp = (lane >> 3) & 1, i8 = lane & 7;
    const v4u SENT4 = {SENT, SENT, SENT, SENT};

    if (is_mx) {
        const int hx = role - 112;
        POLINIT()
        if (w >= 4) {
            const unsigned orst = comm_off(1, hx * HS + 896 + (lane & 31) * 16, 0) + (lane >> 5) * 65536u + polo;
            const unsigned orst2 = comm_off(1, ST_OFF + (hx >> 1) * 128 + (hx & 1) * 64 + (lane & 1) * 16, 0) + ((lane >> 1) & 3) * 65536u + polo;
            const unsigned gfl2 = C_GU + 128u * (unsigned)min((14 * hx) / 4 + (lane & 7), (14 * hx + 13) / 4);
            unsigned kn = 0, gvo[2], glo[2];
            GADDR(gvo, glo, 2 * (w - 4), (w < 7 ? 2 : 1), -1)
            unsigned r0 = 0, m0 = 0, cq = 5900u;
            if (w == 4) {
                for (int sl = 0; sl < 8; ++sl) {
                    const unsigned sbz = comm_step_base(sl, 0);
                    st_sc1(SENT4, rs, orst, sbz); st_sc1(SENT4, rs, orst + 131072u, sbz);
                    if (lane < 8) st_sc1(SENT4, rs, orst2, sbz);
                }
                asm volatile("s_waitcnt vmcnt(0)" ::: "memory");
            }
            if (w == 7) { clock_ref(r0, m0); CLKINIT() }
            if (w == 5 && lane == 0) LDSV(L_UF) = 0u;
            for (int step = 0; step < NSEG; ++step) {
                const unsigned sbr = comm_step_base(step, 0) + myc;
                if (step > 0 && w == 7) WAITR1()
                bar();
                if (w < 7) nfail += gather_fast<2, false>(rs, sbr, ldsm, gvo, glo, 2 * (w - 4), lane, -1);
                else nfail += gather_fast<1, true>(rs, sbr, ldsm, gvo, glo, 6, lane, -1);
                bar();
                READCTRL()
                bar();
                if (w == 4 && step >= 4) {
                    __builtin_amdgcn_s_sleep(4);
                    const unsigned sbz = comm_step_base(step + 4, 0);
                    st_sc1(SENT4, rs, orst, sbz); st_sc1(SENT4, rs, orst + 131072u, sbz);
                    if (lane < 8) st_sc1(SENT4, rs, orst2, sbz);
                }
                if (w == 7) CLKUPD()
                if (w == 5 && kn < 64u) {
                    __builtin_amdgcn_s_sleep(4);
                    kn = wait_flags(rs, gfl2, 0, 1);
                    if (lane == 0) LDSV(L_UF) = kn;
                }
            }
        } else {
            __builtin_amdgcn_s_setprio(2);
            const int tau = w & 1, kh = w >> 1;
            v4u wa[8], wb[8];
            const unsigned gbase = (unsigned)((hx * 2 + tau) * 2 + kh) * 16384u + lane * 16;
            const unsigned g67 = gbase + (kh ? 4096u : 6144u);
#define GLOAD(wt, sb_) { _Pragma("unroll") for (int ks = 0; ks < 6; ++ks) wt[ks] = ld_sc1(rs, gbase + ks * 1024, sb_); \
                     wt[6] = ld_sc1(rs, g67, sb_); wt[7] = ld_sc1(rs, g67 + 1024, sb_); }
            const unsigned gfl = C_GU + 128u * (unsigned)min((14 * hx) / 4 + (lane & 7), (14 * hx + 13) / 4);
            unsigned known = wait_flags(rs, gfl, 2, 1);
            GLOAD(wa, segb(0, polo))
            GLOAD(wb, segb(1, polo))
            known = wait_flags(rs, gfl, FIRSTN, 1);
            float* sc = red;
            const int tq = lane & 7, ir = lane >> 3;
            const unsigned oes = comm_off(1, hx * HS + 896 + lane * 8, 0) + w * 65536u + polo;
            const unsigned oss = comm_off(1, ST_OFF + (hx >> 1) * 128 + (hx & 1) * 64 + ir * 4, 0) + w * 65536u + polo;
#define MXGT
#define MXSTEP(wt, step) { \
                const bool last = ((step) == NSEG - 1); \
                const unsigned sbs = comm_step_base(step, 0); \
                __builtin_amdgcn_s_barrier(); \
                bar(); \
                f4 aE = {0, 0, 0, 0}, aO = {0, 0, 0, 0}; \
                v4u mf[4]; \
                _Pragma("unroll") for (int q = 0; q < 3; ++q) mf[q] = *(const v4u*)(ldsm + (4 * kh + q) * 1024 + pfrag_off); \
                mf[3] = *(const v4u*)(ldsm + 3 * 1024 + pfrag_off); \
                SB0 \
                _Pragma("unroll") for (int q = 0; q < 3; ++q) { aE = mfma(wt[2 * q], mf[q], aE); aO = mfma(wt[2 * q + 1], mf[q], aO); } \
                if (kh == 0) { aE = mfma(wt[6], mf[3], aE); aO = mfma(wt[7], mf[3], aO); } \
                f4 xs = hp ? aO : aE; \
                *(f4*)(sc + ((2 * kh + hp) * 8 + i8) * 36 + 16 * tau + 4 * g) = xs; \
                bar(); \
                const float* sp = sc + ir * 36 + 4 * tq; \
                f4 a0 = (*(const f4*)(sp) + *(const f4*)(sp + 288)) + (*(const f4*)(sp + 576) + *(const f4*)(sp + 864)); \
                float mx; \
                asm("v_max3_f32 %0, %1, %2, %3\n\tv_max_f32 %0, %0, %4\n\ts_nop 1\n\t" \
                    "v_max_f32_dpp %0, %0, %0 quad_perm:[1,0,3,2] row_mask:0xf bank_mask:0xf\n\ts_nop 1\n\t" \
                    "v_max_f32_dpp %0, %0, %0 quad_perm:[2,3,0,1] row_mask:0xf bank_mask:0xf\n\ts_nop 1\n\t" \
                    "v_max_f32_dpp %0, %0, %0 row_half_mirror row_mask:0xf bank_mask:0xf" \
                    : "=&v"(mx) : "v"(a0[0]), "v"(a0[1]), "v"(a0[2]), "v"(a0[3])); \
                if (!last) { \
                    unsigned e0 = pk2(__builtin_amdgcn_exp2f(a0[0] - mx), __builtin_amdgcn_exp2f(a0[1] - mx)); \
                    unsigned e1 = pk2(__builtin_amdgcn_exp2f(a0[2] - mx), __builtin_amdgcn_exp2f(a0[3] - mx)); \
                    v2u eo; eo.x = min(e0, 0xFFFFFFFEu); eo.y = min(e1, 0xFFFFFFFEu); \
                    st2_sc1(eo, rs, oes, sbs); \
                    if (tq == 1) __builtin_amdgcn_raw_buffer_store_b32(min(asu(mx), 0xFFFFFFFEu), rs, oss, sbs, 16); \
                    MXGT \
                } \
                { const int sn = min((step) + 2, NSEG - 1); \
                  known = max(known, (unsigned)__builtin_amdgcn_readfirstlane(LDSV(L_UF))); \
                  if (known < (unsigned)sn + 1) known = wait_flags(rs, gfl, sn + 1, 1); \
                  const unsigned sb = segb(sn, polo); \
                  GLOAD(wt, sb) } }
            for (int step = 0; step < NSEG; step += 2) {
                MXSTEP(wa, step)
                MXSTEP(wb, step + 1)
            }
        }
    } else if (w >= 4) {
        const int wv = w - 4;
        v4u wtv[2][14];
        if (is_v) {
            const unsigned char* src = ws + BS_OFF + ((size_t)j * 8 + 2 * wv) * 14 * 1024;
#pragma unroll
            for (int t = 0; t < 2; ++t)
#pragma unroll
                for (int f = 0; f < 14; ++f) wtv[t][f] = *(const v4u*)(src + (t * 14 + f) * 1024 + lane * 16);
        } else {
#pragma unroll
            for (int t = 0; t < 2; ++t)
#pragma unroll
                for (int f = 0; f < 14; ++f) wtv[t][f] = v4u{0, 0, 0, 0};
        }
        POLINIT()
        __builtin_amdgcn_s_waitcnt(0x0F70);
        const bool cond = is_v && j == 0;
        const bool slow = w >= 6;
        const unsigned ufl = C_GU + 128u * (56 + j);
        const unsigned char* usrc = ws + (size_t)(64 + j) * 16384 + (wv & 1) * 4096 + lane * 16;
        const unsigned udst = (unsigned)(size_t)(__attribute__((address_space(3))) unsigned char*)lds + L_UF + (wv & 1) * 4096;
#define UDMA(sg) { const unsigned char* p_ = usrc + segb(sg, polo); const unsigned d_ = __builtin_amdgcn_readfirstlane(udst + ((sg) & 3) * 8192); \
            glds16x4(p_, d_); }
#define VPRIME() { \
                f4 e0 = {0, 0, 0, 0}, o0 = {0, 0, 0, 0}, e1 = {0, 0, 0, 0}, o1 = {0, 0, 0, 0}; \
                _Pragma("unroll") for (int q = 0; q < 7; ++q) { \
                    e0 = mfma(mf[q], wtv[0][2 * q], e0); o0 = mfma(mf[q], wtv[0][2 * q + 1], o0); \
                    e1 = mfma(mf[q], wtv[1][2 * q], e1); o1 = mfma(mf[q], wtv[1][2 * q + 1], o1); } \
                f4 cs; \
                _Pragma("unroll") for (int r = 0; r < 4; ++r) { \
                    auto sw = __builtin_amdgcn_permlane32_swap(asu(o0[r]), asu(e1[r]), false, false); \
                    unsigned d0 = sw[0], d1 = sw[1]; \
                    cs[r] = g < 2 ? e0[r] + asf(d1) : o1[r] + asf(d0); } \
                v2u vp; vp.x = pk2(cs[0], cs[1]); vp.y = pk2(cs[2], cs[3]); \
                *(v2u*)(lds + L_VP + wv * 512 + (2 * (g >> 1) + hp) * 128 + i8 * 16 + (g & 1) * 8) = vp; }
#define BYPROD(st_) { \
            if (w == 7) { v4u gmv = *(const v4u*)(lds + L_GM + lane * 16); \
                st_sc1(gmv, rs, (unsigned)(GM_OFF + ((size_t)h * 14 + 2 * p) * 512) + lane * 16, __builtin_amdgcn_readfirstlane((st_) * 16 * 14 * 512)); } \
            if (w == 6 && is_v) { v4u v0 = *(const v4u*)(lds + L_VP + lane * 16), v1 = *(const v4u*)(lds + L_VP + 1024 + lane * 16); \
                const unsigned sbv = __builtin_amdgcn_readfirstlane((st_) * 56 * 4 * 512); \
                st_sc1(v0, rs, (unsigned)(VP_OFF + (size_t)j * 2048) + lane * 16, sbv); \
                st_sc1(v1, rs, (unsigned)(VP_OFF + (size_t)j * 2048 + 1024) + lane * 16, sbv); } }
#define RESETS() if (w == 7 && step >= 4) { \
                    if (dorst) st_sc1(SENT4, rs, orst, comm_step_base(step + 4, 0)); \
                    if (cond && lane < 4) st_sc1(SENT4, rs, polo + 7168u + lane * 65536u, comm_step_base(step + 4, 0)); }
        const unsigned orst = (lane < 32 ? comm_off(1, h * HS + p * 128 + (lane & 7) * 16, 0) : (unsigned)(j * 128 + (lane & 7) * 16)) + ((lane >> 3) & 3) * 65536u + polo;
        const bool dorst = lane < 32 || is_v;
#define CSTORES() { \
                    if (cond && lane < 4) st_sc1(SENT4, rs, polo + 7168u + lane * 65536u, comm_step_base(step + 4, 0)); \
                    if (dorst) st_sc1(SENT4, rs, orst, comm_step_base(step + 4, 0)); \
                    v4u gmv = *(const v4u*)(lds + L_GM + lane * 16); \
                    st_sc1(gmv, rs, (unsigned)(GM_OFF + ((size_t)h * 14 + 2 * p) * 512) + lane * 16, __builtin_amdgcn_readfirstlane(step * 16 * 14 * 512)); }
        if (w == 7) {
            unsigned known = 0;
            for (int sl = 0; sl < 8; ++sl) {
                if (dorst && (sl > 0 || lane < 32)) st_sc1(SENT4, rs, orst, comm_step_base(sl, 0));
                if (cond && lane < 4 && sl > 0) st_sc1(SENT4, rs, polo + 7168u + lane * 65536u, comm_step_base(sl, 0));
            }
            asm volatile("s_waitcnt vmcnt(0)" ::: "memory");
            unsigned r0, m0, cq = 5900u; clock_ref(r0, m0);
            CLKINIT()
            if (is_v) { known = wait_flags(rs, ufl, 2, 1); UDMA(0) UDMA(1) asm volatile("s_waitcnt vmcnt(0)" ::: "memory"); }
            for (int step = 0; step < NSEG; ++step) {
                if (step > 0) WAITR1()
                bar();
                BT(0)
                CLKUPD()
                if (lane == 0) *(v4u*)(lds + L_FL + 512) = v4u{r0, m0 - TOFF2, cq, 0u};
                bar();
                __builtin_amdgcn_s_sleep(HSLEEP);
                READCTRL()
                LOADP(mf)
                bar();
                if (is_v) VPRIME()
                bar();
                asm volatile("s_waitcnt vmcnt(0)" ::: "memory");
                if (is_v && step + 3 < NSEG) UDMA(step + 2)
                if (is_v) bar();
                else CSTORES()
            }
            bar();
            asm volatile("s_waitcnt vmcnt(0)" ::: "memory");
            __builtin_amdgcn_s_barrier();
        } else if (w == 6) {
            unsigned known = 0;
            if (is_v) { known = wait_flags(rs, ufl, 2, 1); UDMA(0) UDMA(1) asm volatile("s_waitcnt vmcnt(0)" ::: "memory"); known = wait_flags(rs, ufl, FIRSTN, 1); }
            const bool reporter = (role == 4);
            const unsigned char* fsrc = ws + CTRL_OFF + ufl;
            const unsigned fdst = __builtin_amdgcn_readfirstlane((unsigned)(size_t)(__attribute__((address_space(3))) unsigned char*)lds + L_FL);
            if (lane == 0) LDSV(L_FL) = 0u;
#define VPSTORE(st_) { v4u v0 = *(const v4u*)(lds + L_VP + lane * 16), v1 = *(const v4u*)(lds + L_VP + 1024 + lane * 16); \
                const unsigned sbv = __builtin_amdgcn_readfirstlane((st_) * 56 * 4 * 512); \
                st_sc1(v0, rs, (unsigned)(VP_OFF + (size_t)j * 2048) + lane * 16, sbv); \
                st_sc1(v1, rs, (unsigned)(VP_OFF + (size_t)j * 2048 + 1024) + lane * 16, sbv); }
            for (int step = 0; step < NSEG; ++step) {
                bar();
                if (is_v) {
                    asm volatile("s_waitcnt vmcnt(2)" ::: "memory");
                    __builtin_amdgcn_s_sleep(ASLEEP);
                    if (step + 3 < NSEG) {
                        if (known < 64u) known = max(known, (unsigned)__builtin_amdgcn_readfirstlane(LDSV(L_FL)));
                        if (known < (unsigned)step + 3) known = wait_flags(rs, ufl, step + 3, 1);
                        UDMA(step + 2)
                    }
                    if (step > 0) VPSTORE(step - 1)
                    if (known < (unsigned)step + 10 && known < 64u && lane == 0) glds4(fsrc, fdst);
                }
                bar();
                __builtin_amdgcn_s_sleep(HSLEEP);
                LOADP(mf)
                bar();
                if (is_v) VPRIME()
                bar();
                if (reporter && lane == 0 && step >= 3) __builtin_amdgcn_raw_buffer_store_b32((unsigned)(step - 2), rs, (unsigned)CTRL_OFF + C_REC, 0, 16);
                if (is_v) { CSTORES() bar(); }
            }
            bar();
            if (is_v) VPSTORE(NSEG - 1)
            asm volatile("s_waitcnt vmcnt(0)" ::: "memory");
            __builtin_amdgcn_s_barrier();
        } else {
            unsigned extra = X0, calm = 0, ont = 0, gvo[2] = {0, 0}, glo[2] = {0, 0};
            if (cond && w == 5) {
                wait_flags(rs, ufl, FIRSTN, 1);
                unsigned t = rt32() + 120u;
                v2u c; c.x = min(t, 0xFFFFFFFEu); c.y = min(t + TB, 0xFFFFFFFEu);
                if (lane < KCOPY) st2_sc1(c, rs, polo + 7168u + lane * 65536u, 0);
                if (lane == 0) *(v2u*)(ldsm + 7168) = c;
            }
            const unsigned opub = polo + j * 128 + (lane >> 1) * 4 + (lane & 1) * 65536u;
            const unsigned opubS = comm_off(1, h * HS + p * 128 + (lane >> 1) * 4, 0) + (lane & 1) * 65536u + polo;
            for (int step = 0; step < NSEG; ++step) {
                const bool last = (step == NSEG - 1);
                const unsigned sbs = comm_step_base(step, 0);
                const unsigned sbr = sbs + myc;
                const unsigned sbn = comm_step_base(step + 1, 0);
                const int skl = (is_v && step > 0) ? j : -1;
                if (step < 2) GADDR(gvo, glo, w + 1, 1, skl)
                bar();
                if (w == 4 || cond) nfail += gather_fast<1, false>(rs, sbr, ldsm, gvo, glo, w + 1, lane, skl);
                else nfail += gather_fast<1, true>(rs, sbr, ldsm, gvo, glo, 6, lane, skl);
                bar();
                if (w == 5) BT(1)
                READCTRL()
                __builtin_amdgcn_s_sleep(HSLEEP);
                LOADP(mf)
                if (w == 4) __builtin_amdgcn_s_setprio(3);
                bar();
                if (w == 5) BT(2)
                if (w == 4) {
                    if (!last) {
                        unsigned o = reduce8(red, lane);
                        __builtin_amdgcn_raw_buffer_store_b32(o, rs, opubS, sbs, 16);
                        __builtin_amdgcn_raw_buffer_store_b32(o, rs, opubS + 131072u, sbs, 16);
                        BT(5)
                        if ((lane & 1) == 0) *(unsigned*)(lds + L_FL + 640 + (lane >> 1) * 4) = o;
                    }
                    __builtin_amdgcn_s_setprio(0);
                }
                if (is_v) {
                    VPRIME()
                    bar();
                    if (w == 5) BT(3)
                    unsigned xclk = 0;
                    if (cond && w == 5) {
                        const int cqv = (int)__builtin_amdgcn_readfirstlane(LDSV(L_FL + 520));
                        xclk = (unsigned)max((5950 - cqv) * XCK, 0) >> 13;
                    }
                    if (w == 4) __builtin_amdgcn_s_setprio(3);
                    bar();
                    if (w == 5 && !cond) BT(4)
                    if (!last) {
                        if (w == 4) {
                            unsigned o = reduce8(red2, lane);
                            __builtin_amdgcn_raw_buffer_store_b32(o, rs, opub, sbn, 16);
                            __builtin_amdgcn_raw_buffer_store_b32(o, rs, opub + 131072u, sbn, 16);
                            BT(6)
                            if ((lane & 1) == 0) *(unsigned*)(ldsm + j * 128 + (lane >> 1) * 4) = o;
                            __builtin_amdgcn_s_setprio(0);
                        }
                        if (cond && w == 5) {
                            const unsigned tp = rt32();
                            unsigned base = R1n;
                            if ((int)(tp + 4u - R1n) > 0) { base = tp + TLATE; if (ont) extra = min(extra + XUP, 10u); ont = 0; calm = 0; }
                            else { ont = 1; if (++calm >= XDN) { calm = 0; extra = extra ? extra - 1u : 0u; } }
                            const unsigned ex = extra + xclk;
                            v2u c; c.x = min(base + TA + ex, 0xFFFFFFFEu); c.y = min(base + TA + TB + 2u * ex, 0xFFFFFFFEu);
                            if (lane < KCOPY) st2_sc1(c, rs, polo + 7168u + lane * 65536u, sbn);
                            if (lane == 0) *(v2u*)(ldsm + 7168) = c;
                        }
                    }
                } else bar();
            }
            bar();
            __builtin_amdgcn_s_barrier();
            if (w == 4 && lane == 0) __builtin_amdgcn_raw_buffer_store_b32(1u, rs, (unsigned)CTRL_OFF + C_DN2 + role * 4, 0, 16);
        }
    } else {
        __builtin_amdgcn_s_setprio(2);
        const int b = 2 * p + (w >> 1), t = w & 1;
        v4u wt[14];
        {
            const unsigned char* src = ws + AS_OFF + ((size_t)(h * 14 + b) * 2 + t) * 14 * 1024;
#pragma unroll
            for (int ks = 0; ks < 14; ++ks) wt[ks] = *(const v4u*)(src + ks * 1024 + lane * 16);
        }
        POLINIT()
        __builtin_amdgcn_s_waitcnt(0x0F70);
        unsigned op[2][2], oe[2], ost, gvo[2] = {0, 0}, glo[2] = {0, 0};
        v4u pf[2][2], ef[2]; unsigned stt;
#pragma unroll
        for (int a = 0; a < 2; ++a) {
            const int hd = 4 * w + 2 * a + hp;
#pragma unroll
            for (int u = 0; u < 2; ++u) {
                const int q = 4 * u + g;
                const bool z = (q == 7) || (hd == h && q == p);
                op[a][u] = z ? 0x80000000u : comm_off(1, hd * HS + q * 128 + i8 * 16, 0);
            }
            oe[a] = comm_off(1, hd * HS + 896 + i8 * 64 + g * 16, 0);
        }
        const int hl = 4 * w + 2 * (g >> 1) + hp;
        ost = comm_off(1, ST_OFF + (hl >> 1) * 128 + (hl & 1) * 64 + i8 * 4, 0);
        const bool ownhead = (hl == h);
        v4u selLo = {0, 0, 0, 0}, selHi = {0, 0, 0, 0}, oneLo, oneHi;
        {
            const unsigned one = 0x3F80u << (16 * (rho & 1));
            const int wd = (rho & 7) >> 1;
            v4u sv; sv.x = wd == 0 ? one : 0; sv.y = wd == 1 ? one : 0; sv.z = wd == 2 ? one : 0; sv.w = wd == 3 ? one : 0;
            if (rho < 8) selLo = sv; else selHi = sv;
            const unsigned o2 = 0x3F803F80u;
            oneLo = rho < 8 ? v4u{o2, o2, o2, o2} : v4u{0, 0, 0, 0};
            oneHi = rho < 8 ? v4u{0, 0, 0, 0} : v4u{o2, o2, o2, o2};
        }
        for (int step = 0; step < NSEG; ++step) {
            const bool last = (step == NSEG - 1);
            const unsigned sbs = comm_step_base(step, 0);
            const unsigned sbr = sbs + myc;
            STAMP(step == 1, 0); STAMP(step == 63, 1);
            if (step < 2) GADDR(gvo, glo, (w == 0 ? 0 : w + 1), 2, ((is_v && step > 0) ? j : -1))
            bar();
            TL(0)
            const int skl = (is_v && step > 0) ? j : -1;
            if (w == 0) nfail += gather_fast<2, false>(rs, sbr, ldsm, gvo, glo, 0, lane, skl);
            else nfail += gather_fast<1, false>(rs, sbr, ldsm, gvo, glo, w + 1, lane, skl);
            bar();
            TL(1)
            f4 aE = {0, 0, 0, 0}, aO = {0, 0, 0, 0};
            v4u mf[7];
#pragma unroll
            for (int q_ = 0; q_ < 7; ++q_) mf[q_] = *(const v4u*)(ldsm + q_ * 1024 + pfrag_off);
            v2u mb = *(const v2u*)(ldsm + b * 512 + g * 128 + i8 * 16 + t * 8);
            const unsigned cwx = *(const unsigned*)(ldsm + 7168);
            const v4u ckr = *(const v4u*)(lds + L_FL + 512);
            SB0
#pragma unroll
            for (int q = 0; q < 7; ++q) { aE = mfma(wt[2 * q], mf[q], aE); aO = mfma(wt[2 * q + 1], mf[q], aO); }
            f4 xs = hp ? aO : aE;
            v2u gh; gh.x = pk2(xs[0], xs[1]); gh.y = pk2(xs[2], xs[3]);
            f4 sp = mfma16(gh, mb, f4{0, 0, 0, 0});
            if (rho < 8) {
                float* rq = red + rho * 100 + (4 * (g & 1)) * 12 + 2 * w + (g >> 1);
                rq[0] = sp[0]; rq[12] = sp[1]; rq[24] = sp[2]; rq[36] = sp[3];
            }
            SB0
            TL(7)
            bar();
#define GMSTAGE() { f4 fs; _Pragma("unroll") for (int r = 0; r < 4; ++r) fs[r] = xs[r] + dpp_xor8(xs[r]); \
                v2u gm; gm.x = pk2(fs[0], fs[1]); gm.y = pk2(fs[2], fs[3]); \
                if (rho < 8) *(v2u*)(lds + L_GM + (w >> 1) * 512 + g * 128 + rho * 16 + t * 8) = gm; }
            if (!is_v) { GMSTAGE() bar(); continue; }
            if (last) { GMSTAGE() bar(); bar(); continue; }
#define E_LOADS(sb_) { stt = __builtin_amdgcn_raw_buffer_load_b32(rs, ost, sb_, 16); ef[0] = ld_sc1(rs, oe[0], sb_); ef[1] = ld_sc1(rs, oe[1], sb_); SB0 }
#define P_LOADS(sb_) { pf[0][0] = ld_sc1(rs, op[0][0], sb_); SB0 pf[1][0] = ld_sc1(rs, op[1][0], sb_); SB0 pf[0][1] = ld_sc1(rs, op[0][1], sb_); SB0 pf[1][1] = ld_sc1(rs, op[1][1], sb_); SB0 }
            const unsigned tc2 = (unsigned)__builtin_amdgcn_readfirstlane(ckr.y) + (unsigned)(((int)(__builtin_amdgcn_readfirstlane(cwx) - __builtin_amdgcn_readfirstlane(ckr.x)) * (int)__builtin_amdgcn_readfirstlane(ckr.z)) >> 8);
            spin_to(tc2 - TE * 23u);
            E_LOADS(sbr)
            asm volatile("" ::: "memory");
            spin_to(tc2);
            TL(3)
            P_LOADS(sbr)
            asm volatile("" ::: "memory");
            SB0
            if (w == 1) BT(7)
            GMSTAGE()
            v4u uf[2];
            uf[0] = *(const v4u*)(lds + L_UF + (step & 3) * 8192 + w * 2048 + lane * 16); uf[1] = *(const v4u*)(lds + L_UF + (step & 3) * 8192 + w * 2048 + 1024 + lane * 16);
            bar();
            v2u vpA, vpB;
            f4 sini = {0, 0, 0, 0};
            {
                v2u vp = *(const v2u*)(lds + L_VP + w * 512 + (2 * (g >> 1) + hp) * 128 + i8 * 16 + (g & 1) * 8);
                v2u ow = *(const v2u*)(lds + L_FL + 640 + i8 * 16 + (g & 1) * 8);
                vpA = g < 2 ? vp : v2u{0, 0};
                vpB = g < 2 ? v2u{0, 0} : vp;
                if (ownhead) { sini[0] = bflo(ow.x); sini[1] = bfhi(ow.x); sini[2] = bflo(ow.y); sini[3] = bfhi(ow.y); }
            }
            asm volatile("" ::: "memory");
            SB0
            TL(4)
            bool okall;
            auto combine = [&]() __attribute__((always_inline)) {
                f4 ls = mfma(oneLo, ef[0], f4{0, 0, 0, 0});
                ls = mfma(oneHi, ef[1], ls);
                f4 x0 = mfma(uf[0], ef[0], f4{0, 0, 0, 0});
                f4 x1 = mfma(uf[1], ef[1], f4{0, 0, 0, 0});
                unsigned t2 = vmax4(vmax4(stt, ef[0]), ef[1]);
                float lsv = ls[0];
                asm volatile("" : "+v"(t2), "+v"(lsv));
                SB0
                f4 sacc = mfma(selLo, pf[0][0], sini);
                sacc = mfma(selHi, pf[1][0], sacc);
                sacc = mfma(selLo, pf[0][1], sacc);
                sacc = mfma(selHi, pf[1][1], sacc);
                SB0
                unsigned t0 = vmax4(vmax4(t2, pf[0][0]), pf[0][1]), t1 = vmax4(vmax4(0, pf[1][0]), pf[1][1]);
                asm volatile("" : "+v"(t0), "+v"(t1));
                SB0
                const float mloc = asf(stt);
                float mx = vmax3(vmax3_mfma(sacc[0], sacc[1], sacc[2]), sacc[3], mloc);
                mx = rmax16(mx);
                float p0 = __builtin_amdgcn_exp2f(sacc[0] - mx), p1 = __builtin_amdgcn_exp2f(sacc[1] - mx), p2 = __builtin_amdgcn_exp2f(sacc[2] - mx), p3 = __builtin_amdgcn_exp2f(sacc[3] - mx);
                float f = __builtin_amdgcn_exp2f(mloc - mx);
                v2u pb; pb.x = pk2(p0, p1); pb.y = pk2(p2, p3);
                float zq = (p0 + p1) + (p2 + p3);
                SB0
                f4 cmA = mfma16(vpA, pb, f4{0, 0, 0, 0});
                f4 cmB = mfma16(vpB, pb, f4{0, 0, 0, 0});
                SB0
                float z = xsum16(zq);
                auto fs = __builtin_amdgcn_permlane32_swap(asu(f), asu(f), false, false);
                unsigned fa = fs[0], fb = fs[1];
                z += f * lsv;
                float rz = __builtin_amdgcn_rcpf(z);
                okall = __all(max(t0, t1) != SENT);
                f4 ya = x0 * asf(fa) + cmA, yb = x1 * asf(fb) + cmB;
                auto rs2 = __builtin_amdgcn_permlane32_swap(asu(rz), asu(rz), false, false);
                unsigned ra = rs2[0], rb = rs2[1];
                f4 cx = ya * asf(ra) + yb * asf(rb);
                if (hp == (g >> 1)) {
                    float* rq = red2 + i8 * 100 + (4 * (g & 1)) * 12 + 2 * w + hp;
                    rq[0] = cx[0]; rq[12] = cx[1]; rq[24] = cx[2]; rq[36] = cx[3];
                }
            };
            combine();
            if (!okall) {
                nfail2++;
                for (unsigned spin = 0; spin < SPIN_MAX && !okall; ++spin) {
                    __builtin_amdgcn_s_sleep(1);
                    asm volatile("" ::: "memory");
                    E_LOADS(sbr)
                    P_LOADS(sbr)
                    combine();
                }
            }
            TL(6)
            bar();
        }
        bar();
        __builtin_amdgcn_s_barrier();
    }
}

DI void signal_add(unsigned char* ws, unsigned off) {
    asm volatile("s_waitcnt vmcnt(0)" ::: "memory");
    __syncthreads();
    if (threadIdx.x == 0) __hip_atomic_fetch_add((unsigned*)(ws + CTRL_OFF + off), 1u, __ATOMIC_RELAXED, __HIP_MEMORY_SCOPE_AGENT);
}
DI void wg_wait(__amdgpu_buffer_rsrc_t rs, unsigned off_lane, unsigned need, int slp) {
    if (threadIdx.x < 64) wait_flags(rs, off_lane, need, slp);
    __syncthreads();
}

DI void gldsx16(const unsigned char* gsrc, unsigned lds_dst) {
    unsigned keep;
    asm volatile("s_mov_b32 %0, m0\n\ts_mov_b32 m0, %2\n\ts_nop 0\n\tglobal_load_lds_dwordx4 %1, off\n\ts_mov_b32 m0, %0" : "=&s"(keep) : "v"(gsrc), "s"(lds_dst) : "memory");
}
DI void gu_role(unsigned char* ws, unsigned char* lds, const int gw) {
    const int tid = threadIdx.x, lane = tid & 63, w8 = __builtin_amdgcn_readfirstlane(tid >> 6);
    const int w = w8 & 3, sp = w8 >> 2;
    const bool isG = gw < 56;
    const unsigned ldsb = __builtin_amdgcn_readfirstlane((unsigned)(size_t)(__attribute__((address_space(3))) unsigned char*)lds);
    const int nat0 = (((lane & 15) >> 2) & 1) * 14464 + ((4 * ((lane & 15) >> 3) + (lane & 3)) + 16 * (lane >> 4)) * 16;
    const unsigned char* xsrc = ws + XF_OFF + w8 * 1024 + lane * 16;
#define XDMA(pr_) { _Pragma("unroll") for (int i = 0; i < 7; ++i) gldsx16(xsrc + (size_t)(pr_) * 57344 + i * 8192, ldsb + ((pr_) & 1) * 57856 + (i * 8 + w8) * 1024 + ((i * 8 + w8) / 14) * 128); }
    XDMA(0)
    v4u wt[2][14];
    const unsigned char* wsrc = isG ? ws + AS_OFF + (size_t)(4 * gw + w) * 28 * 1024 : ws + BS_OFF + ((size_t)(gw - 56) * 8 + 2 * w) * 14 * 1024;
    unsigned char* xch = lds + 57856;
    v4u tmp[14];
#pragma unroll
    for (int ks = 0; ks < 14; ++ks) tmp[ks] = *(const v4u*)(wsrc + (sp * 14 + ks) * 1024 + lane * 16);
    SB0
#pragma unroll
    for (int half = 0; half < 2; ++half) {
        if (half) bar();
#pragma unroll
        for (int ks = 0; ks < 7; ++ks) *(v4u*)(xch + (w8 * 7 + ks) * 1024 + lane * 16) = tmp[half * 7 + ks];
        bar();
#pragma unroll
        for (int t = 0; t < 2; ++t)
#pragma unroll
            for (int ks = 0; ks < 7; ++ks) wt[t][half * 7 + ks] = *(const v4u*)(xch + ((w + 4 * t) * 7 + ks) * 1024 + lane * 16);
    }
    __amdgpu_buffer_rsrc_t rs = mkrs(ws, 0x7fffffff);
    asm volatile("s_waitcnt vmcnt(0)" ::: "memory");
    bar();
    const int bb = 4 * gw + w, hh = bb / 14, bq = bb % 14;
    const unsigned obase = isG ? (unsigned)(hh * 4 + (bq >> 3)) * 16384u + (bq & 7) * 1024 + lane * 16 : (unsigned)(64 + gw - 56) * 16384u + 2 * w * 1024 + lane * 16;
    const unsigned ostep = isG ? 32768u : 1024u;
    unsigned polo = 0;
    for (int pr = 0; pr < 32; ++pr) {
        const unsigned char* cur = lds + (pr & 1) * 57856 + sp * 28928;
        if (pr + 1 < 32) XDMA(pr + 1)
        f4 acc[2][2] = {{f4{0,0,0,0}, f4{0,0,0,0}}, {f4{0,0,0,0}, f4{0,0,0,0}}};
        {
            const unsigned char* p0 = cur + (isG ? nat0 : lane * 16);
            const unsigned char* p1 = p0 + (isG ? 128 : 14464);
            v4u xa[3], xb[3];
#pragma unroll
            for (int k = 0; k < 3; ++k) { xa[k] = *(const v4u*)(p0 + k * 1024); xb[k] = *(const v4u*)(p1 + k * 1024); }
            if (isG) {
#pragma unroll
                for (int ks = 0; ks < 14; ++ks) {
                    const v4u x0 = xa[ks % 3], x1 = xb[ks % 3];
                    acc[0][0] = mfma(wt[0][ks], x0, acc[0][0]); acc[1][0] = mfma(wt[1][ks], x0, acc[1][0]);
                    acc[0][1] = mfma(wt[0][ks], x1, acc[0][1]); acc[1][1] = mfma(wt[1][ks], x1, acc[1][1]);
                    if (ks + 3 < 14) { xa[ks % 3] = *(const v4u*)(p0 + (ks + 3) * 1024); xb[ks % 3] = *(const v4u*)(p1 + (ks + 3) * 1024); }
                    SB0
                }
            } else {
#pragma unroll
                for (int ks = 0; ks < 14; ++ks) {
                    const v4u x0 = xa[ks % 3], x1 = xb[ks % 3];
                    acc[0][0] = mfma(x0, wt[0][ks], acc[0][0]); acc[1][0] = mfma(x1, wt[0][ks], acc[1][0]);
                    acc[0][1] = mfma(x0, wt[1][ks], acc[0][1]); acc[1][1] = mfma(x1, wt[1][ks], acc[1][1]);
                    if (ks + 3 < 14) { xa[ks % 3] = *(const v4u*)(p0 + (ks + 3) * 1024); xb[ks % 3] = *(const v4u*)(p1 + (ks + 3) * 1024); }
                    SB0
                }
            }
        }
        if (pr == 0) polo = (wait_flags(rs, C_POL, 1, 1) - 1u) * 8192u;
        const unsigned so = segb(2 * pr + sp, polo);
        st_sc1(pack8(acc[0][0], acc[1][0]), rs, obase, so);
        st_sc1(pack8(acc[0][1], acc[1][1]), rs, obase + ostep, so);
        const bool drain = (pr < 4) || (pr == 31);
        if (drain) asm volatile("s_waitcnt vmcnt(0)" ::: "memory");
        else asm volatile("s_waitcnt vmcnt(2)" ::: "memory");
        bar();
        if (tid == 448) __builtin_amdgcn_raw_buffer_store_b32((unsigned)(drain ? 2 * pr + 2 : 2 * pr), rs, (unsigned)CTRL_OFF + C_GU + 128u * gw, 0, 16);
    }
}

DI void a0_prep(unsigned char* ws, const int u, const u16* __restrict__ wqkv, const u16* __restrict__ wphi, const u16* __restrict__ awo) {
    const int tid = threadIdx.x, lane = tid & 63, w = __builtin_amdgcn_readfirstlane(tid >> 6);
    const int rho = lane & 15, g = lane >> 4;
    __amdgpu_buffer_rsrc_t rs = mkrs(ws, 0x7fffffff);
    {
        const int which = w >> 2, h2 = u;
        v4u bf[2];
#pragma unroll
        for (int t = 0; t < 2; ++t) {
            const int f = perm32(t, rho);
            unsigned e[8];
#pragma unroll
            for (int jj = 0; jj < 8; ++jj) { const int d = 8 * g + jj; e[jj] = (d < 28 && f < 28) ? wphi[(h2 * 28 + d) * 28 + f] : 0u; }
            bf[t] = v4u{e[0] | (e[1] << 16), e[2] | (e[3] << 16), e[4] | (e[5] << 16), e[6] | (e[7] << 16)};
        }
        v4u af[4][2];
#pragma unroll
        for (int i = 0; i < 4; ++i) {
            const int ks = min((w & 3) + 4 * i, 13);
#pragma unroll
            for (int t = 0; t < 2; ++t) {
                const u16* p = wqkv + (size_t)(32 * ks + perm32(t, rho)) * 1344 + which * 448 + h2 * 28 + g * 8;
                v2u lo = *(const v2u*)p, hi = {0, 0};
                if (g < 3) hi = *(const v2u*)(p + 4);
                af[i][t] = v4u{lo.x, lo.y, hi.x, hi.y};
            }
        }
#pragma unroll
        for (int i = 0; i < 4; ++i) {
            const int ks = (w & 3) + 4 * i;
            if (ks < 14) {
#pragma unroll
                for (int t = 0; t < 2; ++t) {
                    f4 d0 = mfma(af[i][0], bf[t], f4{0, 0, 0, 0}), d1 = mfma(af[i][1], bf[t], f4{0, 0, 0, 0});
                    st_sc1(pack8(d0 * LOG2E, d1 * LOG2E), rs, (unsigned)(WQF_OFF + which * WQF_ONE + ((size_t)(h2 * 2 + t) * 14 + ks) * 1024) + lane * 16, 0);
                }
            }
        }
    }
#pragma unroll
    for (int i = 0; i < 4; ++i) {
        const int fi = w + 8 * i;
        if (fi < 28) {
            const int t = fi / 14, ks = fi % 14;
            const int d = 16 * t + rho;
            unsigned e[8], e2[8];
            const int cb = fi >> 1, t2 = fi & 1; const int c = 32 * cb + perm32(t2, rho);
#pragma unroll
            for (int jj = 0; jj < 8; ++jj) {
                e[jj] = d < 28 ? wqkv[(size_t)(32 * ks + 8 * g + jj) * 1344 + 896 + u * 28 + d] : 0u;
                const int sl = 8 * g + jj; e2[jj] = sl < 28 ? awo[(size_t)(u * 28 + sl) * 448 + c] : 0u;
            }
            st_sc1(v4u{e[0] | (e[1] << 16), e[2] | (e[3] << 16), e[4] | (e[5] << 16), e[6] | (e[7] << 16)}, rs, (unsigned)(WQF_OFF + 2 * WQF_ONE + (size_t)(u * 28 + fi) * 1024) + lane * 16, 0);
            st_sc1(v4u{e2[0] | (e2[1] << 16), e2[2] | (e2[3] << 16), e2[4] | (e2[5] << 16), e2[6] | (e2[7] << 16)}, rs, (unsigned)(WOF_OFF + (size_t)((cb * 2 + t2) * 16 + u) * 1024) + lane * 16, 0);
        }
    }
}
DI void phi_pair(f4 d0, f4 d1, int g, v4u& pos, v4u& neg) {
    float v[8] = {d0[0], d0[1], d0[2], d0[3], d1[0], d1[1], d1[2], d1[3]};
    const bool tail = (g == 3);
    float mx = fmaxf(fmaxf(v[0], v[1]), fmaxf(v[2], v[3])), mn = fminf(fminf(v[0], v[1]), fminf(v[2], v[3]));
    float mx2 = fmaxf(fmaxf(v[4], v[5]), fmaxf(v[6], v[7])), mn2 = fminf(fminf(v[4], v[5]), fminf(v[6], v[7]));
    mx = tail ? mx : fmaxf(mx, mx2); mn = tail ? mn : fminf(mn, mn2);
    mx = xmax32(xmax16(mx)); mn = -xmax32(xmax16(-mn));
    float ep[8], en[8], sp = 0.f, sn = 0.f;
#pragma unroll
    for (int k = 0; k < 8; ++k) {
        ep[k] = __builtin_amdgcn_exp2f(v[k] - mx); en[k] = __builtin_amdgcn_exp2f(mn - v[k]);
        if (k >= 4) { ep[k] = tail ? 0.f : ep[k]; en[k] = tail ? 0.f : en[k]; }
        sp += ep[k]; sn += en[k];
    }
    sp = xsum32(xsum16(sp)); sn = xsum32(xsum16(sn));
    const float rp = __builtin_amdgcn_rcpf(sp), rn = __builtin_amdgcn_rcpf(sn);
    pos = v4u{pk2(ep[0] * rp, ep[1] * rp), pk2(ep[2] * rp, ep[3] * rp), pk2(ep[4] * rp, ep[5] * rp), pk2(ep[6] * rp, ep[7] * rp)};
    neg = v4u{pk2(en[0] * rn, en[1] * rn), pk2(en[2] * rn, en[3] * rn), pk2(en[4] * rn, en[5] * rn), pk2(en[6] * rn, en[7] * rn)};
}
DI void stage_x64(const u16* __restrict__ ax, unsigned char* lds, int blk64) {
    int tid = threadIdx.x; asm volatile("" : "+v"(tid));
    const int lane = tid & 63, w = tid >> 6;
    v4u stg[7];
#pragma unroll
    for (int i = 0; i < 7; ++i) {
        const int f = i * 8 + w; const int q = f / 14, ks = f % 14;
        stg[i] = *(const v4u*)(ax + (size_t)(blk64 * 64 + 32 * (q >> 1) + perm32(q & 1, lane & 15)) * D + ks * 32 + (lane >> 4) * 8);
    }
#pragma unroll
    for (int i = 0; i < 7; ++i) *(v4u*)(lds + (i * 512 + tid) * 16) = stg[i];
}

DI void a1_unit(unsigned char* ws, unsigned char* lds, const int u, const u16* __restrict__ ax) {
    const int tid = threadIdx.x, lane = tid & 63, w = __builtin_amdgcn_readfirstlane(tid >> 6);
    const int rho = lane & 15, g = lane >> 4;
    __amdgpu_buffer_rsrc_t rs = mkrs(ws, 0x7fffffff);
    unsigned char* T = lds + 57344 + w * 9216;
    constexpr int TS = 144;
    f4 kv[2][4][2];
#pragma unroll
    for (int a = 0; a < 2; ++a)
#pragma unroll
        for (int b = 0; b < 4; ++b) { kv[a][b][0] = f4{0,0,0,0}; kv[a][b][1] = f4{0,0,0,0}; }
    for (int pass = 0; pass < 2; ++pass) {
        __syncthreads();
        stage_x64(ax, lds, 2 * u + pass);
        __syncthreads();
#pragma unroll
        for (int hi = 0; hi < 2; ++hi) {
            const int h = 2 * w + hi;
            const unsigned wk = (unsigned)(WQF_OFF + WQF_ONE + (size_t)h * 28672) + lane * 16;
            const unsigned wv = (unsigned)(WQF_OFF + 2 * WQF_ONE + (size_t)h * 28672) + lane * 16;
            f4 ak[2][4], av[2][4];
#pragma unroll
            for (int t = 0; t < 2; ++t)
#pragma unroll
                for (int q = 0; q < 4; ++q) { ak[t][q] = f4{0,0,0,0}; av[t][q] = f4{0,0,0,0}; }
#pragma unroll 2
            for (int ks = 0; ks < 14; ++ks) {
                v4u k0 = ld_sc1(rs, wk + ks * 1024, 0), k1 = ld_sc1(rs, wk + (14 + ks) * 1024, 0);
                v4u v0 = ld_sc1(rs, wv + ks * 1024, 0), v1 = ld_sc1(rs, wv + (14 + ks) * 1024, 0);
#pragma unroll
                for (int q = 0; q < 4; ++q) {
                    v4u x = *(const v4u*)(lds + (q * 14 + ks) * 1024 + lane * 16);
                    ak[0][q] = mfma(k0, x, ak[0][q]); ak[1][q] = mfma(k1, x, ak[1][q]);
                    av[0][q] = mfma(x, v0, av[0][q]); av[1][q] = mfma(x, v1, av[1][q]);
                }
            }
#pragma unroll
            for (int q = 0; q < 4; ++q) {
                v4u pos, neg; phi_pair(ak[0][q], ak[1][q], g, pos, neg);
                const int tok = 32 * (q >> 1) + perm32(q & 1, rho);
                *(v4u*)(T + tok * TS + g * 16) = pos;
                *(v4u*)(T + tok * TS + 64 + g * 16) = neg;
            }
            asm volatile("s_waitcnt lgkmcnt(0)" ::: "memory");
            v4u vf[2][2];
#pragma unroll
            for (int bk = 0; bk < 2; ++bk)
#pragma unroll
                for (int t = 0; t < 2; ++t) {
                    vf[bk][t] = pack8(av[t][2 * bk], av[t][2 * bk + 1]);
                    if (t == 1 && rho == 12) vf[bk][t] = v4u{0x3F803F80u, 0x3F803F80u, 0x3F803F80u, 0x3F803F80u};
                }
            const unsigned trb = (8 * g + (rho >> 2)) * TS + (lane & 3) * 8;
#pragma unroll
            for (int bk = 0; bk < 2; ++bk)
#pragma unroll
                for (int ft = 0; ft < 4; ++ft) {
                    v2u lo = trread(T + trb + (32 * bk) * TS + ft * 32), hi2 = trread(T + trb + (32 * bk + 4) * TS + ft * 32);
                    v4u pa = {lo.x, lo.y, hi2.x, hi2.y};
                    kv[hi][ft][0] = mfma(pa, vf[bk][0], kv[hi][ft][0]);
                    kv[hi][ft][1] = mfma(pa, vf[bk][1], kv[hi][ft][1]);
                }
            asm volatile("s_waitcnt lgkmcnt(0)" ::: "memory");
        }
    }
#pragma unroll
    for (int hi = 0; hi < 2; ++hi)
#pragma unroll
        for (int ft = 0; ft < 4; ++ft)
#pragma unroll
            for (int dt = 0; dt < 2; ++dt)
                st_sc1(__builtin_bit_cast(v4u, kv[hi][ft][dt]), rs, (unsigned)(KVP_OFF + ((size_t)(u * 16 + 2 * w + hi) * 8 + ft * 2 + dt) * 1024) + lane * 16, 0);
}
DI void a15_unit(unsigned char* ws, unsigned char* lds, const int h) {
    const int tid = threadIdx.x, lane = tid & 63, w = tid >> 6;
    __amdgpu_buffer_rsrc_t rs = mkrs(ws, 0x7fffffff);
    f4 s = {0, 0, 0, 0};
    v4u t[16];
#pragma unroll
    for (int u = 0; u < 16; ++u) t[u] = ld_sc1(rs, (unsigned)(KVP_OFF + ((size_t)(u * 16 + h) * 8) * 1024) + tid * 16, 0);
#pragma unroll
    for (int u = 0; u < 16; ++u) s += __builtin_bit_cast(f4, t[u]);
    const int ft = w >> 1, dt = w & 1, rho = lane & 15, g = lane >> 4;
    __syncthreads();
    v2u o; o.x = pk2(s[0], s[1]); o.y = pk2(s[2], s[3]);
    *(v2u*)(lds + (16 * dt + rho) * 128 + (16 * ft + 4 * g) * 2) = o;
    __syncthreads();
    if (tid < 256) {
        const int fr = tid >> 6;
        const int tt = fr >> 1, kb = fr & 1;
        v4u v = *(const v4u*)(lds + perm32(tt, rho) * 128 + (32 * kb + 8 * g) * 2);
        st_sc1(v, rs, (unsigned)(KVF_OFF + (size_t)(h * 4 + fr) * 1024) + lane * 16, 0);
    }
}
DI void a2_unit(unsigned char* ws, unsigned char* lds, const int u, u16* __restrict__ out, const u16* __restrict__ ax) {
    int tid = threadIdx.x; asm volatile("" : "+v"(tid));
    const int lane = tid & 63, w = __builtin_amdgcn_readfirstlane(tid >> 6);
    const int rho = lane & 15, g = lane >> 4;
    __amdgpu_buffer_rsrc_t rs = mkrs(ws, 0x7fffffff);
    unsigned char* O = lds + 57344;
    __syncthreads();
    stage_x64(ax, lds, u);
    wg_wait(rs, C_A0, 16, 4);
    v4u pos[2][4], neg[2][4];
#pragma unroll
    for (int hi = 0; hi < 2; ++hi) {
        const int h = 2 * w + hi;
        const unsigned wq = (unsigned)(WQF_OFF + (size_t)h * 28672) + lane * 16;
        f4 aq[2][4];
#pragma unroll
        for (int t = 0; t < 2; ++t)
#pragma unroll
            for (int q = 0; q < 4; ++q) aq[t][q] = f4{0,0,0,0};
#pragma unroll 2
        for (int ks = 0; ks < 14; ++ks) {
            v4u k0 = ld_sc1(rs, wq + ks * 1024, 0), k1 = ld_sc1(rs, wq + (14 + ks) * 1024, 0);
#pragma unroll
            for (int q = 0; q < 4; ++q) {
                v4u x = *(const v4u*)(lds + (q * 14 + ks) * 1024 + lane * 16);
                aq[0][q] = mfma(k0, x, aq[0][q]); aq[1][q] = mfma(k1, x, aq[1][q]);
            }
        }
#pragma unroll
        for (int q = 0; q < 4; ++q) phi_pair(aq[0][q], aq[1][q], g, pos[hi][q], neg[hi][q]);
    }
    wg_wait(rs, C_KV, 16, 4);
#pragma unroll
    for (int hi = 0; hi < 2; ++hi) {
        const int h = 2 * w + hi;
        v4u kf[4];
#pragma unroll
        for (int fr = 0; fr < 4; ++fr) kf[fr] = ld_sc1(rs, (unsigned)(KVF_OFF + (size_t)(h * 4 + fr) * 1024) + lane * 16, 0);
#pragma unroll
        for (int q = 0; q < 4; ++q) {
            f4 n0 = mfma(kf[0], pos[hi][q], f4{0,0,0,0}); n0 = mfma(kf[1], neg[hi][q], n0);
            f4 n1 = mfma(kf[2], pos[hi][q], f4{0,0,0,0}); n1 = mfma(kf[3], neg[hi][q], n1);
            const float den = asf((unsigned)__builtin_amdgcn_ds_bpermute((48 + rho) * 4, (int)asu(n1[0])));
            const float rd = 1.0f / (den + 1e-6f);
            if (g == 3) n1 = f4{0, 0, 0, 0};
            *(v4u*)(O + (h * 4 + q) * 1024 + lane * 16) = pack8(n0 * rd, n1 * rd);
        }
    }
    __syncthreads();
    for (int cb = w; cb < 14; cb += 8) {
        f4 acc[2][4];
#pragma unroll
        for (int t = 0; t < 2; ++t)
#pragma unroll
            for (int q = 0; q < 4; ++q) acc[t][q] = f4{0,0,0,0};
        const unsigned wo = (unsigned)(WOF_OFF + (size_t)cb * 32768) + lane * 16;
#pragma unroll 4
        for (int h = 0; h < 16; ++h) {
            v4u w0 = ld_sc1(rs, wo + h * 1024, 0), w1 = ld_sc1(rs, wo + (16 + h) * 1024, 0);
#pragma unroll
            for (int q = 0; q < 4; ++q) {
                v4u o = *(const v4u*)(O + (h * 4 + q) * 1024 + lane * 16);
                acc[0][q] = mfma(w0, o, acc[0][q]); acc[1][q] = mfma(w1, o, acc[1][q]);
            }
        }
#pragma unroll
        for (int q = 0; q < 4; ++q) {
            const int tok = u * 64 + 32 * (q >> 1) + perm32(q & 1, rho);
            *(v4u*)(out + (size_t)tok * OUTW + 32 * cb + 8 * g) = pack8(acc[0][q], acc[1][q]);
        }
    }
}

DI void p2_unit(unsigned char* ws, unsigned char* lds, const int unit, u16* __restrict__ out) {
    const int seg = unit >> 1, chalf = unit & 1;
    int tid = threadIdx.x; asm volatile("" : "+v"(tid));
    const int lane = tid & 63, w = __builtin_amdgcn_readfirstlane(tid >> 6);
    const int rho = lane & 15, g = lane >> 4;
    __amdgpu_buffer_rsrc_t rs = mkrs(ws, 0x7fffffff);
    unsigned char* xl = lds + 40960;
    __syncthreads();
    for (int i = tid; i < 1792; i += 512) *(v4u*)(xl + i * 16) = *(const v4u*)(ws + XF_OFF + (size_t)seg * 28672 + i * 16);
    if (tid < 64 && seg > 8) wait_flags(rs, C_REC, seg - 8, 16);
    if (tid < 128) wait_flags(rs, C_GU + 128u * (unsigned)min(tid, 111), seg + 1, 8);
    __syncthreads();
    const unsigned sgb = segb(seg, (__builtin_amdgcn_readfirstlane(ldflag(rs, C_POL)) - 1u) * 8192u);
    f4 sx[2][2][2];
#pragma unroll
    for (int hi = 0; hi < 2; ++hi) {
#pragma unroll
        for (int a = 0; a < 2; ++a) { sx[hi][a][0] = f4{0,0,0,0}; sx[hi][a][1] = f4{0,0,0,0}; }
        const unsigned gb = sgb + (unsigned)(2 * w + hi) * 65536u + lane * 16;
#pragma unroll
        for (int ks = 0; ks < 14; ++ks) {
            v4u g0 = ld_sc1(rs, gb + (ks >> 3) * 16384 + (ks & 7) * 1024, 0), g1 = ld_sc1(rs, gb + 32768 + (ks >> 3) * 16384 + (ks & 7) * 1024, 0);
            v4u x0 = *(const v4u*)(xl + ks * 1024 + lane * 16), x1 = *(const v4u*)(xl + (14 + ks) * 1024 + lane * 16);
            sx[hi][0][0] = mfma(g0, x0, sx[hi][0][0]); sx[hi][0][1] = mfma(g0, x1, sx[hi][0][1]);
            sx[hi][1][0] = mfma(g1, x0, sx[hi][1][0]); sx[hi][1][1] = mfma(g1, x1, sx[hi][1][1]);
        }
    }
    f4* sxl = (f4*)(lds + 69632) + tid;
#pragma unroll
    for (int hi = 0; hi < 2; ++hi)
#pragma unroll
        for (int a = 0; a < 2; ++a)
#pragma unroll
            for (int b2 = 0; b2 < 2; ++b2) sxl[((hi * 2 + a) * 2 + b2) * 512] = sx[hi][a][b2];
    SB0
    const int cb = 7 * chalf + (w < 7 ? w : 6);
    v4u uf[2][16];
    unsigned uo[2], vo[2];
#pragma unroll
    for (int t = 0; t < 2; ++t) {
        const int c = 32 * cb + perm32(t, rho); const int j = c >> 3, c8 = c & 7;
        uo[t] = sgb + (unsigned)(64 + j) * 16384u + (c8 + 16 * g) * 16;
        vo[t] = (unsigned)(VP_OFF + (size_t)(seg * 56 + j) * 2048) + g * 128 + c8 * 16;
#pragma unroll
        for (int h = 0; h < 16; ++h) uf[t][h] = ld_sc1(rs, uo[t] + (h >> 1) * 1024 + (h & 1) * 128, 0);
    }
    if (seg < 61) { if (tid < 64) wait_flags(rs, C_REC, seg + 1, 8); }
    else if (tid < 128) { wait_flags(rs, C_REC, 59, 8); wait_flags(rs, C_DN2 + 4u * (unsigned)min(tid, 111), 1, 1); }
    __syncthreads();
    f4 sm[2] = {f4{0,0,0,0}, f4{0,0,0,0}};
    v4u vf[2][4];
    {
        const unsigned gmb = (unsigned)(GM_OFF + (size_t)(seg * 16 + 2 * w + (rho >> 3)) * 7168) + g * 128 + (rho & 7) * 16;
        v4u gm[14];
#pragma unroll
        for (int ks = 0; ks < 14; ++ks) gm[ks] = ld_sc1(rs, gmb + ks * 512, 0);
#pragma unroll
        for (int t = 0; t < 2; ++t)
#pragma unroll
            for (int w4 = 0; w4 < 4; ++w4) vf[t][w4] = ld_sc1(rs, vo[t] + w4 * 512, 0);
        SB0
#pragma unroll
        for (int ks = 0; ks < 14; ++ks) {
            v4u x0 = *(const v4u*)(xl + ks * 1024 + lane * 16), x1 = *(const v4u*)(xl + (14 + ks) * 1024 + lane * 16);
            sm[0] = mfma(gm[ks], x0, sm[0]); sm[1] = mfma(gm[ks], x1, sm[1]);
            SB0
        }
    }
#pragma unroll
    for (int hi = 0; hi < 2; ++hi) {
        const int h = 2 * w + hi;
        const bool own = ((g >> 1) == hi);
#pragma unroll
        for (int tp = 0; tp < 2; ++tp) {
            const int tq = perm32(tp, rho);
            float v[8];
#pragma unroll
            for (int tau = 0; tau < 2; ++tau) {
                const f4 sv = sxl[((hi * 2 + tau) * 2 + tp) * 512];
#pragma unroll
                for (int r = 0; r < 4; ++r) { const int key = 16 * tau + 4 * g + r; v[4 * tau + r] = (key <= tq) ? sv[r] : -3.0e38f; }
            }
            float mx = fmaxf(fmaxf(fmaxf(v[0], v[1]), fmaxf(v[2], v[3])), fmaxf(fmaxf(v[4], v[5]), fmaxf(v[6], v[7])));
            const float mo = fmaxf(fmaxf(sm[tp][0], sm[tp][1]), fmaxf(sm[tp][2], sm[tp][3]));
            mx = own ? fmaxf(mx, mo) : mx;
            mx = xmax32(xmax16(mx));
            float sum = 0.f, em[4];
#pragma unroll
            for (int k = 0; k < 8; ++k) { v[k] = __builtin_amdgcn_exp2f(v[k] - mx); sum += v[k]; }
#pragma unroll
            for (int r = 0; r < 4; ++r) { em[r] = own ? __builtin_amdgcn_exp2f(sm[tp][r] - mx) : 0.f; sum += em[r]; }
            sum = xsum32(xsum16(sum));
            const float rz = __builtin_amdgcn_rcpf(sum);
            v2u p0; p0.x = pk2(v[0] * rz, v[1] * rz); p0.y = pk2(v[2] * rz, v[3] * rz);
            v2u p1; p1.x = pk2(v[4] * rz, v[5] * rz); p1.y = pk2(v[6] * rz, v[7] * rz);
            *(v2u*)(lds + (h * 2 + tp) * 1024 + (rho + 16 * (g >> 1)) * 16 + (g & 1) * 8) = p0;
            *(v2u*)(lds + (h * 2 + tp) * 1024 + (rho + 16 * (2 + (g >> 1))) * 16 + (g & 1) * 8) = p1;
            if (own) {
                v2u pm; pm.x = pk2(em[0] * rz, em[1] * rz); pm.y = pk2(em[2] * rz, em[3] * rz);
                *(v2u*)(lds + ((16 + (h >> 2)) * 2 + tp) * 1024 + (rho + 16 * (h & 3)) * 16 + (g & 1) * 8) = pm;
            }
        }
    }
    __syncthreads();
    if (w < 7) {
        f4 acc[2][2] = {{f4{0,0,0,0}, f4{0,0,0,0}}, {f4{0,0,0,0}, f4{0,0,0,0}}};
#pragma unroll
        for (int h = 0; h < 16; ++h) {
            v4u p0 = *(const v4u*)(lds + (h * 2) * 1024 + lane * 16), p1 = *(const v4u*)(lds + (h * 2 + 1) * 1024 + lane * 16);
#pragma unroll
            for (int t = 0; t < 2; ++t) { acc[t][0] = mfma(uf[t][h], p0, acc[t][0]); acc[t][1] = mfma(uf[t][h], p1, acc[t][1]); }
        }
#pragma unroll
        for (int w4 = 0; w4 < 4; ++w4) {
            v4u p0 = *(const v4u*)(lds + ((16 + w4) * 2) * 1024 + lane * 16), p1 = *(const v4u*)(lds + ((16 + w4) * 2 + 1) * 1024 + lane * 16);
#pragma unroll
            for (int t = 0; t < 2; ++t) { acc[t][0] = mfma(vf[t][w4], p0, acc[t][0]); acc[t][1] = mfma(vf[t][w4], p1, acc[t][1]); }
        }
#pragma unroll
        for (int tp = 0; tp < 2; ++tp) {
            const int tok = seg * 32 + perm32(tp, rho);
            *(v4u*)(out + (size_t)tok * OUTW + 448 + 32 * cb + 8 * g) = pack8(acc[0][tp], acc[1][tp]);
        }
    }
}

constexpr int LDS_MAIN = 135168 + 64;
__global__ void __launch_bounds__(512, 2) k_main(unsigned char* ws, u16* __restrict__ out, const u16* __restrict__ ax, const u16* __restrict__ wqkv, const u16* __restrict__ wphi, const u16* __restrict__ awo, int dS, int dV, int dVm, int pad_) {
    extern __shared__ __attribute__((aligned(16))) unsigned char lds[];
    const int bidx = blockIdx.x;
    const int idx = (bidx >> 3) * 4 + (bidx & 3);
#define PT(i)
    if ((bidx & 7) < 4) { rec_role(ws, lds, idx, dS, dV, dVm); return; }
    __amdgpu_buffer_rsrc_t rs = mkrs(ws, 0x7fffffff);
    if (idx < 112) gu_role(ws, lds, idx);
    else {
        const int u = idx - 112;
        a0_prep(ws, u, wqkv, wphi, awo);
        signal_add(ws, C_A0);
        wg_wait(rs, C_A0, 16, 2);
        a1_unit(ws, lds, u, ax);
        signal_add(ws, C_A1);
        wg_wait(rs, C_A1, 16, 4);
        a15_unit(ws, lds, u);
        signal_add(ws, C_KV);
    }
    PT(0)
    for (;;) {
        __syncthreads();
        if (threadIdx.x == 0) LDSV(LDS_MAIN - 16) = __hip_atomic_fetch_add((unsigned*)(ws + CTRL_OFF + C_TICKET), 1u, __ATOMIC_RELAXED, __HIP_MEMORY_SCOPE_AGENT);
        __syncthreads();
        const int t = __builtin_amdgcn_readfirstlane(LDSV(LDS_MAIN - 16));
        if (t >= 160) break;
        if (t < 32) a2_unit(ws, lds, t, out, ax);
        else p2_unit(ws, lds, t - 32, out);
        if (t < 32) { PT(1) } else { PT(2) }
    }
}

#define REC_DS (81 | (50 << 8))
#define REC_DV (78 | (1 << 8))
#define REC_DVM (4 | (12 << 8) | (2 << 16))
constexpr int g_dS = REC_DS, g_dV = REC_DV, g_dVm = REC_DVM;

extern "C" void kernel_launch(void* const* d_in, const int* in_sizes, int n_in, void* d_out, int out_size, void* d_ws, size_t ws_size, hipStream_t stream) {
    const u16* A_x = (const u16*)d_in[0]; const u16* A_wqkv = (const u16*)d_in[1]; const u16* A_wphi = (const u16*)d_in[2]; const u16* A_wo = (const u16*)d_in[3];
    const u16* B_x = (const u16*)d_in[4]; const u16* B_mem0 = (const u16*)d_in[5];
    const u16* Wq = (const u16*)d_in[6]; const u16* Wk = (const u16*)d_in[7]; const u16* Wv = (const u16*)d_in[8]; const u16* Wo = (const u16*)d_in[9];
    u16* out = (u16*)d_out;
    unsigned char* ws = (unsigned char*)(((uintptr_t)d_ws + 2 * MB - 1) & ~(uintptr_t)(2 * MB - 1));
    (void)hipFuncSetAttribute((const void*)k_pre, hipFuncAttributeMaxDynamicSharedMemorySize, 61440);
    (void)hipFuncSetAttribute((const void*)k_main, hipFuncAttributeMaxDynamicSharedMemorySize, LDS_MAIN);
    k_pre<<<256, 448, 61440, stream>>>(ws, Wq, Wk, Wv, Wo, B_x, B_mem0);
    k_main<<<256, 512, LDS_MAIN, stream>>>(ws, out, A_x, A_wqkv, A_wphi, A_wo, g_dS, g_dV, g_dVm, 0);
}
```

```cpp
#include <hip/hip_runtime.h>
#include <hip/hip_bf16.h>
#include <stdint.h>

typedef unsigned short u16;
typedef unsigned v4u __attribute__((ext_vector_type(4)));
typedef unsigned v2u __attribute__((ext_vector_type(2)));
typedef float f4 __attribute__((ext_vector_type(4)));
typedef float f2 __attribute__((ext_vector_type(2)));
typedef short s8 __attribute__((ext_vector_type(8)));
typedef __bf16 bf2 __attribute__((ext_vector_type(2)));
#define DI __device__ __forceinline__

constexpr int D = 448, H = 16, DH = 256, MEM = 8, SEG = 32, NSEG = 64, S_TOK = 2048;
constexpr int NKS = 14;
constexpr int OUTW = 896;
constexpr unsigned SENT = 0xFFFFFFFFu;
constexpr float LOG2E = 1.4426950408889634f;

constexpr size_t MB = 1024 * 1024;
constexpr int KCOPY = 4;
#define FARX 8192u
constexpr size_t COMM_OFF = 0;
constexpr size_t COMM_BYTES = 2 * MB;
constexpr size_t CTRL_OFF = COMM_BYTES;
constexpr size_t AS_OFF = CTRL_OFF + 262144;
constexpr size_t AS_BYTES = (size_t)16 * 14 * 2 * 14 * 1024;
constexpr size_t BS_OFF = AS_OFF + AS_BYTES;
constexpr size_t BS_BYTES = (size_t)56 * 8 * 14 * 1024;
constexpr size_t XF_OFF = BS_OFF + BS_BYTES;
constexpr size_t XF_BYTES = (size_t)64 * 28 * 1024;
constexpr size_t GU_BASE = 40 * MB;
constexpr size_t GM_OFF = XF_OFF + XF_BYTES;
constexpr size_t GM_BYTES = (size_t)64 * 16 * 14 * 512;
constexpr size_t VP_OFF = GM_OFF + GM_BYTES;
constexpr size_t VP_BYTES = (size_t)64 * 56 * 4 * 512;
constexpr size_t AXF_OFF = VP_OFF + VP_BYTES;
constexpr size_t WQF_OFF = AXF_OFF + XF_BYTES;
constexpr size_t WQF_ONE = (size_t)16 * 2 * 14 * 1024;
constexpr size_t WOF_OFF = WQF_OFF + 3 * WQF_ONE;
constexpr size_t KVP_OFF = WOF_OFF + WQF_ONE;
constexpr size_t KVF_OFF = KVP_OFF + (size_t)16 * 16 * 8192;
constexpr size_t WS_END = KVF_OFF + 65536;
static_assert(WS_END + MB <= GU_BASE && GU_BASE % (4 * MB) == 0 && GU_BASE + 128 * MB + 2 * MB <= 256 * MB, "workspace layout");
constexpr unsigned C_POL = 0, C_REC = 128, C_DONE = 256, C_A1 = 384, C_KV = 512, C_TICKET = 640, C_A0 = 768, C_GU = 1024;
constexpr unsigned C_DN2 = 16384;
constexpr unsigned C_DBG = 32768;

DI unsigned pk2(float a, float b) { f2 v = {a, b}; return __builtin_bit_cast(unsigned, __builtin_convertvector(v, bf2)); }
DI float asf(unsigned x) { return __builtin_bit_cast(float, x); }
DI unsigned asu(float x) { return __builtin_bit_cast(unsigned, x); }
DI float bflo(unsigned x) { return __builtin_bit_cast(float, x << 16); }
DI float bfhi(unsigned x) { return __builtin_bit_cast(float, x & 0xffff0000u); }
DI float bf2f(u16 x) { return __builtin_bit_cast(float, (unsigned)x << 16); }
DI f4 mfma(v4u a, v4u b, f4 c) { return __builtin_amdgcn_mfma_f32_16x16x32_bf16(__builtin_bit_cast(s8, a), __builtin_bit_cast(s8, b), c, 0, 0, 0); }
DI v4u pack8(f4 a, f4 b) { v4u r; r.x = pk2(a[0], a[1]); r.y = pk2(a[2], a[3]); r.z = pk2(b[0], b[1]); r.w = pk2(b[2], b[3]); return r; }
DI v4u nosent(v4u v) { v.x = min(v.x, 0xFFFFFFFEu); v.y = min(v.y, 0xFFFFFFFEu); v.z = min(v.z, 0xFFFFFFFEu); v.w = min(v.w, 0xFFFFFFFEu); return v; }
DI int perm32(int t, int rho) { return 8 * (rho >> 2) + 4 * t + (rho & 3); }
DI __amdgpu_buffer_rsrc_t mkrs(void* p, unsigned bytes) { return __builtin_amdgcn_make_buffer_rsrc(p, 0, bytes, 0x00020000); }
DI v4u ld_sc1(__amdgpu_buffer_rsrc_t rs, unsigned voff, unsigned soff) { return __builtin_amdgcn_raw_buffer_load_b128(rs, voff, soff, 16); }
DI v2u ld2_sc1(__amdgpu_buffer_rsrc_t rs, unsigned voff, unsigned soff) { return __builtin_amdgcn_raw_buffer_load_b64(rs, voff, soff, 16); }
DI void st_sc1(v4u v, __amdgpu_buffer_rsrc_t rs, unsigned voff, unsigned soff) { __builtin_amdgcn_raw_buffer_store_b128(v, rs, voff, soff, 16); }
DI void st2_sc1(v2u v, __amdgpu_buffer_rsrc_t rs, unsigned voff, unsigned soff) { __builtin_amdgcn_raw_buffer_store_b64(v, rs, voff, soff, 16); }
DI unsigned long long mtime() { unsigned long long t; asm volatile("s_memtime %0\n\ts_waitcnt lgkmcnt(0)" : "=s"(t) :: "memory"); return t; }

typedef short s4t __attribute__((ext_vector_type(4)));
DI v2u trread(const unsigned char* p) { return __builtin_bit_cast(v2u, __builtin_amdgcn_ds_read_tr16_b64_v4i16((__attribute__((address_space(3))) s4t*)p)); }
DI unsigned comm_step_base(int step, int copy) { return __builtin_amdgcn_readfirstlane((unsigned)(step & 7) * 262144u + (unsigned)copy * 65536u); }
DI unsigned segb(int sg, unsigned polo) { return __builtin_amdgcn_readfirstlane((unsigned)GU_BASE + (unsigned)sg * 2097152u + ((polo ^ FARX ^ ((unsigned)(sg & 1) << 13)) & 8192u)); }
DI unsigned comm_off(int chunk0, unsigned o, unsigned pol) { return (unsigned)(chunk0 + (o >> 13)) * 16384u + pol * 8192u + (o & 8191u); }

__global__ void __launch_bounds__(448) k_pre(unsigned char* ws, const u16* __restrict__ Wq, const u16* __restrict__ Wk, const u16* __restrict__ Wv, const u16* __restrict__ Wo,
                                             const u16* __restrict__ bx, const u16* __restrict__ mem0) {
    extern __shared__ __attribute__((aligned(16))) unsigned char lds[];
    const int tid = threadIdx.x, lane = tid & 63, w = __builtin_amdgcn_readfirstlane(tid >> 6);
    const int wg = blockIdx.x;
#define PP(i)
    const int h = (wg & 7) * 2 + ((wg >> 3) & 1), rest = wg >> 4;
    const int mat = rest >> 3, rh = (rest >> 2) & 1, q = rest & 3;
    const int rho = lane & 15, g = lane >> 4;
    const int rb = 7 * rh + w;
    __amdgpu_buffer_rsrc_t rs = mkrs(ws, 0x7fffffff);
    const int gt = wg * 448 + tid; constexpr int GN = 256 * 448;
    constexpr int WST = 240;
    v4u bt[8];
    if (mat == 0) {
        const int ct = 7 * q + w; const int d = 32 * (ct >> 1) + perm32(ct & 1, rho);
#pragma unroll
        for (int es = 0; es < 8; ++es) bt[es] = *(const v4u*)(Wq + ((size_t)d * H + h) * DH + es * 32 + g * 8);
    } else {
#pragma unroll
        for (int it = 0; it < 8; ++it) {
            const int idx = it * 448 + tid; const int e = idx / 14, c8 = idx % 14;
            bt[it] = *(const v4u*)(Wo + ((size_t)h * DH + e) * D + 112 * q + c8 * 8);
        }
    }
    const u16* Wr = mat ? Wv : Wk;
    v4u a[2][8];
#pragma unroll
    for (int t = 0; t < 2; ++t) {
        const int row = 32 * rb + perm32(t, rho);
#pragma unroll
        for (int es = 0; es < 8; ++es) a[t][es] = *(const v4u*)(Wr + ((size_t)row * H + h) * DH + es * 32 + g * 8);
    }
    v4u xfv;
    {
        const int f = gt >> 6; const int ks = f % 14; const int st = f / 14; const int tau = st & 1, seg = st >> 1;
        xfv = *(const v4u*)(bx + (size_t)(seg * 32 + perm32(tau, rho)) * D + ks * 32 + g * 8);
    }
    v4u m0v = {0, 0, 0, 0};
    if (wg < 8) m0v = *(const v4u*)(mem0 + (tid & 7) * D + (tid >> 3) * 8);
    __builtin_amdgcn_sched_barrier(0);
    PP(0)
    if (mat == 0) {
#pragma unroll
        for (int es = 0; es < 8; ++es) *(v4u*)(lds + (w * 8 + es) * 1024 + lane * 16) = bt[es];
    } else {
#pragma unroll
        for (int it = 0; it < 8; ++it) { const int idx = it * 448 + tid; const int e = idx / 14, c8 = idx % 14; *(v4u*)(lds + e * WST + c8 * 16) = bt[it]; }
    }
    PP(3)
    asm volatile("s_waitcnt lgkmcnt(0)" ::: "memory"); __builtin_amdgcn_s_barrier(); asm volatile("" ::: "memory");
    PP(4)
    f4 acc[2][7];
#pragma unroll
    for (int t = 0; t < 2; ++t)
#pragma unroll
        for (int c = 0; c < 7; ++c) acc[t][c] = f4{0, 0, 0, 0};
    if (mat == 0) {
#pragma unroll
        for (int es = 0; es < 8; ++es)
#pragma unroll
            for (int c = 0; c < 7; ++c) {
                v4u bfr = *(const v4u*)(lds + (c * 8 + es) * 1024 + lane * 16);
                acc[0][c] = mfma(a[0][es], bfr, acc[0][c]);
                acc[1][c] = mfma(a[1][es], bfr, acc[1][c]);
            }
        const float sc = LOG2E * 0.0625f;
#pragma unroll
        for (int c = 0; c < 7; ++c) {
            const int ct = 7 * q + c;
            st_sc1(pack8(acc[0][c] * sc, acc[1][c] * sc), rs, (unsigned)(AS_OFF + ((size_t)((h * 14 + (ct >> 1)) * 2 + (ct & 1)) * 14 + rb) * 1024) + lane * 16, 0);
        }
    } else {
        const unsigned trb = (8 * g + (rho >> 2)) * WST + (lane & 3) * 8;
#pragma unroll
        for (int es = 0; es < 8; ++es)
#pragma unroll
            for (int c = 0; c < 7; ++c) {
                v2u lo = trread(lds + trb + (32 * es) * WST + c * 32), hi = trread(lds + trb + (32 * es + 4) * WST + c * 32);
                v4u bfr = {lo.x, lo.y, hi.x, hi.y};
                acc[0][c] = mfma(a[0][es], bfr, acc[0][c]);
                acc[1][c] = mfma(a[1][es], bfr, acc[1][c]);
            }
#pragma unroll
        for (int c = 0; c < 7; ++c) {
            const int ct = 7 * q + c;
            const int j = 2 * ct + (rho >> 3);
            st_sc1(pack8(acc[0][c], acc[1][c]), rs, (unsigned)(BS_OFF + ((size_t)(j * 8 + (h >> 1)) * 14 + rb) * 1024) + (((h & 1) * 8 + (rho & 7)) + 16 * g) * 16, 0);
        }
    }
    if (wg == 1 && tid < 128) __builtin_amdgcn_raw_buffer_store_b32(0u, rs, (unsigned)CTRL_OFF + tid * 128, 0, 16);
    if (wg == 2 && tid < 128) __builtin_amdgcn_raw_buffer_store_b32(0u, rs, (unsigned)CTRL_OFF + C_DN2 + tid * 4, 0, 16);
    for (unsigned i = gt; i < COMM_BYTES / 16; i += GN) {
        unsigned off = i * 16;
        unsigned within = off & 65535u, slot = off >> 18;
        unsigned chunk = (within >> 14) & 3, o = within & 8191u;
        if (chunk == 0 && (o >= 7184 || (slot == 0 && o < 7168))) continue;
        st_sc1(v4u{SENT, SENT, SENT, SENT}, rs, off, 0);
    }
    st_sc1(xfv, rs, (unsigned)XF_OFF + gt * 16, 0);
    if (wg < 8) st_sc1(nosent(m0v), rs, (wg >> 1) * 65536u + (wg & 1) * 8192u + tid * 16, 0);
    PP(5)
}

DI unsigned umax3(unsigned a, unsigned b, unsigned c) { return max(max(a, b), c); }
DI unsigned vmax4(unsigned t, v4u v) { return umax3(umax3(t, v.x, v.y), v.z, v.w); }
DI float dpp_xor8(float v) { return asf((unsigned)__builtin_amdgcn_update_dpp(0, (int)asu(v), 0x128, 0xf, 0xf, false)); }
DI unsigned dpp_xor8u(unsigned v) { return (unsigned)__builtin_amdgcn_update_dpp(0, (int)v, 0x128, 0xf, 0xf, false); }
constexpr unsigned SPIN_MAX = 200000;
DI float dpp_x1(float v) { return asf((unsigned)__builtin_amdgcn_update_dpp(0, (int)asu(v), 0xB1, 0xf, 0xf, false)); }
DI float dpp_x2(float v) { return asf((unsigned)__builtin_amdgcn_update_dpp(0, (int)asu(v), 0x4E, 0xf, 0xf, false)); }
DI f4 quadsum(f4 v) {
#pragma unroll
    for (int r = 0; r < 4; ++r) v[r] += dpp_x1(v[r]);
#pragma unroll
    for (int r = 0; r < 4; ++r) v[r] += dpp_x2(v[r]);
    return v;
}
DI float xmax16(float x) { auto r = __builtin_amdgcn_permlane16_swap(asu(x), asu(x), false, false); unsigned a = r[0], b = r[1]; return fmaxf(asf(a), asf(b)); }
DI float xmax32(float x) { auto r = __builtin_amdgcn_permlane32_swap(asu(x), asu(x), false, false); unsigned a = r[0], b = r[1]; return fmaxf(asf(a), asf(b)); }
DI float xsum16(float x) { auto r = __builtin_amdgcn_permlane16_swap(asu(x), asu(x), false, false); unsigned a = r[0], b = r[1]; return asf(a) + asf(b); }
DI float xsum32(float x) { auto r = __builtin_amdgcn_permlane32_swap(asu(x), asu(x), false, false); unsigned a = r[0], b = r[1]; return asf(a) + asf(b); }
#define LOADM(mf) v4u mf[14]; _Pragma("unroll") for (int ks = 0; ks < 14; ++ks) mf[ks] = *(const v4u*)(ldsm + ks * 512 + mfrag_off); __builtin_amdgcn_sched_barrier(0);
DI void bar() { asm volatile("s_waitcnt lgkmcnt(0)" ::: "memory"); __builtin_amdgcn_s_barrier(); asm volatile("" ::: "memory"); }
DI unsigned now32() { return (unsigned)mtime(); }
DI void wait_until(unsigned target) {
    int rem = (int)(target - now32());
    while (rem > 80) { __builtin_amdgcn_s_sleep(1); rem -= 64; }
    while (rem > 0) { asm volatile("s_nop 7" ::: "memory"); rem -= 16; }
}

DI unsigned rt32() { unsigned long long t; asm volatile("s_memrealtime %0\n\ts_waitcnt lgkmcnt(0)" : "=s"(t) :: "memory"); return (unsigned)t; }
DI unsigned approach_rt(unsigned target, int lead) {
    int rem;
    for (;;) {
        rem = (int)(target - rt32());
        if (rem <= lead + 4) break;
        int n = (rem - lead) >> 2;
        while (n-- > 0) __builtin_amdgcn_s_sleep(1);
    }
    return now32() + (unsigned)(rem * 23 - 110);
}
DI void clock_ref(unsigned& re, unsigned& me) {
    unsigned long long m0, m1, r[12];
    asm volatile("s_memtime %0\n\t"
        "s_memrealtime %1\n\ts_nop 1\n\ts_memrealtime %2\n\ts_nop 1\n\ts_memrealtime %3\n\ts_nop 1\n\ts_memrealtime %4\n\ts_nop 1\n\t"
        "s_memrealtime %5\n\ts_nop 1\n\ts_memrealtime %6\n\ts_nop 1\n\ts_memrealtime %7\n\ts_nop 1\n\ts_memrealtime %8\n\ts_nop 1\n\t"
        "s_memrealtime %9\n\ts_nop 1\n\ts_memrealtime %10\n\ts_nop 1\n\ts_memrealtime %11\n\ts_nop 1\n\ts_memrealtime %12\n\ts_nop 1\n\t"
        "s_memtime %13\n\ts_waitcnt lgkmcnt(0)"
        : "=&s"(m0), "=&s"(r[0]), "=&s"(r[1]), "=&s"(r[2]), "=&s"(r[3]), "=&s"(r[4]), "=&s"(r[5]), "=&s"(r[6]), "=&s"(r[7]), "=&s"(r[8]), "=&s"(r[9]), "=&s"(r[10]), "=&s"(r[11]), "=&s"(m1) :: "memory");
    unsigned cnt = 0;
#pragma unroll
    for (int i = 1; i < 12; ++i) cnt += ((unsigned)r[i] == (unsigned)r[0]) ? 1u : 0u;
    re = (unsigned)r[0] + 4u;
    me = (unsigned)m0 + ((((unsigned)m1 - (unsigned)m0) * (2u * cnt + 1u) * 43u) >> 10);
}
DI void sleep_to(unsigned tc) {
    for (;;) {
        const int rem = (int)(tc - now32());
        if (rem <= 0) return;
        if (rem > 260) { int n = (rem - 160) / 96; while (n-- > 0) __builtin_amdgcn_s_sleep(1); }
    }
}
DI void spin_to(unsigned tc) { while ((int)(tc - now32()) > 0) {} }
DI void wait_rt(unsigned target) { spin_to(approach_rt(target, 10)); }
template <int NP, bool CTRL>
DI unsigned gather_m(__amdgpu_buffer_rsrc_t rs, unsigned sb, unsigned char* ldsm, int p0, int lane, int skipline = -1) {
    const unsigned o0 = p0 * 1024 + lane * 16;
    for (unsigned spin = 0;; ++spin) {
        asm volatile("" ::: "memory");
        v4u a[NP]; v2u c = {0, 0};
        bool sk[NP];
#pragma unroll
        for (int k = 0; k < NP; ++k) { sk[k] = (int)((o0 + k * 1024) >> 7) == skipline; a[k] = ld_sc1(rs, (o0 + k * 1024) ^ (sk[k] ? 128u : 0u), sb); }
        if (CTRL) c = ld2_sc1(rs, 7168u, sb);
#pragma unroll
        for (int k = 0; k < NP; ++k) *(v4u*)(ldsm + (sk[k] ? 7424u + (lane & 7) * 16 : o0 + k * 1024)) = a[k];
        if (CTRL && lane == 0) *(v2u*)(ldsm + 7168) = c;
        unsigned t;
        if (NP == 2) t = max(vmax4(0, a[0]), vmax4(0, a[1])); else t = vmax4(0, a[0]);
        if (CTRL) t = umax3(t, c.x, c.y);
        if (__all(t != SENT) || spin > SPIN_MAX) return spin;
        __builtin_amdgcn_s_sleep(1);
    }
}
template <int NP, bool CTRL>
DI unsigned gather_fast(__amdgpu_buffer_rsrc_t rs, unsigned sb, unsigned char* ldsm, const unsigned (&vo)[2], const unsigned (&lo)[2], int p0, int lane, int skipline) {
    asm volatile("; gather %0 %1" :: "i"(NP), "i"((int)CTRL) : "memory");
    v4u a0 = ld_sc1(rs, vo[0], sb), a1; v2u c;
    if (NP == 2) a1 = ld_sc1(rs, vo[1], sb);
    if (CTRL) c = ld2_sc1(rs, 7168u, sb);
    __builtin_amdgcn_sched_barrier(0);
    *(v4u*)(ldsm + lo[0]) = a0;
    unsigned t = vmax4(0, a0);
    if (NP == 2) { *(v4u*)(ldsm + lo[1]) = a1; t = vmax4(t, a1); }
    if (CTRL) { *(v2u*)(ldsm + lo[1]) = c; t = umax3(t, c.x, c.y); }
    if (__builtin_expect(__all(t != SENT), 1)) return 0;
    gather_m<NP, CTRL>(rs, sb, ldsm, p0, lane, skipline);
    return 1;
}
#define GADDR(vo, lo, p0, np, skl) { _Pragma("unroll") for (int k_ = 0; k_ < 2; ++k_) { const unsigned o_ = ((p0) + k_) * 1024 + lane * 16; const bool sk_ = (int)(o_ >> 7) == (skl); \
        vo[k_] = o_ ^ (sk_ ? 128u : 0u); lo[k_] = sk_ ? 7424u + (lane & 7) * 16 : o_; } if ((np) == 1) lo[1] = lane == 0 ? 7168u : 7552u + (lane & 7) * 8; }
#define LOADP(mf) v4u mf[7]; _Pragma("unroll") for (int q_ = 0; q_ < 7; ++q_) mf[q_] = *(const v4u*)(ldsm + q_ * 1024 + pfrag_off); __builtin_amdgcn_sched_barrier(0);
DI unsigned reduce8(const float* rd, int lane) {
    const float* q = rd + (lane >> 3) * 100 + (lane & 7) * 12;
    f4 a = *(const f4*)q, b = *(const f4*)(q + 4);
    a += b;
    float v = (a[0] + a[1]) + (a[2] + a[3]);
    float o = dpp_x1(v);
    unsigned r = (lane & 1) ? pk2(o, v) : pk2(v, o);
    return min(r, 0xFFFFFFFEu);
}
#define READCTRL() { SAVER1 v2u c_ = *(const v2u*)(ldsm + 7168); R2 = __builtin_amdgcn_readfirstlane(c_.x); R1n = __builtin_amdgcn_readfirstlane(c_.y); }

#define SAVER1
typedef short s4 __attribute__((ext_vector_type(4)));
DI f4 mfma16(v2u a, v2u b, f4 c) { return __builtin_amdgcn_mfma_f32_16x16x16bf16_1k(__builtin_bit_cast(s4, a), __builtin_bit_cast(s4, b), c, 0, 0, 0); }
DI float vmax(float a, float b) { float r; asm("s_nop 1\n\tv_max_f32 %0, %1, %2" : "=v"(r) : "v"(a), "v"(b)); return r; }
DI float vmax3(float a, float b, float c) { float r; asm("v_max3_f32 %0, %1, %2, %3" : "=v"(r) : "v"(a), "v"(b), "v"(c)); return r; }
DI float vmax3_mfma(float a, float b, float c) { float r; asm("s_nop 3\n\tv_max3_f32 %0, %1, %2, %3" : "=v"(r) : "v"(a), "v"(b), "v"(c)); return r; }
DI float rmax16(float x) { auto r = __builtin_amdgcn_permlane16_swap(asu(x), asu(x), false, false); unsigned a = r[0], b = r[1]; return vmax(asf(a), asf(b)); }
DI float rmax32(float x) { auto r = __builtin_amdgcn_permlane32_swap(asu(x), asu(x), false, false); unsigned a = r[0], b = r[1]; return vmax(asf(a), asf(b)); }
constexpr int HS = 1408;
constexpr int ST_OFF = 16 * HS;
constexpr int ZL_OFF = ST_OFF + 1024;
constexpr int L_M = 0, L_RED = 8192, L_RED2 = 12800, L_GM = 16128, L_VP = 17152, L_FL = 19200, L_UF = 20480;

DI unsigned gather_ctrl(__amdgpu_buffer_rsrc_t rs, unsigned sb, unsigned char* ldsm, int lane) {
    for (unsigned spin = 0;; ++spin) {
        asm volatile("" ::: "memory");
        v2u c = ld2_sc1(rs, 7168u, sb);
        if (lane == 0) *(v2u*)(ldsm + 7168) = c;
        if (__all(max(c.x, c.y) != SENT) || spin > SPIN_MAX) return spin;
        __builtin_amdgcn_s_sleep(1);
    }
}
#define REREF 0
#define RSLEEP 0
#define XCK 100
#define FIRSTN 2
#define ASLEEP 2
#define HSLEEP 2
#define SB0 __builtin_amdgcn_sched_barrier(0);

DI void glds16(const unsigned char* gsrc, unsigned lds_dst) {
    unsigned keep;
    asm volatile("s_mov_b32 %0, m0\n\ts_mov_b32 m0, %2\n\ts_nop 0\n\tglobal_load_lds_dwordx4 %1, off sc1\n\ts_mov_b32 m0, %0" : "=&s"(keep) : "v"(gsrc), "s"(lds_dst) : "memory");
}
DI void glds16x4(const unsigned char* gsrc, unsigned lds_dst) {
    unsigned keep;
    asm volatile("s_mov_b32 %0, m0\n\ts_mov_b32 m0, %2\n\ts_nop 0\n\t"
                 "global_load_lds_dwordx4 %1, off sc1\n\tglobal_load_lds_dwordx4 %1, off offset:1024 sc1\n\t"
                 "global_load_lds_dwordx4 %1, off offset:2048 sc1\n\tglobal_load_lds_dwordx4 %1, off offset:3072 sc1\n\t"
                 "s_mov_b32 m0, %0" : "=&s"(keep) : "v"(gsrc), "s"(lds_dst) : "memory");
}
DI void glds4(const unsigned char* gsrc, unsigned lds_dst) {
    unsigned keep;
    asm volatile("s_mov_b32 %0, m0\n\ts_mov_b32 m0, %2\n\ts_nop 0\n\tglobal_load_lds_dword %1, off sc1\n\ts_mov_b32 m0, %0" : "=&s"(keep) : "v"(gsrc), "s"(lds_dst) : "memory");
}
DI unsigned ldflag(__amdgpu_buffer_rsrc_t rs, unsigned off) { return __builtin_amdgcn_raw_buffer_load_b32(rs, (unsigned)CTRL_OFF + off, 0, 16); }
DI unsigned wait_flags(__amdgpu_buffer_rsrc_t rs, unsigned off, unsigned need, int slp) {
    unsigned v;
    for (unsigned spin = 0;; ++spin) {
        asm volatile("" ::: "memory");
        v = ldflag(rs, off);
        if (__all(v >= need) || spin > 4 * SPIN_MAX) break;
        for (int i = 0; i < slp; ++i) __builtin_amdgcn_s_sleep(4);
    }
#pragma unroll
    for (int o = 32; o >= 1; o >>= 1) v = min(v, (unsigned)__shfl_xor((int)v, o));
    return __builtin_amdgcn_readfirstlane(v);
}

#define LDSV(off) (*(volatile __attribute__((address_space(3))) unsigned*)((__attribute__((address_space(3))) unsigned char*)lds + (off)))
DI void rec_role(unsigned char* ws, unsigned char* lds, const int role, int dS, int dV, int dVm) {
    const int tid = threadIdx.x, lane = tid & 63, w = __builtin_amdgcn_readfirstlane(tid >> 6);
    const int rho = lane & 15, g = lane >> 4;
    __amdgpu_buffer_rsrc_t rs = mkrs(ws, 0x7fffffff);
    unsigned char* ldsm = lds + L_M;
    float* red = (float*)(lds + L_RED);
    float* red2 = (float*)(lds + L_RED2);
    if (role == 112) {
        unsigned best = 0xffffffffu;
        for (int r = 0; r < 10; ++r) {
            const v4u z = {SENT, SENT, SENT, SENT};
            const unsigned off = (unsigned)((w >> 1) + 4 * (r & 1)) * 262144u + ((r >> 1) & 3) * 65536u + 7680u + ((r >> 3) + 2 * (r & 1)) * 128u + (w & 1) * 8192u;
            const unsigned t0 = now32();
            if (lane < 8) st_sc1(z, rs, off + lane * 16, 0);
            asm volatile("s_waitcnt vmcnt(0)" ::: "memory");
            const unsigned dt = now32() - t0;
            if (r > 0) best = min(best, dt);
        }
        if (lane == 0) ((unsigned*)lds)[w] = best;
        __syncthreads();
        if (tid == 0) {
            const unsigned* bl = (const unsigned*)lds;
            const unsigned b0 = (bl[0] + bl[2]) + (bl[4] + bl[6]), b1 = (bl[1] + bl[3]) + (bl[5] + bl[7]);
            __builtin_amdgcn_raw_buffer_store_b32(b1 < b0 ? 2u : 1u, rs, (unsigned)CTRL_OFF + C_POL, 0, 16);
        }
        __syncthreads();
    }
#define POLINIT() const unsigned polo = (wait_flags(rs, C_POL, 1, 1) - 1u) * 8192u; const unsigned myc = (unsigned)(role & 3) * 65536u + polo;
    if (role < 112) { for (int i = 0; i < RSLEEP; ++i) __builtin_amdgcn_s_sleep(16); }
    unsigned nfail = 0, nfail2 = 0;
    unsigned long long* dbg = (unsigned long long*)(ws + CTRL_OFF + C_DBG);
#define STAMP(cond, idx)
#define TL(idx)
#define GT(idx, ref)
#define BT(i) {}
    constexpr unsigned TOFF = 70, TOFF2 = 30;
#define CLKUPD() { unsigned r1_, m1_; clock_ref(r1_, m1_); const unsigned dr = r1_ - r0, dm = m1_ - m0; \
        if (dr > 60u && dr < TA + TB + 44u) { const int err = (int)(dm * 256u) - (int)(cq * dr); cq = (unsigned)min(max((int)cq + (err >> 8), 4096), 6400); } r0 = r1_; m0 = m1_; }
#define CLKINIT() { for (int i_ = 0; i_ < 6; ++i_) __builtin_amdgcn_s_sleep(8); unsigned r1_, m1_; clock_ref(r1_, m1_); const unsigned dr = r1_ - r0, dm = m1_ - m0; \
        if (dr > 60u) cq = __builtin_amdgcn_readfirstlane(min(max((unsigned)((float)dm * 256.0f / (float)dr), 4096u), 6400u)); r0 = r1_; m0 = m1_; }
#define WAITR1() { unsigned tg_ = m0 - TOFF + (unsigned)(((int)(R1n - r0) * (int)cq) >> 8); \
        if (REREF && (int)(tg_ - now32()) > 700) { sleep_to(tg_ - 520u); unsigned ra_, ma_; clock_ref(ra_, ma_); tg_ = ma_ - TOFF + (unsigned)(((int)(R1n - ra_) * (int)cq) >> 8); } \
        sleep_to(tg_); }
    const unsigned pfrag_off = (rho >> 3) * 512 + g * 128 + (rho & 7) * 16;
    const unsigned TA = dS & 255, TB = dV & 255, TE = dVm & 255, TLATE = (dS >> 8) & 255, XUP = (dV >> 8) & 255, XDN = (dVm >> 8) & 255, X0 = (dVm >> 16) & 255;
    unsigned R2 = 0, R1n = 0;
    const bool is_mx = role >= 112;
    const bool is_v = !is_mx && (((role >> 2) & 1) == 0);
    const int j = (role >> 3) * 4 + (role & 3);
    const int h = role / 7, p = role % 7;
    const int hp = (lane >> 3) & 1, i8 = lane & 7;
    const v4u SENT4 = {SENT, SENT, SENT, SENT};

    if (is_mx) {
        const int hx = role - 112;
        POLINIT()
        if (w >= 4) {
            const unsigned orst = comm_off(1, hx * HS + 896 + (lane & 31) * 16, 0) + (lane >> 5) * 65536u + polo;
            const unsigned orst2 = comm_off(1, ST_OFF + (hx >> 1) * 128 + (hx & 1) * 64 + (lane & 1) * 16, 0) + ((lane >> 1) & 3) * 65536u + polo;
            const unsigned gfl2 = C_GU + 128u * (unsigned)min((14 * hx) / 4 + (lane & 7), (14 * hx + 13) / 4);
            unsigned kn = 0, gvo[2], glo[2];
            GADDR(gvo, glo, 2 * (w - 4), (w < 7 ? 2 : 1), -1)
            unsigned r0 = 0, m0 = 0, cq = 5900u;
            if (w == 4) {
                for (int sl = 0; sl < 8; ++sl) {
                    const unsigned sbz = comm_step_base(sl, 0);
                    st_sc1(SENT4, rs, orst, sbz); st_sc1(SENT4, rs, orst + 131072u, sbz);
                    if (lane < 8) st_sc1(SENT4, rs, orst2, sbz);
                }
                asm volatile("s_waitcnt vmcnt(0)" ::: "memory");
            }
            if (w == 7) { clock_ref(r0, m0); CLKINIT() }
            if (w == 5 && lane == 0) LDSV(L_UF) = 0u;
            for (int step = 0; step < NSEG; ++step) {
                const unsigned sbr = comm_step_base(step, 0) + myc;
                if (step > 0 && w == 7) WAITR1()
                bar();
                if (w < 7) nfail += gather_fast<2, false>(rs, sbr, ldsm, gvo, glo, 2 * (w - 4), lane, -1);
                else nfail += gather_fast<1, true>(rs, sbr, ldsm, gvo, glo, 6, lane, -1);
                bar();
                READCTRL()
                bar();
                if (w == 4 && step >= 4) {
                    __builtin_amdgcn_s_sleep(4);
                    const unsigned sbz = comm_step_base(step + 4, 0);
                    st_sc1(SENT4, rs, orst, sbz); st_sc1(SENT4, rs, orst + 131072u, sbz);
                    if (lane < 8) st_sc1(SENT4, rs, orst2, sbz);
                }
                if (w == 7) CLKUPD()
                if (w == 5 && kn < 64u) {
                    __builtin_amdgcn_s_sleep(4);
                    kn = wait_flags(rs, gfl2, 0, 1);
                    if (lane == 0) LDSV(L_UF) = kn;
                }
            }
        } else {
            __builtin_amdgcn_s_setprio(2);
            const int tau = w & 1, kh = w >> 1;
            v4u wa[8], wb[8];
            const unsigned gbase = (unsigned)((hx * 2 + tau) * 2 + kh) * 16384u + lane * 16;
            const unsigned g67 = gbase + (kh ? 4096u : 6144u);
#define GLOAD(wt, sb_) { _Pragma("unroll") for (int ks = 0; ks < 6; ++ks) wt[ks] = ld_sc1(rs, gbase + ks * 1024, sb_); \
                     wt[6] = ld_sc1(rs, g67, sb_); wt[7] = ld_sc1(rs, g67 + 1024, sb_); }
            const unsigned gfl = C_GU + 128u * (unsigned)min((14 * hx) / 4 + (lane & 7), (14 * hx + 13) / 4);
            unsigned known = wait_flags(rs, gfl, 2, 1);
            GLOAD(wa, segb(0, polo))
            GLOAD(wb, segb(1, polo))
            known = wait_flags(rs, gfl, FIRSTN, 1);
            float* sc = red;
            const int tq = lane & 7, ir = lane >> 3;
            const unsigned oes = comm_off(1, hx * HS + 896 + lane * 8, 0) + w * 65536u + polo;
            const unsigned oss = comm_off(1, ST_OFF + (hx >> 1) * 128 + (hx & 1) * 64 + ir * 4, 0) + w * 65536u + polo;
#define MXGT
#define MXSTEP(wt, step) { \
                const bool last = ((step) == NSEG - 1); \
                const unsigned sbs = comm_step_base(step, 0); \
                __builtin_amdgcn_s_barrier(); \
                bar(); \
                f4 aE = {0, 0, 0, 0}, aO = {0, 0, 0, 0}; \
                v4u mf[4]; \
                _Pragma("unroll") for (int q = 0; q < 3; ++q) mf[q] = *(const v4u*)(ldsm + (4 * kh + q) * 1024 + pfrag_off); \
                mf[3] = *(const v4u*)(ldsm + 3 * 1024 + pfrag_off); \
                SB0 \
                _Pragma("unroll") for (int q = 0; q < 3; ++q) { aE = mfma(wt[2 * q], mf[q], aE); aO = mfma(wt[2 * q + 1], mf[q], aO); } \
                if (kh == 0) { aE = mfma(wt[6], mf[3], aE); aO = mfma(wt[7], mf[3], aO); } \
                f4 xs = hp ? aO : aE; \
                *(f4*)(sc + ((2 * kh + hp) * 8 + i8) * 36 + 16 * tau + 4 * g) = xs; \
                bar(); \
                const float* sp = sc + ir * 36 + 4 * tq; \
                f4 a0 = (*(const f4*)(sp) + *(const f4*)(sp + 288)) + (*(const f4*)(sp + 576) + *(const f4*)(sp + 864)); \
                float mx; \
                asm("v_max3_f32 %0, %1, %2, %3\n\tv_max_f32 %0, %0, %4\n\ts_nop 1\n\t" \
                    "v_max_f32_dpp %0, %0, %0 quad_perm:[1,0,3,2] row_mask:0xf bank_mask:0xf\n\ts_nop 1\n\t" \
                    "v_max_f32_dpp %0, %0, %0 quad_perm:[2,3,0,1] row_mask:0xf bank_mask:0xf\n\ts_nop 1\n\t" \
                    "v_max_f32_dpp %0, %0, %0 row_half_mirror row_mask:0xf bank_mask:0xf" \
                    : "=&v"(mx) : "v"(a0[0]), "v"(a0[1]), "v"(a0[2]), "v"(a0[3])); \
                if (!last) { \
                    unsigned e0 = pk2(__builtin_amdgcn_exp2f(a0[0] - mx), __builtin_amdgcn_exp2f(a0[1] - mx)); \
                    unsigned e1 = pk2(__builtin_amdgcn_exp2f(a0[2] - mx), __builtin_amdgcn_exp2f(a0[3] - mx)); \
                    v2u eo; eo.x = min(e0, 0xFFFFFFFEu); eo.y = min(e1, 0xFFFFFFFEu); \
                    st2_sc1(eo, rs, oes, sbs); \
                    if (tq == 1) __builtin_amdgcn_raw_buffer_store_b32(min(asu(mx), 0xFFFFFFFEu), rs, oss, sbs, 16); \
                    MXGT \
                } \
                { const int sn = min((step) + 2, NSEG - 1); \
                  known = max(known, (unsigned)__builtin_amdgcn_readfirstlane(LDSV(L_UF))); \
                  if (known < (unsigned)sn + 1) known = wait_flags(rs, gfl, sn + 1, 1); \
                  const unsigned sb = segb(sn, polo); \
                  GLOAD(wt, sb) } }
            for (int step = 0; step < NSEG; step += 2) {
                MXSTEP(wa, step)
                MXSTEP(wb, step + 1)
            }
        }
    } else if (w >= 4) {
        const int wv = w - 4;
        v4u wtv[2][14];
        if (is_v) {
            const unsigned char* src = ws + BS_OFF + ((size_t)j * 8 + 2 * wv) * 14 * 1024;
#pragma unroll
            for (int t = 0; t < 2; ++t)
#pragma unroll
                for (int f = 0; f < 14; ++f) wtv[t][f] = *(const v4u*)(src + (t * 14 + f) * 1024 + lane * 16);
        } else {
#pragma unroll
            for (int t = 0; t < 2; ++t)
#pragma unroll
                for (int f = 0; f < 14; ++f) wtv[t][f] = v4u{0, 0, 0, 0};
        }
        POLINIT()
        __builtin_amdgcn_s_waitcnt(0x0F70);
        const bool cond = is_v && j == 0;
        const bool slow = w >= 6;
        const unsigned ufl = C_GU + 128u * (56 + j);
        const unsigned char* usrc = ws + (size_t)(64 + j) * 16384 + (wv & 1) * 4096 + lane * 16;
        const unsigned udst = (unsigned)(size_t)(__attribute__((address_space(3))) unsigned char*)lds + L_UF + (wv & 1) * 4096;
#define UDMA(sg) { const unsigned char* p_ = usrc + segb(sg, polo); const unsigned d_ = __builtin_amdgcn_readfirstlane(udst + ((sg) & 3) * 8192); \
            glds16x4(p_, d_); }
#define VPRIME() { \
                f4 e0 = {0, 0, 0, 0}, o0 = {0, 0, 0, 0}, e1 = {0, 0, 0, 0}, o1 = {0, 0, 0, 0}; \
                _Pragma("unroll") for (int q = 0; q < 7; ++q) { \
                    e0 = mfma(mf[q], wtv[0][2 * q], e0); o0 = mfma(mf[q], wtv[0][2 * q + 1], o0); \
                    e1 = mfma(mf[q], wtv[1][2 * q], e1); o1 = mfma(mf[q], wtv[1][2 * q + 1], o1); } \
                f4 cs; \
                _Pragma("unroll") for (int r = 0; r < 4; ++r) { \
                    auto sw = __builtin_amdgcn_permlane32_swap(asu(o0[r]), asu(e1[r]), false, false); \
                    unsigned d0 = sw[0], d1 = sw[1]; \
                    cs[r] = g < 2 ? e0[r] + asf(d1) : o1[r] + asf(d0); } \
                v2u vp; vp.x = pk2(cs[0], cs[1]); vp.y = pk2(cs[2], cs[3]); \
                *(v2u*)(lds + L_VP + wv * 512 + (2 * (g >> 1) + hp) * 128 + i8 * 16 + (g & 1) * 8) = vp; }
#define BYPROD(st_) { \
            if (w == 7) { v4u gmv = *(const v4u*)(lds + L_GM + lane * 16); \
                st_sc1(gmv, rs, (unsigned)(GM_OFF + ((size_t)h * 14 + 2 * p) * 512) + lane * 16, __builtin_amdgcn_readfirstlane((st_) * 16 * 14 * 512)); } \
            if (w == 6 && is_v) { v4u v0 = *(const v4u*)(lds + L_VP + lane * 16), v1 = *(const v4u*)(lds + L_VP + 1024 + lane * 16); \
                const unsigned sbv = __builtin_amdgcn_readfirstlane((st_) * 56 * 4 * 512); \
                st_sc1(v0, rs, (unsigned)(VP_OFF + (size_t)j * 2048) + lane * 16, sbv); \
                st_sc1(v1, rs, (unsigned)(VP_OFF + (size_t)j * 2048 + 1024) + lane * 16, sbv); } }
#define RESETS() if (w == 7 && step >= 4) { \
                    if (dorst) st_sc1(SENT4, rs, orst, comm_step_base(step + 4, 0)); \
                    if (cond && lane < 4) st_sc1(SENT4, rs, polo + 7168u + lane * 65536u, comm_step_base(step + 4, 0)); }
        const unsigned orst = (lane < 32 ? comm_off(1, h * HS + p * 128 + (lane & 7) * 16, 0) : (unsigned)(j * 128 + (lane & 7) * 16)) + ((lane >> 3) & 3) * 65536u + polo;
        const bool dorst = lane < 32 || is_v;
#define CSTORES() { \
                    if (cond && lane < 4) st_sc1(SENT4, rs, polo + 7168u + lane * 65536u, comm_step_base(step + 4, 0)); \
                    if (dorst) st_sc1(SENT4, rs, orst, comm_step_base(step + 4, 0)); \
                    v4u gmv = *(const v4u*)(lds + L_GM + lane * 16); \
                    st_sc1(gmv, rs, (unsigned)(GM_OFF + ((size_t)h * 14 + 2 * p) * 512) + lane * 16, __builtin_amdgcn_readfirstlane(step * 16 * 14 * 512)); }
        if (w == 7) {
            unsigned known = 0;
            for (int sl = 0; sl < 8; ++sl) {
                if (dorst && (sl > 0 || lane < 32)) st_sc1(SENT4, rs, orst, comm_step_base(sl, 0));
                if (cond && lane < 4 && sl > 0) st_sc1(SENT4, rs, polo + 7168u + lane * 65536u, comm_step_base(sl, 0));
            }
            asm volatile("s_waitcnt vmcnt(0)" ::: "memory");
            unsigned r0, m0, cq = 5900u; clock_ref(r0, m0);
            CLKINIT()
            if (is_v) { known = wait_flags(rs, ufl, 2, 1); UDMA(0) UDMA(1) asm volatile("s_waitcnt vmcnt(0)" ::: "memory"); }
            for (int step = 0; step < NSEG; ++step) {
                if (step > 0) WAITR1()
                bar();
                BT(0)
                CLKUPD()
                if (lane == 0) *(v4u*)(lds + L_FL + 512) = v4u{r0, m0 - TOFF2, cq, 0u};
                bar();
                __builtin_amdgcn_s_sleep(HSLEEP);
                READCTRL()
                LOADP(mf)
                bar();
                if (is_v) VPRIME()
                bar();
                asm volatile("s_waitcnt vmcnt(0)" ::: "memory");
                if (is_v && step + 3 < NSEG) UDMA(step + 2)
                if (is_v) bar();
                else CSTORES()
            }
            bar();
            asm volatile("s_waitcnt vmcnt(0)" ::: "memory");
            __builtin_amdgcn_s_barrier();
        } else if (w == 6) {
            unsigned known = 0;
            if (is_v) { known = wait_flags(rs, ufl, 2, 1); UDMA(0) UDMA(1) asm volatile("s_waitcnt vmcnt(0)" ::: "memory"); known = wait_flags(rs, ufl, FIRSTN, 1); }
            const bool reporter = (role == 4);
            const unsigned char* fsrc = ws + CTRL_OFF + ufl;
            const unsigned fdst = __builtin_amdgcn_readfirstlane((unsigned)(size_t)(__attribute__((address_space(3))) unsigned char*)lds + L_FL);
            if (lane == 0) LDSV(L_FL) = 0u;
#define VPSTORE(st_) { v4u v0 = *(const v4u*)(lds + L_VP + lane * 16), v1 = *(const v4u*)(lds + L_VP + 1024 + lane * 16); \
                const unsigned sbv = __builtin_amdgcn_readfirstlane((st_) * 56 * 4 * 512); \
                st_sc1(v0, rs, (unsigned)(VP_OFF + (size_t)j * 2048) + lane * 16, sbv); \
                st_sc1(v1, rs, (unsigned)(VP_OFF + (size_t)j * 2048 + 1024) + lane * 16, sbv); }
            for (int step = 0; step < NSEG; ++step) {
                bar();
                if (is_v) {
                    asm volatile("s_waitcnt vmcnt(2)" ::: "memory");
                    __builtin_amdgcn_s_sleep(ASLEEP);
                    if (step + 3 < NSEG) {
                        if (known < 64u) known = max(known, (unsigned)__builtin_amdgcn_readfirstlane(LDSV(L_FL)));
                        if (known < (unsigned)step + 3) known = wait_flags(rs, ufl, step + 3, 1);
                        UDMA(step + 2)
                    }
                    if (step > 0) VPSTORE(step - 1)
                    if (known < (unsigned)step + 10 && known < 64u && lane == 0) glds4(fsrc, fdst);
                }
                bar();
                __builtin_amdgcn_s_sleep(HSLEEP);
                LOADP(mf)
                bar();
                if (is_v) VPRIME()
                bar();
                if (reporter && lane == 0 && step >= 3) __builtin_amdgcn_raw_buffer_store_b32((unsigned)(step - 2), rs, (unsigned)CTRL_OFF + C_REC, 0, 16);
                if (is_v) { CSTORES() bar(); }
            }
            bar();
            if (is_v) VPSTORE(NSEG - 1)
            asm volatile("s_waitcnt vmcnt(0)" ::: "memory");
            __builtin_amdgcn_s_barrier();
        } else {
            unsigned extra = X0, calm = 0, ont = 0, gvo[2] = {0, 0}, glo[2] = {0, 0};
            if (cond && w == 5) {
                wait_flags(rs, ufl, FIRSTN, 1);
                unsigned t = rt32() + 120u;
                v2u c; c.x = min(t, 0xFFFFFFFEu); c.y = min(t + TB, 0xFFFFFFFEu);
                if (lane < KCOPY) st2_sc1(c, rs, polo + 7168u + lane * 65536u, 0);
                if (lane == 0) *(v2u*)(ldsm + 7168) = c;
            }
            const unsigned opub = polo + j * 128 + (lane >> 1) * 4 + (lane & 1) * 65536u;
            const unsigned opubS = comm_off(1, h * HS + p * 128 + (lane >> 1) * 4, 0) + (lane & 1) * 65536u + polo;
            for (int step = 0; step < NSEG; ++step) {
                const bool last = (step == NSEG - 1);
                const unsigned sbs = comm_step_base(step, 0);
                const unsigned sbr = sbs + myc;
                const unsigned sbn = comm_step_base(step + 1, 0);
                const int skl = (is_v && step > 0) ? j : -1;
                if (step < 2) GADDR(gvo, glo, w + 1, 1, skl)
                bar();
                if (w == 4 || cond) nfail += gather_fast<1, false>(rs, sbr, ldsm, gvo, glo, w + 1, lane, skl);
                else nfail += gather_fast<1, true>(rs, sbr, ldsm, gvo, glo, 6, lane, skl);
                bar();
                if (w == 5) BT(1)
                READCTRL()
                __builtin_amdgcn_s_sleep(HSLEEP);
                LOADP(mf)
                if (w == 4) __builtin_amdgcn_s_setprio(3);
                bar();
                if (w == 5) BT(2)
                if (w == 4) {
                    if (!last) {
                        unsigned o = reduce8(red, lane);
                        __builtin_amdgcn_raw_buffer_store_b32(o, rs, opubS, sbs, 16);
                        __builtin_amdgcn_raw_buffer_store_b32(o, rs, opubS + 131072u, sbs, 16);
                        BT(5)
                        if ((lane & 1) == 0) *(unsigned*)(lds + L_FL + 640 + (lane >> 1) * 4) = o;
                    }
                    __builtin_amdgcn_s_setprio(0);
                }
                if (is_v) {
                    VPRIME()
                    bar();
                    if (w == 5) BT(3)
                    unsigned xclk = 0;
                    if (cond && w == 5) {
                        const int cqv = (int)__builtin_amdgcn_readfirstlane(LDSV(L_FL + 520));
                        xclk = (unsigned)max((5950 - cqv) * XCK, 0) >> 13;
                    }
                    if (w == 4) __builtin_amdgcn_s_setprio(3);
                    bar();
                    if (w == 5 && !cond) BT(4)
                    if (!last) {
                        if (w == 4) {
                            unsigned o = reduce8(red2, lane);
                            __builtin_amdgcn_raw_buffer_store_b32(o, rs, opub, sbn, 16);
                            __builtin_amdgcn_raw_buffer_store_b32(o, rs, opub + 131072u, sbn, 16);
                            BT(6)
                            if ((lane & 1) == 0) *(unsigned*)(ldsm + j * 128 + (lane >> 1) * 4) = o;
                            __builtin_amdgcn_s_setprio(0);
                        }
                        if (cond && w == 5) {
                            const unsigned tp = rt32();
                            unsigned base = R1n;
                            if ((int)(tp + 4u - R1n) > 0) { base = tp + TLATE; if (ont) extra = min(extra + XUP, 10u); ont = 0; calm = 0; }
                            else { ont = 1; if (++calm >= XDN) { calm = 0; extra = extra ? extra - 1u : 0u; } }
                            const unsigned ex = extra + xclk;
                            v2u c; c.x = min(base + TA + ex, 0xFFFFFFFEu); c.y = min(base + TA + TB + 2u * ex, 0xFFFFFFFEu);
                            if (lane < KCOPY) st2_sc1(c, rs, polo + 7168u + lane * 65536u, sbn);
                            if (lane == 0) *(v2u*)(ldsm + 7168) = c;
                        }
                    }
                } else bar();
            }
            bar();
            __builtin_amdgcn_s_barrier();
            if (w == 4 && lane == 0) __builtin_amdgcn_raw_buffer_store_b32(1u, rs, (unsigned)CTRL_OFF + C_DN2 + role * 4, 0, 16);
        }
    } else {
        __builtin_amdgcn_s_setprio(2);
        const int b = 2 * p + (w >> 1), t = w & 1;
        v4u wt[14];
        {
            const unsigned char* src = ws + AS_OFF + ((size_t)(h * 14 + b) * 2 + t) * 14 * 1024;
#pragma unroll
            for (int ks = 0; ks < 14; ++ks) wt[ks] = *(const v4u*)(src + ks * 1024 + lane * 16);
        }
        POLINIT()
        __builtin_amdgcn_s_waitcnt(0x0F70);
        unsigned op[2][2], oe[2], ost, gvo[2] = {0, 0}, glo[2] = {0, 0};
        v4u pf[2][2], ef[2]; unsigned stt;
#pragma unroll
        for (int a = 0; a < 2; ++a) {
            const int hd = 4 * w + 2 * a + hp;
#pragma unroll
            for (int u = 0; u < 2; ++u) {
                const int q = 4 * u + g;
                const bool z = (q == 7) || (hd == h && q == p);
                op[a][u] = z ? 0x80000000u : comm_off(1, hd * HS + q * 128 + i8 * 16, 0);
            }
            oe[a] = comm_off(1, hd * HS + 896 + i8 * 64 + g * 16, 0);
        }
        const int hl = 4 * w + 2 * (g >> 1) + hp;
        ost = comm_off(1, ST_OFF + (hl >> 1) * 128 + (hl & 1) * 64 + i8 * 4, 0);
        const bool ownhead = (hl == h);
        v4u selLo = {0, 0, 0, 0}, selHi = {0, 0, 0, 0}, oneLo, oneHi;
        {
            const unsigned one = 0x3F80u << (16 * (rho & 1));
            const int wd = (rho & 7) >> 1;
            v4u sv; sv.x = wd == 0 ? one : 0; sv.y = wd == 1 ? one : 0; sv.z = wd == 2 ? one : 0; sv.w = wd == 3 ? one : 0;
            if (rho < 8) selLo = sv; else selHi = sv;
            const unsigned o2 = 0x3F803F80u;
            oneLo = rho < 8 ? v4u{o2, o2, o2, o2} : v4u{0, 0, 0, 0};
            oneHi = rho < 8 ? v4u{0, 0, 0, 0} : v4u{o2, o2, o2, o2};
        }
        for (int step = 0; step < NSEG; ++step) {
            const bool last = (step == NSEG - 1);
            const unsigned sbs = comm_step_base(step, 0);
            const unsigned sbr = sbs + myc;
            STAMP(step == 1, 0); STAMP(step == 63, 1);
            if (step < 2) GADDR(gvo, glo, (w == 0 ? 0 : w + 1), 2, ((is_v && step > 0) ? j : -1))
            bar();
            TL(0)
            const int skl = (is_v && step > 0) ? j : -1;
            if (w == 0) nfail += gather_fast<2, false>(rs, sbr, ldsm, gvo, glo, 0, lane, skl);
            else nfail += gather_fast<1, false>(rs, sbr, ldsm, gvo, glo, w + 1, lane, skl);
            bar();
            TL(1)
            f4 aE = {0, 0, 0, 0}, aO = {0, 0, 0, 0};
            v4u mf[7];
#pragma unroll
            for (int q_ = 0; q_ < 7; ++q_) mf[q_] = *(const v4u*)(ldsm + q_ * 1024 + pfrag_off);
            v2u mb = *(const v2u*)(ldsm + b * 512 + g * 128 + i8 * 16 + t * 8);
            const unsigned cwx = *(const unsigned*)(ldsm + 7168);
            const v4u ckr = *(const v4u*)(lds + L_FL + 512);
            SB0
#pragma unroll
            for (int q = 0; q < 7; ++q) { aE = mfma(wt[2 * q], mf[q], aE); aO = mfma(wt[2 * q + 1], mf[q], aO); }
            f4 xs = hp ? aO : aE;
            v2u gh; gh.x = pk2(xs[0], xs[1]); gh.y = pk2(xs[2], xs[3]);
            f4 sp = mfma16(gh, mb, f4{0, 0, 0, 0});
            if (rho < 8) {
                float* rq = red + rho * 100 + (4 * (g & 1)) * 12 + 2 * w + (g >> 1);
                rq[0] = sp[0]; rq[12] = sp[1]; rq[24] = sp[2]; rq[36] = sp[3];
            }
            SB0
            TL(7)
            bar();
#define GMSTAGE() { f4 fs; _Pragma("unroll") for (int r = 0; r < 4; ++r) fs[r] = xs[r] + dpp_xor8(xs[r]); \
                v2u gm; gm.x = pk2(fs[0], fs[1]); gm.y = pk2(fs[2], fs[3]); \
                if (rho < 8) *(v2u*)(lds + L_GM + (w >> 1) * 512 + g * 128 + rho * 16 + t * 8) = gm; }
            if (!is_v) { GMSTAGE() bar(); continue; }
            if (last) { GMSTAGE() bar(); bar(); continue; }
#define E_LOADS(sb_) { stt = __builtin_amdgcn_raw_buffer_load_b32(rs, ost, sb_, 16); ef[0] = ld_sc1(rs, oe[0], sb_); ef[1] = ld_sc1(rs, oe[1], sb_); SB0 }
#define P_LOADS(sb_) { pf[0][0] = ld_sc1(rs, op[0][0], sb_); SB0 pf[1][0] = ld_sc1(rs, op[1][0], sb_); SB0 pf[0][1] = ld_sc1(rs, op[0][1], sb_); SB0 pf[1][1] = ld_sc1(rs, op[1][1], sb_); SB0 }
            const unsigned tc2 = (unsigned)__builtin_amdgcn_readfirstlane(ckr.y) + (unsigned)(((int)(__builtin_amdgcn_readfirstlane(cwx) - __builtin_amdgcn_readfirstlane(ckr.x)) * (int)__builtin_amdgcn_readfirstlane(ckr.z)) >> 8);
            spin_to(tc2 - TE * 23u);
            E_LOADS(sbr)
            asm volatile("" ::: "memory");
            spin_to(tc2);
            TL(3)
            P_LOADS(sbr)
            asm volatile("" ::: "memory");
            SB0
            if (w == 1) BT(7)
            GMSTAGE()
            v4u uf[2];
            uf[0] = *(const v4u*)(lds + L_UF + (step & 3) * 8192 + w * 2048 + lane * 16); uf[1] = *(const v4u*)(lds + L_UF + (step & 3) * 8192 + w * 2048 + 1024 + lane * 16);
            bar();
            v2u vpA, vpB;
            f4 sini = {0, 0, 0, 0};
            {
                v2u vp = *(const v2u*)(lds + L_VP + w * 512 + (2 * (g >> 1) + hp) * 128 + i8 * 16 + (g & 1) * 8);
                v2u ow = *(const v2u*)(lds + L_FL + 640 + i8 * 16 + (g & 1) * 8);
                vpA = g < 2 ? vp : v2u{0, 0};
                vpB = g < 2 ? v2u{0, 0} : vp;
                if (ownhead) { sini[0] = bflo(ow.x); sini[1] = bfhi(ow.x); sini[2] = bflo(ow.y); sini[3] = bfhi(ow.y); }
            }
            asm volatile("" ::: "memory");
            SB0
            TL(4)
            bool okall;
            auto combine = [&]() __attribute__((always_inline)) {
                f4 ls = mfma(oneLo, ef[0], f4{0, 0, 0, 0});
                ls = mfma(oneHi, ef[1], ls);
                f4 x0 = mfma(uf[0], ef[0], f4{0, 0, 0, 0});
                f4 x1 = mfma(uf[1], ef[1], f4{0, 0, 0, 0});
                unsigned t2 = vmax4(vmax4(stt, ef[0]), ef[1]);
                float lsv = ls[0];
                asm volatile("" : "+v"(t2), "+v"(lsv));
                SB0
                f4 sacc = mfma(selLo, pf[0][0], sini);
                sacc = mfma(selHi, pf[1][0], sacc);
                sacc = mfma(selLo, pf[0][1], sacc);
                sacc = mfma(selHi, pf[1][1], sacc);
                SB0
                unsigned t0 = vmax4(vmax4(t2, pf[0][0]), pf[0][1]), t1 = vmax4(vmax4(0, pf[1][0]), pf[1][1]);
                asm volatile("" : "+v"(t0), "+v"(t1));
                SB0
                const float mloc = asf(stt);
                float mx = vmax3(vmax3_mfma(sacc[0], sacc[1], sacc[2]), sacc[3], mloc);
                mx = rmax16(mx);
                float p0 = __builtin_amdgcn_exp2f(sacc[0] - mx), p1 = __builtin_amdgcn_exp2f(sacc[1] - mx), p2 = __builtin_amdgcn_exp2f(sacc[2] - mx), p3 = __builtin_amdgcn_exp2f(sacc[3] - mx);
                float f = __builtin_amdgcn_exp2f(mloc - mx);
                v2u pb; pb.x = pk2(p0, p1); pb.y = pk2(p2, p3);
                float zq = (p0 + p1) + (p2 + p3);
                SB0
                f4 cmA = mfma16(vpA, pb, f4{0, 0, 0, 0});
                f4 cmB = mfma16(vpB, pb, f4{0, 0, 0, 0});
                SB0
                float z = xsum16(zq);
                auto fs = __builtin_amdgcn_permlane32_swap(asu(f), asu(f), false, false);
                unsigned fa = fs[0], fb = fs[1];
                z += f * lsv;
                float rz = __builtin_amdgcn_rcpf(z);
                okall = __all(max(t0, t1) != SENT);
                f4 ya = x0 * asf(fa) + cmA, yb = x1 * asf(fb) + cmB;
                auto rs2 = __builtin_amdgcn_permlane32_swap(asu(rz), asu(rz), false, false);
                unsigned ra = rs2[0], rb = rs2[1];
                f4 cx = ya * asf(ra) + yb * asf(rb);
                if (hp == (g >> 1)) {
                    float* rq = red2 + i8 * 100 + (4 * (g & 1)) * 12 + 2 * w + hp;
                    rq[0] = cx[0]; rq[12] = cx[1]; rq[24] = cx[2]; rq[36] = cx[3];
                }
            };
            combine();
            if (!okall) {
                nfail2++;
                for (unsigned spin = 0; spin < SPIN_MAX && !okall; ++spin) {
                    __builtin_amdgcn_s_sleep(1);
                    asm volatile("" ::: "memory");
                    E_LOADS(sbr)
                    P_LOADS(sbr)
                    combine();
                }
            }
            TL(6)
            bar();
        }
        bar();
        __builtin_amdgcn_s_barrier();
    }
}

DI void signal_add(unsigned char* ws, unsigned off) {
    asm volatile("s_waitcnt vmcnt(0)" ::: "memory");
    __syncthreads();
    if (threadIdx.x == 0) __hip_atomic_fetch_add((unsigned*)(ws + CTRL_OFF + off), 1u, __ATOMIC_RELAXED, __HIP_MEMORY_SCOPE_AGENT);
}
DI void wg_wait(__amdgpu_buffer_rsrc_t rs, unsigned off_lane, unsigned need, int slp) {
    if (threadIdx.x < 64) wait_flags(rs, off_lane, need, slp);
    __syncthreads();
}

DI void gldsx16(const unsigned char* gsrc, unsigned lds_dst) {
    unsigned keep;
    asm volatile("s_mov_b32 %0, m0\n\ts_mov_b32 m0, %2\n\ts_nop 0\n\tglobal_load_lds_dwordx4 %1, off\n\ts_mov_b32 m0, %0" : "=&s"(keep) : "v"(gsrc), "s"(lds_dst) : "memory");
}
DI void gu_role(unsigned char* ws, unsigned char* lds, const int gw) {
    const int tid = threadIdx.x, lane = tid & 63, w8 = __builtin_amdgcn_readfirstlane(tid >> 6);
    const int w = w8 & 3, sp = w8 >> 2;
    const bool isG = gw < 56;
    const unsigned ldsb = __builtin_amdgcn_readfirstlane((unsigned)(size_t)(__attribute__((address_space(3))) unsigned char*)lds);
    const int nat0 = (((lane & 15) >> 2) & 1) * 14464 + ((4 * ((lane & 15) >> 3) + (lane & 3)) + 16 * (lane >> 4)) * 16;
    const unsigned char* xsrc = ws + XF_OFF + w8 * 1024 + lane * 16;
#define XDMA(pr_) { _Pragma("unroll") for (int i = 0; i < 7; ++i) gldsx16(xsrc + (size_t)(pr_) * 57344 + i * 8192, ldsb + ((pr_) & 1) * 57856 + (i * 8 + w8) * 1024 + ((i * 8 + w8) / 14) * 128); }
    XDMA(0)
    v4u wt[2][14];
    const unsigned char* wsrc = isG ? ws + AS_OFF + (size_t)(4 * gw + w) * 28 * 1024 : ws + BS_OFF + ((size_t)(gw - 56) * 8 + 2 * w) * 14 * 1024;
    unsigned char* xch = lds + 57856;
    v4u tmp[14];
#pragma unroll
    for (int ks = 0; ks < 14; ++ks) tmp[ks] = *(const v4u*)(wsrc + (sp * 14 + ks) * 1024 + lane * 16);
    SB0
#pragma unroll
    for (int half = 0; half < 2; ++half) {
        if (half) bar();
#pragma unroll
        for (int ks = 0; ks < 7; ++ks) *(v4u*)(xch + (w8 * 7 + ks) * 1024 + lane * 16) = tmp[half * 7 + ks];
        bar();
#pragma unroll
        for (int t = 0; t < 2; ++t)
#pragma unroll
            for (int ks = 0; ks < 7; ++ks) wt[t][half * 7 + ks] = *(const v4u*)(xch + ((w + 4 * t) * 7 + ks) * 1024 + lane * 16);
    }
    __amdgpu_buffer_rsrc_t rs = mkrs(ws, 0x7fffffff);
    asm volatile("s_waitcnt vmcnt(0)" ::: "memory");
    bar();
    const int bb = 4 * gw + w, hh = bb / 14, bq = bb % 14;
    const unsigned obase = isG ? (unsigned)(hh * 4 + (bq >> 3)) * 16384u + (bq & 7) * 1024 + lane * 16 : (unsigned)(64 + gw - 56) * 16384u + 2 * w * 1024 + lane * 16;
    const unsigned ostep = isG ? 32768u : 1024u;
    unsigned polo = 0;
    for (int pr = 0; pr < 32; ++pr) {
        const unsigned char* cur = lds + (pr & 1) * 57856 + sp * 28928;
        if (pr + 1 < 32) XDMA(pr + 1)
        f4 acc[2][2] = {{f4{0,0,0,0}, f4{0,0,0,0}}, {f4{0,0,0,0}, f4{0,0,0,0}}};
        {
            const unsigned char* p0 = cur + (isG ? nat0 : lane * 16);
            const unsigned char* p1 = p0 + (isG ? 128 : 14464);
            v4u xa[3], xb[3];
#pragma unroll
            for (int k = 0; k < 3; ++k) { xa[k] = *(const v4u*)(p0 + k * 1024); xb[k] = *(const v4u*)(p1 + k * 1024); }
            if (isG) {
#pragma unroll
                for (int ks = 0; ks < 14; ++ks) {
                    const v4u x0 = xa[ks % 3], x1 = xb[ks % 3];
                    acc[0][0] = mfma(wt[0][ks], x0, acc[0][0]); acc[1][0] = mfma(wt[1][ks], x0, acc[1][0]);
                    acc[0][1] = mfma(wt[0][ks], x1, acc[0][1]); acc[1][1] = mfma(wt[1][ks], x1, acc[1][1]);
                    if (ks + 3 < 14) { xa[ks % 3] = *(const v4u*)(p0 + (ks + 3) * 1024); xb[ks % 3] = *(const v4u*)(p1 + (ks + 3) * 1024); }
                    SB0
                }
            } else {
#pragma unroll
                for (int ks = 0; ks < 14; ++ks) {
                    const v4u x0 = xa[ks % 3], x1 = xb[ks % 3];
                    acc[0][0] = mfma(x0, wt[0][ks], acc[0][0]); acc[1][0] = mfma(x1, wt[0][ks], acc[1][0]);
                    acc[0][1] = mfma(x0, wt[1][ks], acc[0][1]); acc[1][1] = mfma(x1, wt[1][ks], acc[1][1]);
                    if (ks + 3 < 14) { xa[ks % 3] = *(const v4u*)(p0 + (ks + 3) * 1024); xb[ks % 3] = *(const v4u*)(p1 + (ks + 3) * 1024); }
                    SB0
                }
            }
        }
        if (pr == 0) polo = (wait_flags(rs, C_POL, 1, 1) - 1u) * 8192u;
        const unsigned so = segb(2 * pr + sp, polo);
        st_sc1(pack8(acc[0][0], acc[1][0]), rs, obase, so);
        st_sc1(pack8(acc[0][1], acc[1][1]), rs, obase + ostep, so);
        const bool drain = (pr < 4) || (pr == 31);
        if (drain) asm volatile("s_waitcnt vmcnt(0)" ::: "memory");
        else asm volatile("s_waitcnt vmcnt(2)" ::: "memory");
        bar();
        if (tid == 448) __builtin_amdgcn_raw_buffer_store_b32((unsigned)(drain ? 2 * pr + 2 : 2 * pr), rs, (unsigned)CTRL_OFF + C_GU + 128u * gw, 0, 16);
    }
}

DI void a0_prep(unsigned char* ws, const int u, const u16* __restrict__ wqkv, const u16* __restrict__ wphi, const u16* __restrict__ awo) {
    const int tid = threadIdx.x, lane = tid & 63, w = __builtin_amdgcn_readfirstlane(tid >> 6);
    const int rho = lane & 15, g = lane >> 4;
    __amdgpu_buffer_rsrc_t rs = mkrs(ws, 0x7fffffff);
    {
        const int which = w >> 2, h2 = u;
        v4u bf[2];
#pragma unroll
        for (int t = 0; t < 2; ++t) {
            const int f = perm32(t, rho);
            unsigned e[8];
#pragma unroll
            for (int jj = 0; jj < 8; ++jj) { const int d = 8 * g + jj; e[jj] = (d < 28 && f < 28) ? wphi[(h2 * 28 + d) * 28 + f] : 0u; }
            bf[t] = v4u{e[0] | (e[1] << 16), e[2] | (e[3] << 16), e[4] | (e[5] << 16), e[6] | (e[7] << 16)};
        }
        v4u af[4][2];
#pragma unroll
        for (int i = 0; i < 4; ++i) {
            const int ks = min((w & 3) + 4 * i, 13);
#pragma unroll
            for (int t = 0; t < 2; ++t) {
                const u16* p = wqkv + (size_t)(32 * ks + perm32(t, rho)) * 1344 + which * 448 + h2 * 28 + g * 8;
                v2u lo = *(const v2u*)p, hi = {0, 0};
                if (g < 3) hi = *(const v2u*)(p + 4);
                af[i][t] = v4u{lo.x, lo.y, hi.x, hi.y};
            }
        }
#pragma unroll
        for (int i = 0; i < 4; ++i) {
            const int ks = (w & 3) + 4 * i;
            if (ks < 14) {
#pragma unroll
                for (int t = 0; t < 2; ++t) {
                    f4 d0 = mfma(af[i][0], bf[t], f4{0, 0, 0, 0}), d1 = mfma(af[i][1], bf[t], f4{0, 0, 0, 0});
                    st_sc1(pack8(d0 * LOG2E, d1 * LOG2E), rs, (unsigned)(WQF_OFF + which * WQF_ONE + ((size_t)(h2 * 2 + t) * 14 + ks) * 1024) + lane * 16, 0);
                }
            }
        }
    }
#pragma unroll
    for (int i = 0; i < 4; ++i) {
        const int fi = w + 8 * i;
        if (fi < 28) {
            const int t = fi / 14, ks = fi % 14;
            const int d = 16 * t + rho;
            unsigned e[8], e2[8];
            const int cb = fi >> 1, t2 = fi & 1; const int c = 32 * cb + perm32(t2, rho);
#pragma unroll
            for (int jj = 0; jj < 8; ++jj) {
                e[jj] = d < 28 ? wqkv[(size_t)(32 * ks + 8 * g + jj) * 1344 + 896 + u * 28 + d] : 0u;
                const int sl = 8 * g + jj; e2[jj] = sl < 28 ? awo[(size_t)(u * 28 + sl) * 448 + c] : 0u;
            }
            st_sc1(v4u{e[0] | (e[1] << 16), e[2] | (e[3] << 16), e[4] | (e[5] << 16), e[6] | (e[7] << 16)}, rs, (unsigned)(WQF_OFF + 2 * WQF_ONE + (size_t)(u * 28 + fi) * 1024) + lane * 16, 0);
            st_sc1(v4u{e2[0] | (e2[1] << 16), e2[2] | (e2[3] << 16), e2[4] | (e2[5] << 16), e2[6] | (e2[7] << 16)}, rs, (unsigned)(WOF_OFF + (size_t)((cb * 2 + t2) * 16 + u) * 1024) + lane * 16, 0);
        }
    }
}
DI void phi_pair(f4 d0, f4 d1, int g, v4u& pos, v4u& neg) {
    float v[8] = {d0[0], d0[1], d0[2], d0[3], d1[0], d1[1], d1[2], d1[3]};
    const bool tail = (g == 3);
    float mx = fmaxf(fmaxf(v[0], v[1]), fmaxf(v[2], v[3])), mn = fminf(fminf(v[0], v[1]), fminf(v[2], v[3]));
    float mx2 = fmaxf(fmaxf(v[4], v[5]), fmaxf(v[6], v[7])), mn2 = fminf(fminf(v[4], v[5]), fminf(v[6], v[7]));
    mx = tail ? mx : fmaxf(mx, mx2); mn = tail ? mn : fminf(mn, mn2);
    mx = xmax32(xmax16(mx)); mn = -xmax32(xmax16(-mn));
    float ep[8], en[8], sp = 0.f, sn = 0.f;
#pragma unroll
    for (int k = 0; k < 8; ++k) {
        ep[k] = __builtin_amdgcn_exp2f(v[k] - mx); en[k] = __builtin_amdgcn_exp2f(mn - v[k]);
        if (k >= 4) { ep[k] = tail ? 0.f : ep[k]; en[k] = tail ? 0.f : en[k]; }
        sp += ep[k]; sn += en[k];
    }
    sp = xsum32(xsum16(sp)); sn = xsum32(xsum16(sn));
    const float rp = __builtin_amdgcn_rcpf(sp), rn = __builtin_amdgcn_rcpf(sn);
    pos = v4u{pk2(ep[0] * rp, ep[1] * rp), pk2(ep[2] * rp, ep[3] * rp), pk2(ep[4] * rp, ep[5] * rp), pk2(ep[6] * rp, ep[7] * rp)};
    neg = v4u{pk2(en[0] * rn, en[1] * rn), pk2(en[2] * rn, en[3] * rn), pk2(en[4] * rn, en[5] * rn), pk2(en[6] * rn, en[7] * rn)};
}
DI void stage_x64(const u16* __restrict__ ax, unsigned char* lds, int blk64) {
    int tid = threadIdx.x; asm volatile("" : "+v"(tid));
    const int lane = tid & 63, w = tid >> 6;
    v4u stg[7];
#pragma unroll
    for (int i = 0; i < 7; ++i) {
        const int f = i * 8 + w; const int q = f / 14, ks = f % 14;
        stg[i] = *(const v4u*)(ax + (size_t)(blk64 * 64 + 32 * (q >> 1) + perm32(q & 1, lane & 15)) * D + ks * 32 + (lane >> 4) * 8);
    }
#pragma unroll
    for (int i = 0; i < 7; ++i) *(v4u*)(lds + (i * 512 + tid) * 16) = stg[i];
}

DI void a1_unit(unsigned char* ws, unsigned char* lds, const int u, const u16* __restrict__ ax) {
    const int tid = threadIdx.x, lane = tid & 63, w = __builtin_amdgcn_readfirstlane(tid >> 6);
    const int rho = lane & 15, g = lane >> 4;
    __amdgpu_buffer_rsrc_t rs = mkrs(ws, 0x7fffffff);
    unsigned char* T = lds + 57344 + w * 9216;
    constexpr int TS = 144;
    f4 kv[2][4][2];
#pragma unroll
    for (int a = 0; a < 2; ++a)
#pragma unroll
        for (int b = 0; b < 4; ++b) { kv[a][b][0] = f4{0,0,0,0}; kv[a][b][1] = f4{0,0,0,0}; }
    for (int pass = 0; pass < 2; ++pass) {
        __syncthreads();
        stage_x64(ax, lds, 2 * u + pass);
        __syncthreads();
#pragma unroll
        for (int hi = 0; hi < 2; ++hi) {
            const int h = 2 * w + hi;
            const unsigned wk = (unsigned)(WQF_OFF + WQF_ONE + (size_t)h * 28672) + lane * 16;
            const unsigned wv = (unsigned)(WQF_OFF + 2 * WQF_ONE + (size_t)h * 28672) + lane * 16;
            f4 ak[2][4], av[2][4];
#pragma unroll
            for (int t = 0; t < 2; ++t)
#pragma unroll
                for (int q = 0; q < 4; ++q) { ak[t][q] = f4{0,0,0,0}; av[t][q] = f4{0,0,0,0}; }
#pragma unroll 2
            for (int ks = 0; ks < 14; ++ks) {
                v4u k0 = ld_sc1(rs, wk + ks * 1024, 0), k1 = ld_sc1(rs, wk + (14 + ks) * 1024, 0);
                v4u v0 = ld_sc1(rs, wv + ks * 1024, 0), v1 = ld_sc1(rs, wv + (14 + ks) * 1024, 0);
#pragma unroll
                for (int q = 0; q < 4; ++q) {
                    v4u x = *(const v4u*)(lds + (q * 14 + ks) * 1024 + lane * 16);
                    ak[0][q] = mfma(k0, x, ak[0][q]); ak[1][q] = mfma(k1, x, ak[1][q]);
                    av[0][q] = mfma(x, v0, av[0][q]); av[1][q] = mfma(x, v1, av[1][q]);
                }
            }
#pragma unroll
            for (int q = 0; q < 4; ++q) {
                v4u pos, neg; phi_pair(ak[0][q], ak[1][q], g, pos, neg);
                const int tok = 32 * (q >> 1) + perm32(q & 1, rho);
                *(v4u*)(T + tok * TS + g * 16) = pos;
                *(v4u*)(T + tok * TS + 64 + g * 16) = neg;
            }
            asm volatile("s_waitcnt lgkmcnt(0)" ::: "memory");
            v4u vf[2][2];
#pragma unroll
            for (int bk = 0; bk < 2; ++bk)
#pragma unroll
                for (int t = 0; t < 2; ++t) {
                    vf[bk][t] = pack8(av[t][2 * bk], av[t][2 * bk + 1]);
                    if (t == 1 && rho == 12) vf[bk][t] = v4u{0x3F803F80u, 0x3F803F80u, 0x3F803F80u, 0x3F803F80u};
                }
            const unsigned trb = (8 * g + (rho >> 2)) * TS + (lane & 3) * 8;
#pragma unroll
            for (int bk = 0; bk < 2; ++bk)
#pragma unroll
                for (int ft = 0; ft < 4; ++ft) {
                    v2u lo = trread(T + trb + (32 * bk) * TS + ft * 32), hi2 = trread(T + trb + (32 * bk + 4) * TS + ft * 32);
                    v4u pa = {lo.x, lo.y, hi2.x, hi2.y};
                    kv[hi][ft][0] = mfma(pa, vf[bk][0], kv[hi][ft][0]);
                    kv[hi][ft][1] = mfma(pa, vf[bk][1], kv[hi][ft][1]);
                }
            asm volatile("s_waitcnt lgkmcnt(0)" ::: "memory");
        }
    }
#pragma unroll
    for (int hi = 0; hi < 2; ++hi)
#pragma unroll
        for (int ft = 0; ft < 4; ++ft)
#pragma unroll
            for (int dt = 0; dt < 2; ++dt)
                st_sc1(__builtin_bit_cast(v4u, kv[hi][ft][dt]), rs, (unsigned)(KVP_OFF + ((size_t)(u * 16 + 2 * w + hi) * 8 + ft * 2 + dt) * 1024) + lane * 16, 0);
}
DI void a15_unit(unsigned char* ws, unsigned char* lds, const int h) {
    const int tid = threadIdx.x, lane = tid & 63, w = tid >> 6;
    __amdgpu_buffer_rsrc_t rs = mkrs(ws, 0x7fffffff);
    f4 s = {0, 0, 0, 0};
    v4u t[16];
#pragma unroll
    for (int u = 0; u < 16; ++u) t[u] = ld_sc1(rs, (unsigned)(KVP_OFF + ((size_t)(u * 16 + h) * 8) * 1024) + tid * 16, 0);
#pragma unroll
    for (int u = 0; u < 16; ++u) s += __builtin_bit_cast(f4, t[u]);
    const int ft = w >> 1, dt = w & 1, rho = lane & 15, g = lane >> 4;
    __syncthreads();
    v2u o; o.x = pk2(s[0], s[1]); o.y = pk2(s[2], s[3]);
    *(v2u*)(lds + (16 * dt + rho) * 128 + (16 * ft + 4 * g) * 2) = o;
    __syncthreads();
    if (tid < 256) {
        const int fr = tid >> 6;
        const int tt = fr >> 1, kb = fr & 1;
        v4u v = *(const v4u*)(lds + perm32(tt, rho) * 128 + (32 * kb + 8 * g) * 2);
        st_sc1(v, rs, (unsigned)(KVF_OFF + (size_t)(h * 4 + fr) * 1024) + lane * 16, 0);
    }
}
DI void a2_unit(unsigned char* ws, unsigned char* lds, const int u, u16* __restrict__ out, const u16* __restrict__ ax) {
    int tid = threadIdx.x; asm volatile("" : "+v"(tid));
    const int lane = tid & 63, w = __builtin_amdgcn_readfirstlane(tid >> 6);
    const int rho = lane & 15, g = lane >> 4;
    __amdgpu_buffer_rsrc_t rs = mkrs(ws, 0x7fffffff);
    unsigned char* O = lds + 57344;
    __syncthreads();
    stage_x64(ax, lds, u);
    wg_wait(rs, C_A0, 16, 4);
    v4u pos[2][4], neg[2][4];
#pragma unroll
    for (int hi = 0; hi < 2; ++hi) {
        const int h = 2 * w + hi;
        const unsigned wq = (unsigned)(WQF_OFF + (size_t)h * 28672) + lane * 16;
        f4 aq[2][4];
#pragma unroll
        for (int t = 0; t < 2; ++t)
#pragma unroll
            for (int q = 0; q < 4; ++q) aq[t][q] = f4{0,0,0,0};
#pragma unroll 2
        for (int ks = 0; ks < 14; ++ks) {
            v4u k0 = ld_sc1(rs, wq + ks * 1024, 0), k1 = ld_sc1(rs, wq + (14 + ks) * 1024, 0);
#pragma unroll
            for (int q = 0; q < 4; ++q) {
                v4u x = *(const v4u*)(lds + (q * 14 + ks) * 1024 + lane * 16);
                aq[0][q] = mfma(k0, x, aq[0][q]); aq[1][q] = mfma(k1, x, aq[1][q]);
            }
        }
#pragma unroll
        for (int q = 0; q < 4; ++q) phi_pair(aq[0][q], aq[1][q], g, pos[hi][q], neg[hi][q]);
    }
    wg_wait(rs, C_KV, 16, 4);
#pragma unroll
    for (int hi = 0; hi < 2; ++hi) {
        const int h = 2 * w + hi;
        v4u kf[4];
#pragma unroll
        for (int fr = 0; fr < 4; ++fr) kf[fr] = ld_sc1(rs, (unsigned)(KVF_OFF + (size_t)(h * 4 + fr) * 1024) + lane * 16, 0);
#pragma unroll
        for (int q = 0; q < 4; ++q) {
            f4 n0 = mfma(kf[0], pos[hi][q], f4{0,0,0,0}); n0 = mfma(kf[1], neg[hi][q], n0);
            f4 n1 = mfma(kf[2], pos[hi][q], f4{0,0,0,0}); n1 = mfma(kf[3], neg[hi][q], n1);
            const float den = asf((unsigned)__builtin_amdgcn_ds_bpermute((48 + rho) * 4, (int)asu(n1[0])));
            const float rd = 1.0f / (den + 1e-6f);
            if (g == 3) n1 = f4{0, 0, 0, 0};
            *(v4u*)(O + (h * 4 + q) * 1024 + lane * 16) = pack8(n0 * rd, n1 * rd);
        }
    }
    __syncthreads();
    for (int cb = w; cb < 14; cb += 8) {
        f4 acc[2][4];
#pragma unroll
        for (int t = 0; t < 2; ++t)
#pragma unroll
            for (int q = 0; q < 4; ++q) acc[t][q] = f4{0,0,0,0};
        const unsigned wo = (unsigned)(WOF_OFF + (size_t)cb * 32768) + lane * 16;
#pragma unroll 4
        for (int h = 0; h < 16; ++h) {
            v4u w0 = ld_sc1(rs, wo + h * 1024, 0), w1 = ld_sc1(rs, wo + (16 + h) * 1024, 0);
#pragma unroll
            for (int q = 0; q < 4; ++q) {
                v4u o = *(const v4u*)(O + (h * 4 + q) * 1024 + lane * 16);
                acc[0][q] = mfma(w0, o, acc[0][q]); acc[1][q] = mfma(w1, o, acc[1][q]);
            }
        }
#pragma unroll
        for (int q = 0; q < 4; ++q) {
            const int tok = u * 64 + 32 * (q >> 1) + perm32(q & 1, rho);
            *(v4u*)(out + (size_t)tok * OUTW + 32 * cb + 8 * g) = pack8(acc[0][q], acc[1][q]);
        }
    }
}

DI void p2_unit(unsigned char* ws, unsigned char* lds, const int unit, u16* __restrict__ out) {
    const int seg = unit >> 1, chalf = unit & 1;
    int tid = threadIdx.x; asm volatile("" : "+v"(tid));
    const int lane = tid & 63, w = __builtin_amdgcn_readfirstlane(tid >> 6);
    const int rho = lane & 15, g = lane >> 4;
    __amdgpu_buffer_rsrc_t rs = mkrs(ws, 0x7fffffff);
    unsigned char* xl = lds + 40960;
    __syncthreads();
    for (int i = tid; i < 1792; i += 512) *(v4u*)(xl + i * 16) = *(const v4u*)(ws + XF_OFF + (size_t)seg * 28672 + i * 16);
    if (tid < 64 && seg > 8) wait_flags(rs, C_REC, seg - 8, 16);
    if (tid < 128) wait_flags(rs, C_GU + 128u * (unsigned)min(tid, 111), seg + 1, 8);
    __syncthreads();
    const unsigned sgb = segb(seg, (__builtin_amdgcn_readfirstlane(ldflag(rs, C_POL)) - 1u) * 8192u);
    f4 sx[2][2][2];
#pragma unroll
    for (int hi = 0; hi < 2; ++hi) {
#pragma unroll
        for (int a = 0; a < 2; ++a) { sx[hi][a][0] = f4{0,0,0,0}; sx[hi][a][1] = f4{0,0,0,0}; }
        const unsigned gb = sgb + (unsigned)(2 * w + hi) * 65536u + lane * 16;
#pragma unroll
        for (int ks = 0; ks < 14; ++ks) {
            v4u g0 = ld_sc1(rs, gb + (ks >> 3) * 16384 + (ks & 7) * 1024, 0), g1 = ld_sc1(rs, gb + 32768 + (ks >> 3) * 16384 + (ks & 7) * 1024, 0);
            v4u x0 = *(const v4u*)(xl + ks * 1024 + lane * 16), x1 = *(const v4u*)(xl + (14 + ks) * 1024 + lane * 16);
            sx[hi][0][0] = mfma(g0, x0, sx[hi][0][0]); sx[hi][0][1] = mfma(g0, x1, sx[hi][0][1]);
            sx[hi][1][0] = mfma(g1, x0, sx[hi][1][0]); sx[hi][1][1] = mfma(g1, x1, sx[hi][1][1]);
        }
    }
    f4* sxl = (f4*)(lds + 69632) + tid;
#pragma unroll
    for (int hi = 0; hi < 2; ++hi)
#pragma unroll
        for (int a = 0; a < 2; ++a)
#pragma unroll
            for (int b2 = 0; b2 < 2; ++b2) sxl[((hi * 2 + a) * 2 + b2) * 512] = sx[hi][a][b2];
    SB0
    const int cb = 7 * chalf + (w < 7 ? w : 6);
    v4u uf[2][16];
    unsigned uo[2], vo[2];
#pragma unroll
    for (int t = 0; t < 2; ++t) {
        const int c = 32 * cb + perm32(t, rho); const int j = c >> 3, c8 = c & 7;
        uo[t] = sgb + (unsigned)(64 + j) * 16384u + (c8 + 16 * g) * 16;
        vo[t] = (unsigned)(VP_OFF + (size_t)(seg * 56 + j) * 2048) + g * 128 + c8 * 16;
#pragma unroll
        for (int h = 0; h < 16; ++h) uf[t][h] = ld_sc1(rs, uo[t] + (h >> 1) * 1024 + (h & 1) * 128, 0);
    }
    if (seg < 61) { if (tid < 64) wait_flags(rs, C_REC, seg + 1, 8); }
    else if (tid < 128) { wait_flags(rs, C_REC, 59, 8); wait_flags(rs, C_DN2 + 4u * (unsigned)min(tid, 111), 1, 1); }
    __syncthreads();
    f4 sm[2] = {f4{0,0,0,0}, f4{0,0,0,0}};
    v4u vf[2][4];
    {
        const unsigned gmb = (unsigned)(GM_OFF + (size_t)(seg * 16 + 2 * w + (rho >> 3)) * 7168) + g * 128 + (rho & 7) * 16;
        v4u gm[14];
#pragma unroll
        for (int ks = 0; ks < 14; ++ks) gm[ks] = ld_sc1(rs, gmb + ks * 512, 0);
#pragma unroll
        for (int t = 0; t < 2; ++t)
#pragma unroll
            for (int w4 = 0; w4 < 4; ++w4) vf[t][w4] = ld_sc1(rs, vo[t] + w4 * 512, 0);
        SB0
#pragma unroll
        for (int ks = 0; ks < 14; ++ks) {
            v4u x0 = *(const v4u*)(xl + ks * 1024 + lane * 16), x1 = *(const v4u*)(xl + (14 + ks) * 1024 + lane * 16);
            sm[0] = mfma(gm[ks], x0, sm[0]); sm[1] = mfma(gm[ks], x1, sm[1]);
            SB0
        }
    }
#pragma unroll
    for (int hi = 0; hi < 2; ++hi) {
        const int h = 2 * w + hi;
        const bool own = ((g >> 1) == hi);
#pragma unroll
        for (int tp = 0; tp < 2; ++tp) {
            const int tq = perm32(tp, rho);
            float v[8];
#pragma unroll
            for (int tau = 0; tau < 2; ++tau) {
                const f4 sv = sxl[((hi * 2 + tau) * 2 + tp) * 512];
#pragma unroll
                for (int r = 0; r < 4; ++r) { const int key = 16 * tau + 4 * g + r; v[4 * tau + r] = (key <= tq) ? sv[r] : -3.0e38f; }
            }
            float mx = fmaxf(fmaxf(fmaxf(v[0], v[1]), fmaxf(v[2], v[3])), fmaxf(fmaxf(v[4], v[5]), fmaxf(v[6], v[7])));
            const float mo = fmaxf(fmaxf(sm[tp][0], sm[tp][1]), fmaxf(sm[tp][2], sm[tp][3]));
            mx = own ? fmaxf(mx, mo) : mx;
            mx = xmax32(xmax16(mx));
            float sum = 0.f, em[4];
#pragma unroll
            for (int k = 0; k < 8; ++k) { v[k] = __builtin_amdgcn_exp2f(v[k] - mx); sum += v[k]; }
#pragma unroll
            for (int r = 0; r < 4; ++r) { em[r] = own ? __builtin_amdgcn_exp2f(sm[tp][r] - mx) : 0.f; sum += em[r]; }
            sum = xsum32(xsum16(sum));
            const float rz = __builtin_amdgcn_rcpf(sum);
            v2u p0; p0.x = pk2(v[0] * rz, v[1] * rz); p0.y = pk2(v[2] * rz, v[3] * rz);
            v2u p1; p1.x = pk2(v[4] * rz, v[5] * rz); p1.y = pk2(v[6] * rz, v[7] * rz);
            *(v2u*)(lds + (h * 2 + tp) * 1024 + (rho + 16 * (g >> 1)) * 16 + (g & 1) * 8) = p0;
            *(v2u*)(lds + (h * 2 + tp) * 1024 + (rho + 16 * (2 + (g >> 1))) * 16 + (g & 1) * 8) = p1;
            if (own) {
                v2u pm; pm.x = pk2(em[0] * rz, em[1] * rz); pm.y = pk2(em[2] * rz, em[3] * rz);
                *(v2u*)(lds + ((16 + (h >> 2)) * 2 + tp) * 1024 + (rho + 16 * (h & 3)) * 16 + (g & 1) * 8) = pm;
            }
        }
    }
    __syncthreads();
    if (w < 7) {
        f4 acc[2][2] = {{f4{0,0,0,0}, f4{0,0,0,0}}, {f4{0,0,0,0}, f4{0,0,0,0}}};
#pragma unroll
        for (int h = 0; h < 16; ++h) {
            v4u p0 = *(const v4u*)(lds + (h * 2) * 1024 + lane * 16), p1 = *(const v4u*)(lds + (h * 2 + 1) * 1024 + lane * 16);
#pragma unroll
            for (int t = 0; t < 2; ++t) { acc[t][0] = mfma(uf[t][h], p0, acc[t][0]); acc[t][1] = mfma(uf[t][h], p1, acc[t][1]); }
        }
#pragma unroll
        for (int w4 = 0; w4 < 4; ++w4) {
            v4u p0 = *(const v4u*)(lds + ((16 + w4) * 2) * 1024 + lane * 16), p1 = *(const v4u*)(lds + ((16 + w4) * 2 + 1) * 1024 + lane * 16);
#pragma unroll
            for (int t = 0; t < 2; ++t) { acc[t][0] = mfma(vf[t][w4], p0, acc[t][0]); acc[t][1] = mfma(vf[t][w4], p1, acc[t][1]); }
        }
#pragma unroll
        for (int tp = 0; tp < 2; ++tp) {
            const int tok = seg * 32 + perm32(tp, rho);
            *(v4u*)(out + (size_t)tok * OUTW + 448 + 32 * cb + 8 * g) = pack8(acc[0][tp], acc[1][tp]);
        }
    }
}

constexpr int LDS_MAIN = 135168 + 64;
__global__ void __launch_bounds__(512, 2) k_main(unsigned char* ws, u16* __restrict__ out, const u16* __restrict__ ax, const u16* __restrict__ wqkv, const u16* __restrict__ wphi, const u16* __restrict__ awo, int dS, int dV, int dVm, int pad_) {
    extern __shared__ __attribute__((aligned(16))) unsigned char lds[];
    const int bidx = blockIdx.x;
    const int idx = (bidx >> 3) * 4 + (bidx & 3);
#define PT(i)
    if ((bidx & 7) < 4) { rec_role(ws, lds, idx, dS, dV, dVm); return; }
    __amdgpu_buffer_rsrc_t rs = mkrs(ws, 0x7fffffff);
    if (idx < 112) gu_role(ws, lds, idx);
    else {
        const int u = idx - 112;
        a0_prep(ws, u, wqkv, wphi, awo);
        signal_add(ws, C_A0);
        wg_wait(rs, C_A0, 16, 2);
        a1_unit(ws, lds, u, ax);
        signal_add(ws, C_A1);
        wg_wait(rs, C_A1, 16, 4);
        a15_unit(ws, lds, u);
        signal_add(ws, C_KV);
    }
    PT(0)
    for (;;) {
        __syncthreads();
        if (threadIdx.x == 0) LDSV(LDS_MAIN - 16) = __hip_atomic_fetch_add((unsigned*)(ws + CTRL_OFF + C_TICKET), 1u, __ATOMIC_RELAXED, __HIP_MEMORY_SCOPE_AGENT);
        __syncthreads();
        const int t = __builtin_amdgcn_readfirstlane(LDSV(LDS_MAIN - 16));
        if (t >= 160) break;
        if (t < 32) a2_unit(ws, lds, t, out, ax);
        else p2_unit(ws, lds, t - 32, out);
        if (t < 32) { PT(1) } else { PT(2) }
    }
}

#define REC_DS (81 | (50 << 8))
#define REC_DV (76 | (1 << 8))
#define REC_DVM (6 | (12 << 8) | (2 << 16))
constexpr int g_dS = REC_DS, g_dV = REC_DV, g_dVm = REC_DVM;

extern "C" void kernel_launch(void* const* d_in, const int* in_sizes, int n_in, void* d_out, int out_size, void* d_ws, size_t ws_size, hipStream_t stream) {
    const u16* A_x = (const u16*)d_in[0]; const u16* A_wqkv = (const u16*)d_in[1]; const u16* A_wphi = (const u16*)d_in[2]; const u16* A_wo = (const u16*)d_in[3];
    const u16* B_x = (const u16*)d_in[4]; const u16* B_mem0 = (const u16*)d_in[5];
    const u16* Wq = (const u16*)d_in[6]; const u16* Wk = (const u16*)d_in[7]; const u16* Wv = (const u16*)d_in[8]; const u16* Wo = (const u16*)d_in[9];
    u16* out = (u16*)d_out;
    unsigned char* ws = (unsigned char*)(((uintptr_t)d_ws + 2 * MB - 1) & ~(uintptr_t)(2 * MB - 1));
    (void)hipFuncSetAttribute((const void*)k_pre, hipFuncAttributeMaxDynamicSharedMemorySize, 61440);
    (void)hipFuncSetAttribute((const void*)k_main, hipFuncAttributeMaxDynamicSharedMemorySize, LDS_MAIN);
    k_pre<<<256, 448, 61440, stream>>>(ws, Wq, Wk, Wv, Wo, B_x, B_mem0);
    k_main<<<256, 512, LDS_MAIN, stream>>>(ws, out, A_x, A_wqkv, A_wphi, A_wo, g_dS, g_dV, g_dVm, 0);
}
```

```cpp
#include <hip/hip_runtime.h>
#include <hip/hip_bf16.h>
#include <stdint.h>

typedef unsigned short u16;
typedef unsigned v4u __attribute__((ext_vector_type(4)));
typedef unsigned v2u __attribute__((ext_vector_type(2)));
typedef float f4 __attribute__((ext_vector_type(4)));
typedef float f2 __attribute__((ext_vector_type(2)));
typedef short s8 __attribute__((ext_vector_type(8)));
typedef __bf16 bf2 __attribute__((ext_vector_type(2)));
#define DI __device__ __forceinline__

constexpr int D = 448, H = 16, DH = 256, MEM = 8, SEG = 32, NSEG = 64, S_TOK = 2048;
constexpr int NKS = 14;
constexpr int OUTW = 896;
constexpr unsigned SENT = 0xFFFFFFFFu;
constexpr float LOG2E = 1.4426950408889634f;

constexpr size_t MB = 1024 * 1024;
constexpr int KCOPY = 4;
#define FARX 8192u
constexpr size_t COMM_OFF = 0;
constexpr size_t COMM_BYTES = 2 * MB;
constexpr size_t CTRL_OFF = COMM_BYTES;
constexpr size_t AS_OFF = CTRL_OFF + 262144;
constexpr size_t AS_BYTES = (size_t)16 * 14 * 2 * 14 * 1024;
constexpr size_t BS_OFF = AS_OFF + AS_BYTES;
constexpr size_t BS_BYTES = (size_t)56 * 8 * 14 * 1024;
constexpr size_t XF_OFF = BS_OFF + BS_BYTES;
constexpr size_t XF_BYTES = (size_t)64 * 28 * 1024;
constexpr size_t GU_BASE = 40 * MB;
constexpr size_t GM_OFF = XF_OFF + XF_BYTES;
constexpr size_t GM_BYTES = (size_t)64 * 16 * 14 * 512;
constexpr size_t VP_OFF = GM_OFF + GM_BYTES;
constexpr size_t VP_BYTES = (size_t)64 * 56 * 4 * 512;
constexpr size_t AXF_OFF = VP_OFF + VP_BYTES;
constexpr size_t WQF_OFF = AXF_OFF + XF_BYTES;
constexpr size_t WQF_ONE = (size_t)16 * 2 * 14 * 1024;
constexpr size_t WOF_OFF = WQF_OFF + 3 * WQF_ONE;
constexpr size_t KVP_OFF = WOF_OFF + WQF_ONE;
constexpr size_t KVF_OFF = KVP_OFF + (size_t)16 * 16 * 8192;
constexpr size_t WS_END = KVF_OFF + 65536;
static_assert(WS_END + MB <= GU_BASE && GU_BASE % (4 * MB) == 0 && GU_BASE + 128 * MB + 2 * MB <= 256 * MB, "workspace layout");
constexpr unsigned C_POL = 0, C_REC = 128, C_DONE = 256, C_A1 = 384, C_KV = 512, C_TICKET = 640, C_A0 = 768, C_GU = 1024;
constexpr unsigned C_DN2 = 16384;
constexpr unsigned C_DBG = 32768;

DI unsigned pk2(float a, float b) { f2 v = {a, b}; return __builtin_bit_cast(unsigned, __builtin_convertvector(v, bf2)); }
DI float asf(unsigned x) { return __builtin_bit_cast(float, x); }
DI unsigned asu(float x) { return __builtin_bit_cast(unsigned, x); }
DI float bflo(unsigned x) { return __builtin_bit_cast(float, x << 16); }
DI float bfhi(unsigned x) { return __builtin_bit_cast(float, x & 0xffff0000u); }
DI float bf2f(u16 x) { return __builtin_bit_cast(float, (unsigned)x << 16); }
DI f4 mfma(v4u a, v4u b, f4 c) { return __builtin_amdgcn_mfma_f32_16x16x32_bf16(__builtin_bit_cast(s8, a), __builtin_bit_cast(s8, b), c, 0, 0, 0); }
DI v4u pack8(f4 a, f4 b) { v4u r; r.x = pk2(a[0], a[1]); r.y = pk2(a[2], a[3]); r.z = pk2(b[0], b[1]); r.w = pk2(b[2], b[3]); return r; }
DI v4u nosent(v4u v) { v.x = min(v.x, 0xFFFFFFFEu); v.y = min(v.y, 0xFFFFFFFEu); v.z = min(v.z, 0xFFFFFFFEu); v.w = min(v.w, 0xFFFFFFFEu); return v; }
DI int perm32(int t, int rho) { return 8 * (rho >> 2) + 4 * t + (rho & 3); }
DI __amdgpu_buffer_rsrc_t mkrs(void* p, unsigned bytes) { return __builtin_amdgcn_make_buffer_rsrc(p, 0, bytes, 0x00020000); }
DI v4u ld_sc1(__amdgpu_buffer_rsrc_t rs, unsigned voff, unsigned soff) { return __builtin_amdgcn_raw_buffer_load_b128(rs, voff, soff, 16); }
DI v2u ld2_sc1(__amdgpu_buffer_rsrc_t rs, unsigned voff, unsigned soff) { return __builtin_amdgcn_raw_buffer_load_b64(rs, voff, soff, 16); }
DI void st_sc1(v4u v, __amdgpu_buffer_rsrc_t rs, unsigned voff, unsigned soff) { __builtin_amdgcn_raw_buffer_store_b128(v, rs, voff, soff, 16); }
DI void st2_sc1(v2u v, __amdgpu_buffer_rsrc_t rs, unsigned voff, unsigned soff) { __builtin_amdgcn_raw_buffer_store_b64(v, rs, voff, soff, 16); }
DI unsigned long long mtime() { unsigned long long t; asm volatile("s_memtime %0\n\ts_waitcnt lgkmcnt(0)" : "=s"(t) :: "memory"); return t; }

typedef short s4t __attribute__((ext_vector_type(4)));
DI v2u trread(const unsigned char* p) { return __builtin_bit_cast(v2u, __builtin_amdgcn_ds_read_tr16_b64_v4i16((__attribute__((address_space(3))) s4t*)p)); }
DI unsigned comm_step_base(int step, int copy) { return __builtin_amdgcn_readfirstlane((unsigned)(step & 7) * 262144u + (unsigned)copy * 65536u); }
DI unsigned segb(int sg, unsigned polo) { return __builtin_amdgcn_readfirstlane((unsigned)GU_BASE + (unsigned)sg * 2097152u + ((polo ^ FARX ^ ((unsigned)(sg & 1) << 13)) & 8192u)); }
DI unsigned comm_off(int chunk0, unsigned o, unsigned pol) { return (unsigned)(chunk0 + (o >> 13)) * 16384u + pol * 8192u + (o & 8191u); }

__global__ void __launch_bounds__(448) k_pre(unsigned char* ws, const u16* __restrict__ Wq, const u16* __restrict__ Wk, const u16* __restrict__ Wv, const u16* __restrict__ Wo,
                                             const u16* __restrict__ bx, const u16* __restrict__ mem0) {
    extern __shared__ __attribute__((aligned(16))) unsigned char lds[];
    const int tid = threadIdx.x, lane = tid & 63, w = __builtin_amdgcn_readfirstlane(tid >> 6);
    const int wg = blockIdx.x;
#define PP(i)
    const int h = (wg & 7) * 2 + ((wg >> 3) & 1), rest = wg >> 4;
    const int mat = rest >> 3, rh = (rest >> 2) & 1, q = rest & 3;
    const int rho = lane & 15, g = lane >> 4;
    const int rb = 7 * rh + w;
    __amdgpu_buffer_rsrc_t rs = mkrs(ws, 0x7fffffff);
    const int gt = wg * 448 + tid; constexpr int GN = 256 * 448;
    constexpr int WST = 240;
    v4u bt[8];
    if (mat == 0) {
        const int ct = 7 * q + w; const int d = 32 * (ct >> 1) + perm32(ct & 1, rho);
#pragma unroll
        for (int es = 0; es < 8; ++es) bt[es] = *(const v4u*)(Wq + ((size_t)d * H + h) * DH + es * 32 + g * 8);
    } else {
#pragma unroll
        for (int it = 0; it < 8; ++it) {
            const int idx = it * 448 + tid; const int e = idx / 14, c8 = idx % 14;
            bt[it] = *(const v4u*)(Wo + ((size_t)h * DH + e) * D + 112 * q + c8 * 8);
        }
    }
    const u16* Wr = mat ? Wv : Wk;
    v4u a[2][8];
#pragma unroll
    for (int t = 0; t < 2; ++t) {
        const int row = 32 * rb + perm32(t, rho);
#pragma unroll
        for (int es = 0; es < 8; ++es) a[t][es] = *(const v4u*)(Wr + ((size_t)row * H + h) * DH + es * 32 + g * 8);
    }
    v4u xfv;
    {
        const int f = gt >> 6; const int ks = f % 14; const int st = f / 14; const int tau = st & 1, seg = st >> 1;
        xfv = *(const v4u*)(bx + (size_t)(seg * 32 + perm32(tau, rho)) * D + ks * 32 + g * 8);
    }
    v4u m0v = {0, 0, 0, 0};
    if (wg < 8) m0v = *(const v4u*)(mem0 + (tid & 7) * D + (tid >> 3) * 8);
    __builtin_amdgcn_sched_barrier(0);
    PP(0)
    if (mat == 0) {
#pragma unroll
        for (int es = 0; es < 8; ++es) *(v4u*)(lds + (w * 8 + es) * 1024 + lane * 16) = bt[es];
    } else {
#pragma unroll
        for (int it = 0; it < 8; ++it) { const int idx = it * 448 + tid; const int e = idx / 14, c8 = idx % 14; *(v4u*)(lds + e * WST + c8 * 16) = bt[it]; }
    }
    PP(3)
    asm volatile("s_waitcnt lgkmcnt(0)" ::: "memory"); __builtin_amdgcn_s_barrier(); asm volatile("" ::: "memory");
    PP(4)
    f4 acc[2][7];
#pragma unroll
    for (int t = 0; t < 2; ++t)
#pragma unroll
        for (int c = 0; c < 7; ++c) acc[t][c] = f4{0, 0, 0, 0};
    if (mat == 0) {
#pragma unroll
        for (int es = 0; es < 8; ++es)
#pragma unroll
            for (int c = 0; c < 7; ++c) {
                v4u bfr = *(const v4u*)(lds + (c * 8 + es) * 1024 + lane * 16);
                acc[0][c] = mfma(a[0][es], bfr, acc[0][c]);
                acc[1][c] = mfma(a[1][es], bfr, acc[1][c]);
            }
        const float sc = LOG2E * 0.0625f;
#pragma unroll
        for (int c = 0; c < 7; ++c) {
            const int ct = 7 * q + c;
            st_sc1(pack8(acc[0][c] * sc, acc[1][c] * sc), rs, (unsigned)(AS_OFF + ((size_t)((h * 14 + (ct >> 1)) * 2 + (ct & 1)) * 14 + rb) * 1024) + lane * 16, 0);
        }
    } else {
        const unsigned trb = (8 * g + (rho >> 2)) * WST + (lane & 3) * 8;
#pragma unroll
        for (int es = 0; es < 8; ++es)
#pragma unroll
            for (int c = 0; c < 7; ++c) {
                v2u lo = trread(lds + trb + (32 * es) * WST + c * 32), hi = trread(lds + trb + (32 * es + 4) * WST + c * 32);
                v4u bfr = {lo.x, lo.y, hi.x, hi.y};
                acc[0][c] = mfma(a[0][es], bfr, acc[0][c]);
                acc[1][c] = mfma(a[1][es], bfr, acc[1][c]);
            }
#pragma unroll
        for (int c = 0; c < 7; ++c) {
            const int ct = 7 * q + c;
            const int j = 2 * ct + (rho >> 3);
            st_sc1(pack8(acc[0][c], acc[1][c]), rs, (unsigned)(BS_OFF + ((size_t)(j * 8 + (h >> 1)) * 14 + rb) * 1024) + (((h & 1) * 8 + (rho & 7)) + 16 * g) * 16, 0);
        }
    }
    if (wg == 1 && tid < 128) __builtin_amdgcn_raw_buffer_store_b32(0u, rs, (unsigned)CTRL_OFF + tid * 128, 0, 16);
    if (wg == 2 && tid < 128) __builtin_amdgcn_raw_buffer_store_b32(0u, rs, (unsigned)CTRL_OFF + C_DN2 + tid * 4, 0, 16);
    for (unsigned i = gt; i < COMM_BYTES / 16; i += GN) {
        unsigned off = i * 16;
        unsigned within = off & 65535u, slot = off >> 18;
        unsigned chunk = (within >> 14) & 3, o = within & 8191u;
        if (chunk == 0 && (o >= 7184 || (slot == 0 && o < 7168))) continue;
        st_sc1(v4u{SENT, SENT, SENT, SENT}, rs, off, 0);
    }
    st_sc1(xfv, rs, (unsigned)XF_OFF + gt * 16, 0);
    if (wg < 8) st_sc1(nosent(m0v), rs, (wg >> 1) * 65536u + (wg & 1) * 8192u + tid * 16, 0);
    PP(5)
}

DI unsigned umax3(unsigned a, unsigned b, unsigned c) { return max(max(a, b), c); }
DI unsigned vmax4(unsigned t, v4u v) { return umax3(umax3(t, v.x, v.y), v.z, v.w); }
DI float dpp_xor8(float v) { return asf((unsigned)__builtin_amdgcn_update_dpp(0, (int)asu(v), 0x128, 0xf, 0xf, false)); }
DI unsigned dpp_xor8u(unsigned v) { return (unsigned)__builtin_amdgcn_update_dpp(0, (int)v, 0x128, 0xf, 0xf, false); }
constexpr unsigned SPIN_MAX = 200000;
DI float dpp_x1(float v) { return asf((unsigned)__builtin_amdgcn_update_dpp(0, (int)asu(v), 0xB1, 0xf, 0xf, false)); }
DI float dpp_x2(float v) { return asf((unsigned)__builtin_amdgcn_update_dpp(0, (int)asu(v), 0x4E, 0xf, 0xf, false)); }
DI f4 quadsum(f4 v) {
#pragma unroll
    for (int r = 0; r < 4; ++r) v[r] += dpp_x1(v[r]);
#pragma unroll
    for (int r = 0; r < 4; ++r) v[r] += dpp_x2(v[r]);
    return v;
}
DI float xmax16(float x) { auto r = __builtin_amdgcn_permlane16_swap(asu(x), asu(x), false, false); unsigned a = r[0], b = r[1]; return fmaxf(asf(a), asf(b)); }
DI float xmax32(float x) { auto r = __builtin_amdgcn_permlane32_swap(asu(x), asu(x), false, false); unsigned a = r[0], b = r[1]; return fmaxf(asf(a), asf(b)); }
DI float xsum16(float x) { auto r = __builtin_amdgcn_permlane16_swap(asu(x), asu(x), false, false); unsigned a = r[0], b = r[1]; return asf(a) + asf(b); }
DI float xsum32(float x) { auto r = __builtin_amdgcn_permlane32_swap(asu(x), asu(x), false, false); unsigned a = r[0], b = r[1]; return asf(a) + asf(b); }
#define LOADM(mf) v4u mf[14]; _Pragma("unroll") for (int ks = 0; ks < 14; ++ks) mf[ks] = *(const v4u*)(ldsm + ks * 512 + mfrag_off); __builtin_amdgcn_sched_barrier(0);
DI void bar() { asm volatile("s_waitcnt lgkmcnt(0)" ::: "memory"); __builtin_amdgcn_s_barrier(); asm volatile("" ::: "memory"); }
DI unsigned now32() { return (unsigned)mtime(); }
DI void wait_until(unsigned target) {
    int rem = (int)(target - now32());
    while (rem > 80) { __builtin_amdgcn_s_sleep(1); rem -= 64; }
    while (rem > 0) { asm volatile("s_nop 7" ::: "memory"); rem -= 16; }
}

DI unsigned rt32() { unsigned long long t; asm volatile("s_memrealtime %0\n\ts_waitcnt lgkmcnt(0)" : "=s"(t) :: "memory"); return (unsigned)t; }
DI unsigned approach_rt(unsigned target, int lead) {
    int rem;
    for (;;) {
        rem = (int)(target - rt32());
        if (rem <= lead + 4) break;
        int n = (rem - lead) >> 2;
        while (n-- > 0) __builtin_amdgcn_s_sleep(1);
    }
    return now32() + (unsigned)(rem * 23 - 110);
}
DI void clock_ref(unsigned& re, unsigned& me) {
    unsigned long long m0, m1, r[12];
    asm volatile("s_memtime %0\n\t"
        "s_memrealtime %1\n\ts_nop 1\n\ts_memrealtime %2\n\ts_nop 1\n\ts_memrealtime %3\n\ts_nop 1\n\ts_memrealtime %4\n\ts_nop 1\n\t"
        "s_memrealtime %5\n\ts_nop 1\n\ts_memrealtime %6\n\ts_nop 1\n\ts_memrealtime %7\n\ts_nop 1\n\ts_memrealtime %8\n\ts_nop 1\n\t"
        "s_memrealtime %9\n\ts_nop 1\n\ts_memrealtime %10\n\ts_nop 1\n\ts_memrealtime %11\n\ts_nop 1\n\ts_memrealtime %12\n\ts_nop 1\n\t"
        "s_memtime %13\n\ts_waitcnt lgkmcnt(0)"
        : "=&s"(m0), "=&s"(r[0]), "=&s"(r[1]), "=&s"(r[2]), "=&s"(r[3]), "=&s"(r[4]), "=&s"(r[5]), "=&s"(r[6]), "=&s"(r[7]), "=&s"(r[8]), "=&s"(r[9]), "=&s"(r[10]), "=&s"(r[11]), "=&s"(m1) :: "memory");
    unsigned cnt = 0;
#pragma unroll
    for (int i = 1; i < 12; ++i) cnt += ((unsigned)r[i] == (unsigned)r[0]) ? 1u : 0u;
    re = (unsigned)r[0] + 4u;
    me = (unsigned)m0 + ((((unsigned)m1 - (unsigned)m0) * (2u * cnt + 1u) * 43u) >> 10);
}
DI void sleep_to(unsigned tc) {
    for (;;) {
        const int rem = (int)(tc - now32());
        if (rem <= 0) return;
        if (rem > 260) { int n = (rem - 160) / 96; while (n-- > 0) __builtin_amdgcn_s_sleep(1); }
    }
}
DI void spin_to(unsigned tc) { while ((int)(tc - now32()) > 0) {} }
DI void wait_rt(unsigned target) { spin_to(approach_rt(target, 10)); }
template <int NP, bool CTRL>
DI unsigned gather_m(__amdgpu_buffer_rsrc_t rs, unsigned sb, unsigned char* ldsm, int p0, int lane, int skipline = -1) {
    const unsigned o0 = p0 * 1024 + lane * 16;
    for (unsigned spin = 0;; ++spin) {
        asm volatile("" ::: "memory");
        v4u a[NP]; v2u c = {0, 0};
        bool sk[NP];
#pragma unroll
        for (int k = 0; k < NP; ++k) { sk[k] = (int)((o0 + k * 1024) >> 7) == skipline; a[k] = ld_sc1(rs, (o0 + k * 1024) ^ (sk[k] ? 128u : 0u), sb); }
        if (CTRL) c = ld2_sc1(rs, 7168u, sb);
#pragma unroll
        for (int k = 0; k < NP; ++k) *(v4u*)(ldsm + (sk[k] ? 7424u + (lane & 7) * 16 : o0 + k * 1024)) = a[k];
        if (CTRL && lane == 0) *(v2u*)(ldsm + 7168) = c;
        unsigned t;
        if (NP == 2) t = max(vmax4(0, a[0]), vmax4(0, a[1])); else t = vmax4(0, a[0]);
        if (CTRL) t = umax3(t, c.x, c.y);
        if (__all(t != SENT) || spin > SPIN_MAX) return spin;
        __builtin_amdgcn_s_sleep(1);
    }
}
template <int NP, bool CTRL>
DI unsigned gather_fast(__amdgpu_buffer_rsrc_t rs, unsigned sb, unsigned char* ldsm, const unsigned (&vo)[2], const unsigned (&lo)[2], int p0, int lane, int skipline) {
    asm volatile("; gather %0 %1" :: "i"(NP), "i"((int)CTRL) : "memory");
    v4u a0 = ld_sc1(rs, vo[0], sb), a1; v2u c;
    if (NP == 2) a1 = ld_sc1(rs, vo[1], sb);
    if (CTRL) c = ld2_sc1(rs, 7168u, sb);
    __builtin_amdgcn_sched_barrier(0);
    *(v4u*)(ldsm + lo[0]) = a0;
    unsigned t = vmax4(0, a0);
    if (NP == 2) { *(v4u*)(ldsm + lo[1]) = a1; t = vmax4(t, a1); }
    if (CTRL) { *(v2u*)(ldsm + lo[1]) = c; t = umax3(t, c.x, c.y); }
    if (__builtin_expect(__all(t != SENT), 1)) return 0;
    gather_m<NP, CTRL>(rs, sb, ldsm, p0, lane, skipline);
    return 1;
}
#define GADDR(vo, lo, p0, np, skl) { _Pragma("unroll") for (int k_ = 0; k_ < 2; ++k_) { const unsigned o_ = ((p0) + k_) * 1024 + lane * 16; const bool sk_ = (int)(o_ >> 7) == (skl); \
        vo[k_] = o_ ^ (sk_ ? 128u : 0u); lo[k_] = sk_ ? 7424u + (lane & 7) * 16 : o_; } if ((np) == 1) lo[1] = lane == 0 ? 7168u : 7552u + (lane & 7) * 8; }
#define LOADP(mf) v4u mf[7]; _Pragma("unroll") for (int q_ = 0; q_ < 7; ++q_) mf[q_] = *(const v4u*)(ldsm + q_ * 1024 + pfrag_off); __builtin_amdgcn_sched_barrier(0);
DI unsigned reduce8(const float* rd, int lane) {
    const float* q = rd + (lane >> 3) * 100 + (lane & 7) * 12;
    f4 a = *(const f4*)q, b = *(const f4*)(q + 4);
    a += b;
    float v = (a[0] + a[1]) + (a[2] + a[3]);
    float o = dpp_x1(v);
    unsigned r = (lane & 1) ? pk2(o, v) : pk2(v, o);
    return min(r, 0xFFFFFFFEu);
}
#define READCTRL() { SAVER1 v2u c_ = *(const v2u*)(ldsm + 7168); R2 = __builtin_amdgcn_readfirstlane(c_.x); R1n = __builtin_amdgcn_readfirstlane(c_.y); }

#define SAVER1
typedef short s4 __attribute__((ext_vector_type(4)));
DI f4 mfma16(v2u a, v2u b, f4 c) { return __builtin_amdgcn_mfma_f32_16x16x16bf16_1k(__builtin_bit_cast(s4, a), __builtin_bit_cast(s4, b), c, 0, 0, 0); }
DI float vmax(float a, float b) { float r; asm("s_nop 1\n\tv_max_f32 %0, %1, %2" : "=v"(r) : "v"(a), "v"(b)); return r; }
DI float vmax3(float a, float b, float c) { float r; asm("v_max3_f32 %0, %1, %2, %3" : "=v"(r) : "v"(a), "v"(b), "v"(c)); return r; }
DI float vmax3_mfma(float a, float b, float c) { float r; asm("s_nop 3\n\tv_max3_f32 %0, %1, %2, %3" : "=v"(r) : "v"(a), "v"(b), "v"(c)); return r; }
DI float rmax16(float x) { auto r = __builtin_amdgcn_permlane16_swap(asu(x), asu(x), false, false); unsigned a = r[0], b = r[1]; return vmax(asf(a), asf(b)); }
DI float rmax32(float x) { auto r = __builtin_amdgcn_permlane32_swap(asu(x), asu(x), false, false); unsigned a = r[0], b = r[1]; return vmax(asf(a), asf(b)); }
constexpr int HS = 1408;
constexpr int ST_OFF = 16 * HS;
constexpr int ZL_OFF = ST_OFF + 1024;
constexpr int L_M = 0, L_RED = 8192, L_RED2 = 12800, L_GM = 16128, L_VP = 17152, L_FL = 19200, L_UF = 20480;

DI unsigned gather_ctrl(__amdgpu_buffer_rsrc_t rs, unsigned sb, unsigned char* ldsm, int lane) {
    for (unsigned spin = 0;; ++spin) {
        asm volatile("" ::: "memory");
        v2u c = ld2_sc1(rs, 7168u, sb);
        if (lane == 0) *(v2u*)(ldsm + 7168) = c;
        if (__all(max(c.x, c.y) != SENT) || spin > SPIN_MAX) return spin;
        __builtin_amdgcn_s_sleep(1);
    }
}
#define REREF 1
#define RSLEEP 0
#define XCK 100
#define FIRSTN 2
#define FLK 10
#define CQMAX 500u
#define LATEREF 1
#ifndef NVX
#define NVX 30u
#endif
#define TLBD 6
#define FASTN 0
#define FASTD 4u
#define ASLEEP 2
#define HSLEEP 2
#define SB0 __builtin_amdgcn_sched_barrier(0);

DI void glds16(const unsigned char* gsrc, unsigned lds_dst) {
    unsigned keep;
    asm volatile("s_mov_b32 %0, m0\n\ts_mov_b32 m0, %2\n\ts_nop 0\n\tglobal_load_lds_dwordx4 %1, off sc1\n\ts_mov_b32 m0, %0" : "=&s"(keep) : "v"(gsrc), "s"(lds_dst) : "memory");
}
DI void glds16x4(const unsigned char* gsrc, unsigned lds_dst) {
    unsigned keep;
    asm volatile("s_mov_b32 %0, m0\n\ts_mov_b32 m0, %2\n\ts_nop 0\n\t"
                 "global_load_lds_dwordx4 %1, off sc1\n\tglobal_load_lds_dwordx4 %1, off offset:1024 sc1\n\t"
                 "global_load_lds_dwordx4 %1, off offset:2048 sc1\n\tglobal_load_lds_dwordx4 %1, off offset:3072 sc1\n\t"
                 "s_mov_b32 m0, %0" : "=&s"(keep) : "v"(gsrc), "s"(lds_dst) : "memory");
}
DI void glds4(const unsigned char* gsrc, unsigned lds_dst) {
    unsigned keep;
    asm volatile("s_mov_b32 %0, m0\n\ts_mov_b32 m0, %2\n\ts_nop 0\n\tglobal_load_lds_dword %1, off sc1\n\ts_mov_b32 m0, %0" : "=&s"(keep) : "v"(gsrc), "s"(lds_dst) : "memory");
}
DI unsigned ldflag(__amdgpu_buffer_rsrc_t rs, unsigned off) { return __builtin_amdgcn_raw_buffer_load_b32(rs, (unsigned)CTRL_OFF + off, 0, 16); }
DI unsigned wait_flags(__amdgpu_buffer_rsrc_t rs, unsigned off, unsigned need, int slp) {
    unsigned v;
    for (unsigned spin = 0;; ++spin) {
        asm volatile("" ::: "memory");
        v = ldflag(rs, off);
        if (__all(v >= need) || spin > 4 * SPIN_MAX) break;
        for (int i = 0; i < slp; ++i) __builtin_amdgcn_s_sleep(4);
    }
#pragma unroll
    for (int o = 32; o >= 1; o >>= 1) v = min(v, (unsigned)__shfl_xor((int)v, o));
    return __builtin_amdgcn_readfirstlane(v);
}

#define LDSV(off) (*(volatile __attribute__((address_space(3))) unsigned*)((__attribute__((address_space(3))) unsigned char*)lds + (off)))
DI void rec_role(unsigned char* ws, unsigned char* lds, const int role, int dS, int dV, int dVm) {
    const int tid = threadIdx.x, lane = tid & 63, w = __builtin_amdgcn_readfirstlane(tid >> 6);
    const int rho = lane & 15, g = lane >> 4;
    __amdgpu_buffer_rsrc_t rs = mkrs(ws, 0x7fffffff);
    unsigned char* ldsm = lds + L_M;
    float* red = (float*)(lds + L_RED);
    float* red2 = (float*)(lds + L_RED2);
    if (role == 112) {
        unsigned best = 0xffffffffu;
        for (int r = 0; r < 10; ++r) {
            const v4u z = {SENT, SENT, SENT, SENT};
            const unsigned off = (unsigned)((w >> 1) + 4 * (r & 1)) * 262144u + ((r >> 1) & 3) * 65536u + 7680u + ((r >> 3) + 2 * (r & 1)) * 128u + (w & 1) * 8192u;
            const unsigned t0 = now32();
            if (lane < 8) st_sc1(z, rs, off + lane * 16, 0);
            asm volatile("s_waitcnt vmcnt(0)" ::: "memory");
            const unsigned dt = now32() - t0;
            if (r > 0) best = min(best, dt);
        }
        if (lane == 0) ((unsigned*)lds)[w] = best;
        __syncthreads();
        if (tid == 0) {
            const unsigned* bl = (const unsigned*)lds;
            const unsigned b0 = (bl[0] + bl[2]) + (bl[4] + bl[6]), b1 = (bl[1] + bl[3]) + (bl[5] + bl[7]);
            __builtin_amdgcn_raw_buffer_store_b32(b1 < b0 ? 2u : 1u, rs, (unsigned)CTRL_OFF + C_POL, 0, 16);
        }
        __syncthreads();
    }
#define POLINIT() const unsigned polo = (wait_flags(rs, C_POL, 1, 1) - 1u) * 8192u; const unsigned myc = (unsigned)(role & 3) * 65536u + polo;
    if (role < 112) { for (int i = 0; i < RSLEEP; ++i) __builtin_amdgcn_s_sleep(16); }
    unsigned nfail = 0, nfail2 = 0;
    unsigned long long* dbg = (unsigned long long*)(ws + CTRL_OFF + C_DBG);
#define STAMP(cond, idx)
#define TL(idx)
#define GT(idx, ref)
#define BT(i) {}
    constexpr unsigned TOFF = 70, TOFF2 = 30;
#define CLKUPD() { unsigned r1_, m1_; clock_ref(r1_, m1_); const unsigned dr = r1_ - r0, dm = m1_ - m0; \
        if (dr > 60u && dr < CQMAX) { const int err = (int)(dm * 256u) - (int)(cq * dr); cq = (unsigned)min(max((int)cq + (err >> (dr > 230u ? 9 : 8)), 4096), 6400); } r0 = r1_; m0 = m1_; }
#define CLKINIT() { for (int i_ = 0; i_ < 6; ++i_) __builtin_amdgcn_s_sleep(8); unsigned r1_, m1_; clock_ref(r1_, m1_); const unsigned dr = r1_ - r0, dm = m1_ - m0; \
        if (dr > 60u) cq = __builtin_amdgcn_readfirstlane(min(max((unsigned)((float)dm * 256.0f / (float)dr), 4096u), 6400u)); r0 = r1_; m0 = m1_; }
#define WAITR1() { unsigned tg_ = m0 - TOFF - NVX + (unsigned)(((int)(R1n - r0) * (int)cq) >> 8); \
        if (REREF && (int)(tg_ - now32()) > 1100) { sleep_to(tg_ - 900u); unsigned ra_, ma_; clock_ref(ra_, ma_); tg_ = ma_ - TOFF - NVX + (unsigned)(((int)(R1n - ra_) * (int)cq) >> 8); } \
        sleep_to(tg_); }
    const unsigned pfrag_off = (rho >> 3) * 512 + g * 128 + (rho & 7) * 16;
    const unsigned TA = dS & 255, TB = dV & 255, TE = dVm & 255, TLATE = (dS >> 8) & 255, XUP = (dV >> 8) & 255, XDN = (dVm >> 8) & 255, X0 = (dVm >> 16) & 255;
    unsigned R2 = 0, R1n = 0;
    const bool is_mx = role >= 112;
    const bool is_v = !is_mx && (((role >> 2) & 1) == 0);
    const int j = (role >> 3) * 4 + (role & 3);
    const int h = role / 7, p = role % 7;
    const int hp = (lane >> 3) & 1, i8 = lane & 7;
    const v4u SENT4 = {SENT, SENT, SENT, SENT};

    if (is_mx) {
        const int hx = role - 112;
        POLINIT()
        if (w >= 4) {
            const unsigned orst = comm_off(1, hx * HS + 896 + (lane & 31) * 16, 0) + (lane >> 5) * 65536u + polo;
            const unsigned orst2 = comm_off(1, ST_OFF + (hx >> 1) * 128 + (hx & 1) * 64 + (lane & 1) * 16, 0) + ((lane >> 1) & 3) * 65536u + polo;
            const unsigned gfl2 = C_GU + 128u * (unsigned)min((14 * hx) / 4 + (lane & 7), (14 * hx + 13) / 4);
            unsigned kn = 0, gvo[2], glo[2];
            GADDR(gvo, glo, 2 * (w - 4), (w < 7 ? 2 : 1), -1)
            unsigned r0 = 0, m0 = 0, cq = 5900u;
            if (w == 4) {
                for (int sl = 0; sl < 8; ++sl) {
                    const unsigned sbz = comm_step_base(sl, 0);
                    st_sc1(SENT4, rs, orst, sbz); st_sc1(SENT4, rs, orst + 131072u, sbz);
                    if (lane < 8) st_sc1(SENT4, rs, orst2, sbz);
                }
                asm volatile("s_waitcnt vmcnt(0)" ::: "memory");
            }
            if (w == 7) { clock_ref(r0, m0); CLKINIT() }
            if (w == 5 && lane == 0) LDSV(L_UF) = 0u;
            for (int step = 0; step < NSEG; ++step) {
                const unsigned sbr = comm_step_base(step, 0) + myc;
                if (step > 0 && w == 7) WAITR1()
                bar();
                if (w < 7) nfail += gather_fast<2, false>(rs, sbr, ldsm, gvo, glo, 2 * (w - 4), lane, -1);
                else nfail += gather_fast<1, true>(rs, sbr, ldsm, gvo, glo, 6, lane, -1);
                bar();
                READCTRL()
                bar();
                if (w == 4 && step >= 4) {
                    __builtin_amdgcn_s_sleep(4);
                    const unsigned sbz = comm_step_base(step + 4, 0);
                    st_sc1(SENT4, rs, orst, sbz); st_sc1(SENT4, rs, orst + 131072u, sbz);
                    if (lane < 8) st_sc1(SENT4, rs, orst2, sbz);
                }
                if (w == 7) CLKUPD()
                if (w == 5 && kn < 64u) {
                    __builtin_amdgcn_s_sleep(4);
                    kn = wait_flags(rs, gfl2, 0, 1);
                    if (lane == 0) LDSV(L_UF) = kn;
                }
            }
        } else {
            __builtin_amdgcn_s_setprio(2);
            const int tau = w & 1, kh = w >> 1;
            v4u wa[8], wb[8];
            const unsigned gbase = (unsigned)((hx * 2 + tau) * 2 + kh) * 16384u + lane * 16;
            const unsigned g67 = gbase + (kh ? 4096u : 6144u);
#define GLOAD(wt, sb_) { _Pragma("unroll") for (int ks = 0; ks < 6; ++ks) wt[ks] = ld_sc1(rs, gbase + ks * 1024, sb_); \
                     wt[6] = ld_sc1(rs, g67, sb_); wt[7] = ld_sc1(rs, g67 + 1024, sb_); }
            const unsigned gfl = C_GU + 128u * (unsigned)min((14 * hx) / 4 + (lane & 7), (14 * hx + 13) / 4);
            unsigned known = wait_flags(rs, gfl, 2, 1);
            GLOAD(wa, segb(0, polo))
            GLOAD(wb, segb(1, polo))
            known = wait_flags(rs, gfl, FIRSTN, 1);
            float* sc = red;
            const int tq = lane & 7, ir = lane >> 3;
            const unsigned oes = comm_off(1, hx * HS + 896 + lane * 8, 0) + w * 65536u + polo;
            const unsigned oss = comm_off(1, ST_OFF + (hx >> 1) * 128 + (hx & 1) * 64 + ir * 4, 0) + w * 65536u + polo;
#define MXGT
#define MXSTEP(wt, step) { \
                const bool last = ((step) == NSEG - 1); \
                const unsigned sbs = comm_step_base(step, 0); \
                __builtin_amdgcn_s_barrier(); \
                bar(); \
                f4 aE = {0, 0, 0, 0}, aO = {0, 0, 0, 0}; \
                v4u mf[4]; \
                _Pragma("unroll") for (int q = 0; q < 3; ++q) mf[q] = *(const v4u*)(ldsm + (4 * kh + q) * 1024 + pfrag_off); \
                mf[3] = *(const v4u*)(ldsm + 3 * 1024 + pfrag_off); \
                SB0 \
                _Pragma("unroll") for (int q = 0; q < 3; ++q) { aE = mfma(wt[2 * q], mf[q], aE); aO = mfma(wt[2 * q + 1], mf[q], aO); } \
                if (kh == 0) { aE = mfma(wt[6], mf[3], aE); aO = mfma(wt[7], mf[3], aO); } \
                f4 xs = hp ? aO : aE; \
                *(f4*)(sc + ((2 * kh + hp) * 8 + i8) * 36 + 16 * tau + 4 * g) = xs; \
                bar(); \
                const float* sp = sc + ir * 36 + 4 * tq; \
                f4 a0 = (*(const f4*)(sp) + *(const f4*)(sp + 288)) + (*(const f4*)(sp + 576) + *(const f4*)(sp + 864)); \
                float mx; \
                asm("v_max3_f32 %0, %1, %2, %3\n\tv_max_f32 %0, %0, %4\n\ts_nop 1\n\t" \
                    "v_max_f32_dpp %0, %0, %0 quad_perm:[1,0,3,2] row_mask:0xf bank_mask:0xf\n\ts_nop 1\n\t" \
                    "v_max_f32_dpp %0, %0, %0 quad_perm:[2,3,0,1] row_mask:0xf bank_mask:0xf\n\ts_nop 1\n\t" \
                    "v_max_f32_dpp %0, %0, %0 row_half_mirror row_mask:0xf bank_mask:0xf" \
                    : "=&v"(mx) : "v"(a0[0]), "v"(a0[1]), "v"(a0[2]), "v"(a0[3])); \
                if (!last) { \
                    unsigned e0 = pk2(__builtin_amdgcn_exp2f(a0[0] - mx), __builtin_amdgcn_exp2f(a0[1] - mx)); \
                    unsigned e1 = pk2(__builtin_amdgcn_exp2f(a0[2] - mx), __builtin_amdgcn_exp2f(a0[3] - mx)); \
                    v2u eo; eo.x = min(e0, 0xFFFFFFFEu); eo.y = min(e1, 0xFFFFFFFEu); \
                    st2_sc1(eo, rs, oes, sbs); \
                    if (tq == 1) __builtin_amdgcn_raw_buffer_store_b32(min(asu(mx), 0xFFFFFFFEu), rs, oss, sbs, 16); \
                    MXGT \
                } \
                { const int sn = min((step) + 2, NSEG - 1); \
                  known = max(known, (unsigned)__builtin_amdgcn_readfirstlane(LDSV(L_UF))); \
                  if (known < (unsigned)sn + 1) known = wait_flags(rs, gfl, sn + 1, 1); \
                  const unsigned sb = segb(sn, polo); \
                  GLOAD(wt, sb) } }
            for (int step = 0; step < NSEG; step += 2) {
                MXSTEP(wa, step)
                MXSTEP(wb, step + 1)
            }
        }
    } else if (w >= 4) {
        const int wv = w - 4;
        v4u wtv[2][14];
        if (is_v) {
            const unsigned char* src = ws + BS_OFF + ((size_t)j * 8 + 2 * wv) * 14 * 1024;
#pragma unroll
            for (int t = 0; t < 2; ++t)
#pragma unroll
                for (int f = 0; f < 14; ++f) wtv[t][f] = *(const v4u*)(src + (t * 14 + f) * 1024 + lane * 16);
        } else {
#pragma unroll
            for (int t = 0; t < 2; ++t)
#pragma unroll
                for (int f = 0; f < 14; ++f) wtv[t][f] = v4u{0, 0, 0, 0};
        }
        POLINIT()
        __builtin_amdgcn_s_waitcnt(0x0F70);
        const bool cond = is_v && j == 0;
        const bool slow = w >= 6;
        const unsigned ufl = C_GU + 128u * (56 + j);
        const unsigned char* usrc = ws + (size_t)(64 + j) * 16384 + (wv & 1) * 4096 + lane * 16;
        const unsigned udst = (unsigned)(size_t)(__attribute__((address_space(3))) unsigned char*)lds + L_UF + (wv & 1) * 4096;
#define UDMA(sg) { const unsigned char* p_ = usrc + segb(sg, polo); const unsigned d_ = __builtin_amdgcn_readfirstlane(udst + ((sg) & 3) * 8192); \
            glds16x4(p_, d_); }
#define VPRIME() { \
                f4 e0 = {0, 0, 0, 0}, o0 = {0, 0, 0, 0}, e1 = {0, 0, 0, 0}, o1 = {0, 0, 0, 0}; \
                _Pragma("unroll") for (int q = 0; q < 7; ++q) { \
                    e0 = mfma(mf[q], wtv[0][2 * q], e0); o0 = mfma(mf[q], wtv[0][2 * q + 1], o0); \
                    e1 = mfma(mf[q], wtv[1][2 * q], e1); o1 = mfma(mf[q], wtv[1][2 * q + 1], o1); } \
                f4 cs; \
                _Pragma("unroll") for (int r = 0; r < 4; ++r) { \
                    auto sw = __builtin_amdgcn_permlane32_swap(asu(o0[r]), asu(e1[r]), false, false); \
                    unsigned d0 = sw[0], d1 = sw[1]; \
                    cs[r] = g < 2 ? e0[r] + asf(d1) : o1[r] + asf(d0); } \
                v2u vp; vp.x = pk2(cs[0], cs[1]); vp.y = pk2(cs[2], cs[3]); \
                *(v2u*)(lds + L_VP + wv * 512 + (2 * (g >> 1) + hp) * 128 + i8 * 16 + (g & 1) * 8) = vp; }
#define BYPROD(st_) { \
            if (w == 7) { v4u gmv = *(const v4u*)(lds + L_GM + lane * 16); \
                st_sc1(gmv, rs, (unsigned)(GM_OFF + ((size_t)h * 14 + 2 * p) * 512) + lane * 16, __builtin_amdgcn_readfirstlane((st_) * 16 * 14 * 512)); } \
            if (w == 6 && is_v) { v4u v0 = *(const v4u*)(lds + L_VP + lane * 16), v1 = *(const v4u*)(lds + L_VP + 1024 + lane * 16); \
                const unsigned sbv = __builtin_amdgcn_readfirstlane((st_) * 56 * 4 * 512); \
                st_sc1(v0, rs, (unsigned)(VP_OFF + (size_t)j * 2048) + lane * 16, sbv); \
                st_sc1(v1, rs, (unsigned)(VP_OFF + (size_t)j * 2048 + 1024) + lane * 16, sbv); } }
#define RESETS() if (w == 7 && step >= 4) { \
                    if (dorst) st_sc1(SENT4, rs, orst, comm_step_base(step + 4, 0)); \
                    if (cond && lane < 4) st_sc1(SENT4, rs, polo + 7168u + lane * 65536u, comm_step_base(step + 4, 0)); }
        const unsigned orst = (lane < 32 ? comm_off(1, h * HS + p * 128 + (lane & 7) * 16, 0) : (unsigned)(j * 128 + (lane & 7) * 16)) + ((lane >> 3) & 3) * 65536u + polo;
        const bool dorst = lane < 32 || is_v;
#define CSTORES() { \
                    if (cond && lane < 4) st_sc1(SENT4, rs, polo + 7168u + lane * 65536u, comm_step_base(step + 4, 0)); \
                    if (dorst) st_sc1(SENT4, rs, orst, comm_step_base(step + 4, 0)); \
                    v4u gmv = *(const v4u*)(lds + L_GM + lane * 16); \
                    st_sc1(gmv, rs, (unsigned)(GM_OFF + ((size_t)h * 14 + 2 * p) * 512) + lane * 16, __builtin_amdgcn_readfirstlane(step * 16 * 14 * 512)); }
        if (w == 7) {
            unsigned known = 0;
            for (int sl = 0; sl < 8; ++sl) {
                if (dorst && (sl > 0 || lane < 32)) st_sc1(SENT4, rs, orst, comm_step_base(sl, 0));
                if (cond && lane < 4 && sl > 0) st_sc1(SENT4, rs, polo + 7168u + lane * 65536u, comm_step_base(sl, 0));
            }
            asm volatile("s_waitcnt vmcnt(0)" ::: "memory");
            unsigned r0, m0, cq = 5900u; clock_ref(r0, m0);
            CLKINIT()
            if (is_v) { known = wait_flags(rs, ufl, 2, 1); UDMA(0) UDMA(1) asm volatile("s_waitcnt vmcnt(0)" ::: "memory"); }
            for (int step = 0; step < NSEG; ++step) {
                if (step > 0) {
                    if (LATEREF && is_v) {
                        const v2u lr = *(const v2u*)(lds + L_FL + 528);
                        const unsigned ra_ = __builtin_amdgcn_readfirstlane(lr.x), ma_ = __builtin_amdgcn_readfirstlane(lr.y);
                        sleep_to(ma_ - TOFF + (unsigned)(((int)(R1n - ra_) * (int)cq) >> 8));
                    } else WAITR1()
                }
                bar();
                BT(0)
                CLKUPD()
                if (lane == 0) *(v4u*)(lds + L_FL + 512) = v4u{r0, m0 - TOFF2, cq, 0u};
                bar();
                __builtin_amdgcn_s_sleep(HSLEEP);
                READCTRL()
                LOADP(mf)
                bar();
                if (is_v) VPRIME()
                bar();
                asm volatile("s_waitcnt vmcnt(0)" ::: "memory");
                if (is_v && step + 3 < NSEG) UDMA(step + 2)
                if (is_v) bar();
                else CSTORES()
            }
            bar();
            asm volatile("s_waitcnt vmcnt(0)" ::: "memory");
            __builtin_amdgcn_s_barrier();
        } else if (w == 6) {
            unsigned known = 0;
            if (is_v) { known = wait_flags(rs, ufl, 2, 1); UDMA(0) UDMA(1) asm volatile("s_waitcnt vmcnt(0)" ::: "memory"); known = wait_flags(rs, ufl, FIRSTN, 1); }
            const bool reporter = (role == 4);
            if (role >= 12 && role < 16 && lane < TLBD) { unsigned d_ = *(const unsigned*)(ws + GU_BASE + (size_t)lane * 2097152 + 2031616); asm volatile("" :: "v"(d_)); }
            const unsigned char* fsrc = ws + CTRL_OFF + ufl;
            const unsigned fdst = __builtin_amdgcn_readfirstlane((unsigned)(size_t)(__attribute__((address_space(3))) unsigned char*)lds + L_FL);
            if (lane == 0) LDSV(L_FL) = 0u;
#define VPSTORE(st_) { v4u v0 = *(const v4u*)(lds + L_VP + lane * 16), v1 = *(const v4u*)(lds + L_VP + 1024 + lane * 16); \
                const unsigned sbv = __builtin_amdgcn_readfirstlane((st_) * 56 * 4 * 512); \
                st_sc1(v0, rs, (unsigned)(VP_OFF + (size_t)j * 2048) + lane * 16, sbv); \
                st_sc1(v1, rs, (unsigned)(VP_OFF + (size_t)j * 2048 + 1024) + lane * 16, sbv); }
            for (int step = 0; step < NSEG; ++step) {
                bar();
                if (is_v) {
                    asm volatile("s_waitcnt vmcnt(2)" ::: "memory");
                    __builtin_amdgcn_s_sleep(ASLEEP);
                    if (step + 3 < NSEG) {
                        if (known < 64u) known = max(known, (unsigned)__builtin_amdgcn_readfirstlane(LDSV(L_FL)));
                        if (known < (unsigned)step + 3) known = wait_flags(rs, ufl, step + 3, 1);
                        UDMA(step + 2)
                    }
                    if (step > 0) VPSTORE(step - 1)
                    if (known < (unsigned)step + FLK && known < 64u && lane == 0) glds4(fsrc, fdst);
                }
                bar();
                __builtin_amdgcn_s_sleep(HSLEEP);
                LOADP(mf)
                bar();
                if (is_v) VPRIME()
                bar();
                if (role >= 12 && role < 16 && lane == 0 && step + TLBD < NSEG)
                    glds4(ws + GU_BASE + (size_t)(step + TLBD) * 2097152 + 2031616, fdst + 32);
                if (reporter && lane == 0 && step >= 3) __builtin_amdgcn_raw_buffer_store_b32((unsigned)(step - 2), rs, (unsigned)CTRL_OFF + C_REC, 0, 16);
                if (is_v) { CSTORES() bar(); }
            }
            bar();
            if (is_v) VPSTORE(NSEG - 1)
            asm volatile("s_waitcnt vmcnt(0)" ::: "memory");
            __builtin_amdgcn_s_barrier();
        } else {
            unsigned extra = X0, calm = 0, ont = 0, gvo[2] = {0, 0}, glo[2] = {0, 0};
            if (cond && w == 5) {
                wait_flags(rs, ufl, FIRSTN, 1);
                unsigned t = rt32() + 120u;
                v2u c; c.x = min(t, 0xFFFFFFFEu); c.y = min(t + TB, 0xFFFFFFFEu);
                if (lane < KCOPY) st2_sc1(c, rs, polo + 7168u + lane * 65536u, 0);
                if (lane == 0) *(v2u*)(ldsm + 7168) = c;
            }
            const unsigned opub = polo + j * 128 + (lane >> 1) * 4 + (lane & 1) * 65536u;
            const unsigned opubS = comm_off(1, h * HS + p * 128 + (lane >> 1) * 4, 0) + (lane & 1) * 65536u + polo;
            for (int step = 0; step < NSEG; ++step) {
                const bool last = (step == NSEG - 1);
                const unsigned sbs = comm_step_base(step, 0);
                const unsigned sbr = sbs + myc;
                const unsigned sbn = comm_step_base(step + 1, 0);
                const int skl = (is_v && step > 0) ? j : -1;
                if (step < 2) GADDR(gvo, glo, w + 1, 1, skl)
                bar();
                if (w == 4 || cond) nfail += gather_fast<1, false>(rs, sbr, ldsm, gvo, glo, w + 1, lane, skl);
                else nfail += gather_fast<1, true>(rs, sbr, ldsm, gvo, glo, 6, lane, skl);
                bar();
                if (w == 5) BT(1)
                READCTRL()
                __builtin_amdgcn_s_sleep(HSLEEP);
                LOADP(mf)
                if (w == 4) __builtin_amdgcn_s_setprio(3);
                bar();
                if (w == 5) BT(2)
                if (w == 4) {
                    if (!last) {
                        unsigned o = reduce8(red, lane);
                        __builtin_amdgcn_raw_buffer_store_b32(o, rs, opubS, sbs, 16);
                        __builtin_amdgcn_raw_buffer_store_b32(o, rs, opubS + 131072u, sbs, 16);
                        BT(5)
                        if ((lane & 1) == 0) *(unsigned*)(lds + L_FL + 640 + (lane >> 1) * 4) = o;
                    }
                    __builtin_amdgcn_s_setprio(0);
                }
                if (is_v) {
                    VPRIME()
                    bar();
                    if (w == 5) BT(3)
                    unsigned ra_ = 0, ma_ = 0;
                    if (w == 5) { clock_ref(ra_, ma_); if (lane == 0) *(v2u*)(lds + L_FL + 528) = v2u{ra_, ma_}; }
                    unsigned xclk = 0;
                    if (cond && w == 5) {
                        const int cqv = (int)__builtin_amdgcn_readfirstlane(LDSV(L_FL + 520));
                        xclk = (unsigned)max((5950 - cqv) * XCK, 0) >> 13;
                    }
                    if (w == 4) __builtin_amdgcn_s_setprio(3);
                    bar();
                    if (w == 5 && !cond) BT(4)
                    if (!last) {
                        if (w == 4) {
                            unsigned o = reduce8(red2, lane);
                            __builtin_amdgcn_raw_buffer_store_b32(o, rs, opub, sbn, 16);
                            __builtin_amdgcn_raw_buffer_store_b32(o, rs, opub + 131072u, sbn, 16);
                            BT(6)
                            if ((lane & 1) == 0) *(unsigned*)(ldsm + j * 128 + (lane >> 1) * 4) = o;
                            __builtin_amdgcn_s_setprio(0);
                        }
                        if (cond && w == 5) {
                            const unsigned tp = ra_ + (((now32() - ma_) * 11u) >> 8) + 2u;
                            unsigned base = R1n;
                            if ((int)(tp + 4u - R1n) > 0) { base = tp + TLATE; if (ont) extra = min(extra + XUP, 10u); ont = 0; calm = 0; }
                            else { ont = 1; if (++calm >= ((step < FASTN && extra > 1u) ? FASTD : XDN)) { calm = 0; extra = extra ? extra - 1u : 0u; } }
                            const unsigned ex = extra + xclk;
                            v2u c; c.x = min(base + TA + ex, 0xFFFFFFFEu); c.y = min(base + TA + TB + 2u * ex, 0xFFFFFFFEu);
                            if (lane < KCOPY) st2_sc1(c, rs, polo + 7168u + lane * 65536u, sbn);
                            if (lane == 0) *(v2u*)(ldsm + 7168) = c;
                        }
                    }
                } else bar();
            }
            bar();
            __builtin_amdgcn_s_barrier();
            if (w == 4 && lane == 0) __builtin_amdgcn_raw_buffer_store_b32(1u, rs, (unsigned)CTRL_OFF + C_DN2 + role * 4, 0, 16);
        }
    } else {
        __builtin_amdgcn_s_setprio(2);
        const int b = 2 * p + (w >> 1), t = w & 1;
        v4u wt[14];
        {
            const unsigned char* src = ws + AS_OFF + ((size_t)(h * 14 + b) * 2 + t) * 14 * 1024;
#pragma unroll
            for (int ks = 0; ks < 14; ++ks) wt[ks] = *(const v4u*)(src + ks * 1024 + lane * 16);
        }
        POLINIT()
        __builtin_amdgcn_s_waitcnt(0x0F70);
        unsigned op[2][2], oe[2], ost, gvo[2] = {0, 0}, glo[2] = {0, 0};
        v4u pf[2][2], ef[2]; unsigned stt;
#pragma unroll
        for (int a = 0; a < 2; ++a) {
            const int hd = 4 * w + 2 * a + hp;
#pragma unroll
            for (int u = 0; u < 2; ++u) {
                const int q = 4 * u + g;
                const bool z = (q == 7) || (hd == h && q == p);
                op[a][u] = z ? 0x80000000u : comm_off(1, hd * HS + q * 128 + i8 * 16, 0);
            }
            oe[a] = comm_off(1, hd * HS + 896 + i8 * 64 + g * 16, 0);
        }
        const int hl = 4 * w + 2 * (g >> 1) + hp;
        ost = comm_off(1, ST_OFF + (hl >> 1) * 128 + (hl & 1) * 64 + i8 * 4, 0);
        const bool ownhead = (hl == h);
        v4u selLo = {0, 0, 0, 0}, selHi = {0, 0, 0, 0}, oneLo, oneHi;
        {
            const unsigned one = 0x3F80u << (16 * (rho & 1));
            const int wd = (rho & 7) >> 1;
            v4u sv; sv.x = wd == 0 ? one : 0; sv.y = wd == 1 ? one : 0; sv.z = wd == 2 ? one : 0; sv.w = wd == 3 ? one : 0;
            if (rho < 8) selLo = sv; else selHi = sv;
            const unsigned o2 = 0x3F803F80u;
            oneLo = rho < 8 ? v4u{o2, o2, o2, o2} : v4u{0, 0, 0, 0};
            oneHi = rho < 8 ? v4u{0, 0, 0, 0} : v4u{o2, o2, o2, o2};
        }
        for (int step = 0; step < NSEG; ++step) {
            const bool last = (step == NSEG - 1);
            const unsigned sbs = comm_step_base(step, 0);
            const unsigned sbr = sbs + myc;
            STAMP(step == 1, 0); STAMP(step == 63, 1);
            if (step < 2) GADDR(gvo, glo, (w == 0 ? 0 : w + 1), 2, ((is_v && step > 0) ? j : -1))
            bar();
            TL(0)
            const int skl = (is_v && step > 0) ? j : -1;
            if (w == 0) nfail += gather_fast<2, false>(rs, sbr, ldsm, gvo, glo, 0, lane, skl);
            else nfail += gather_fast<1, false>(rs, sbr, ldsm, gvo, glo, w + 1, lane, skl);
            bar();
            TL(1)
            f4 aE = {0, 0, 0, 0}, aO = {0, 0, 0, 0};
            v4u mf[7];
#pragma unroll
            for (int q_ = 0; q_ < 7; ++q_) mf[q_] = *(const v4u*)(ldsm + q_ * 1024 + pfrag_off);
            v2u mb = *(const v2u*)(ldsm + b * 512 + g * 128 + i8 * 16 + t * 8);
            const unsigned cwx = *(const unsigned*)(ldsm + 7168);
            const v4u ckr = *(const v4u*)(lds + L_FL + 512);
            SB0
#pragma unroll
            for (int q = 0; q < 7; ++q) { aE = mfma(wt[2 * q], mf[q], aE); aO = mfma(wt[2 * q + 1], mf[q], aO); }
            f4 xs = hp ? aO : aE;
            v2u gh; gh.x = pk2(xs[0], xs[1]); gh.y = pk2(xs[2], xs[3]);
            f4 sp = mfma16(gh, mb, f4{0, 0, 0, 0});
            if (rho < 8) {
                float* rq = red + rho * 100 + (4 * (g & 1)) * 12 + 2 * w + (g >> 1);
                rq[0] = sp[0]; rq[12] = sp[1]; rq[24] = sp[2]; rq[36] = sp[3];
            }
            SB0
            TL(7)
            bar();
#define GMSTAGE() { f4 fs; _Pragma("unroll") for (int r = 0; r < 4; ++r) fs[r] = xs[r] + dpp_xor8(xs[r]); \
                v2u gm; gm.x = pk2(fs[0], fs[1]); gm.y = pk2(fs[2], fs[3]); \
                if (rho < 8) *(v2u*)(lds + L_GM + (w >> 1) * 512 + g * 128 + rho * 16 + t * 8) = gm; }
            if (!is_v) { GMSTAGE() bar(); continue; }
            if (last) { GMSTAGE() bar(); bar(); continue; }
#define E_LOADS(sb_) { stt = __builtin_amdgcn_raw_buffer_load_b32(rs, ost, sb_, 16); ef[0] = ld_sc1(rs, oe[0], sb_); ef[1] = ld_sc1(rs, oe[1], sb_); SB0 }
#define P_LOADS(sb_) { pf[0][0] = ld_sc1(rs, op[0][0], sb_); SB0 pf[1][0] = ld_sc1(rs, op[1][0], sb_); SB0 pf[0][1] = ld_sc1(rs, op[0][1], sb_); SB0 pf[1][1] = ld_sc1(rs, op[1][1], sb_); SB0 }
            const unsigned tc2 = (unsigned)__builtin_amdgcn_readfirstlane(ckr.y) + (unsigned)(((int)(__builtin_amdgcn_readfirstlane(cwx) - __builtin_amdgcn_readfirstlane(ckr.x)) * (int)__builtin_amdgcn_readfirstlane(ckr.z)) >> 8);
            spin_to(tc2 - TE * 23u);
            E_LOADS(sbr)
            asm volatile("" ::: "memory");
            spin_to(tc2);
            TL(3)
            P_LOADS(sbr)
            asm volatile("" ::: "memory");
            SB0
            if (w == 1) BT(7)
            GMSTAGE()
            v4u uf[2];
            uf[0] = *(const v4u*)(lds + L_UF + (step & 3) * 8192 + w * 2048 + lane * 16); uf[1] = *(const v4u*)(lds + L_UF + (step & 3) * 8192 + w * 2048 + 1024 + lane * 16);
            bar();
            v2u vpA, vpB;
            f4 sini = {0, 0, 0, 0};
            {
                v2u vp = *(const v2u*)(lds + L_VP + w * 512 + (2 * (g >> 1) + hp) * 128 + i8 * 16 + (g & 1) * 8);
                v2u ow = *(const v2u*)(lds + L_FL + 640 + i8 * 16 + (g & 1) * 8);
                vpA = g < 2 ? vp : v2u{0, 0};
                vpB = g < 2 ? v2u{0, 0} : vp;
                if (ownhead) { sini[0] = bflo(ow.x); sini[1] = bfhi(ow.x); sini[2] = bflo(ow.y); sini[3] = bfhi(ow.y); }
            }
            asm volatile("" ::: "memory");
            SB0
            TL(4)
            bool okall;
            auto combine = [&]() __attribute__((always_inline)) {
                f4 ls = mfma(oneLo, ef[0], f4{0, 0, 0, 0});
                ls = mfma(oneHi, ef[1], ls);
                f4 x0 = mfma(uf[0], ef[0], f4{0, 0, 0, 0});
                f4 x1 = mfma(uf[1], ef[1], f4{0, 0, 0, 0});
                unsigned t2 = vmax4(vmax4(stt, ef[0]), ef[1]);
                float lsv = ls[0];
                asm volatile("" : "+v"(t2), "+v"(lsv));
                SB0
                f4 sacc = mfma(selLo, pf[0][0], sini);
                sacc = mfma(selHi, pf[1][0], sacc);
                sacc = mfma(selLo, pf[0][1], sacc);
                sacc = mfma(selHi, pf[1][1], sacc);
                SB0
                unsigned t0 = vmax4(vmax4(t2, pf[0][0]), pf[0][1]), t1 = vmax4(vmax4(0, pf[1][0]), pf[1][1]);
                asm volatile("" : "+v"(t0), "+v"(t1));
                SB0
                const float mloc = asf(stt);
                float mx = vmax3(vmax3_mfma(sacc[0], sacc[1], sacc[2]), sacc[3], mloc);
                mx = rmax16(mx);
                float p0 = __builtin_amdgcn_exp2f(sacc[0] - mx), p1 = __builtin_amdgcn_exp2f(sacc[1] - mx), p2 = __builtin_amdgcn_exp2f(sacc[2] - mx), p3 = __builtin_amdgcn_exp2f(sacc[3] - mx);
                float f = __builtin_amdgcn_exp2f(mloc - mx);
                v2u pb; pb.x = pk2(p0, p1); pb.y = pk2(p2, p3);
                float zq = (p0 + p1) + (p2 + p3);
                SB0
                f4 cmA = mfma16(vpA, pb, f4{0, 0, 0, 0});
                f4 cmB = mfma16(vpB, pb, f4{0, 0, 0, 0});
                SB0
                float z = xsum16(zq);
                auto fs = __builtin_amdgcn_permlane32_swap(asu(f), asu(f), false, false);
                unsigned fa = fs[0], fb = fs[1];
                z += f * lsv;
                float rz = __builtin_amdgcn_rcpf(z);
                okall = __all(max(t0, t1) != SENT);
                f4 ya = x0 * asf(fa) + cmA, yb = x1 * asf(fb) + cmB;
                auto rs2 = __builtin_amdgcn_permlane32_swap(asu(rz), asu(rz), false, false);
                unsigned ra = rs2[0], rb = rs2[1];
                f4 cx = ya * asf(ra) + yb * asf(rb);
                if (hp == (g >> 1)) {
                    float* rq = red2 + i8 * 100 + (4 * (g & 1)) * 12 + 2 * w + hp;
                    rq[0] = cx[0]; rq[12] = cx[1]; rq[24] = cx[2]; rq[36] = cx[3];
                }
            };
            combine();
            if (!okall) {
                nfail2++;
                for (unsigned spin = 0; spin < SPIN_MAX && !okall; ++spin) {
                    __builtin_amdgcn_s_sleep(1);
                    asm volatile("" ::: "memory");
                    E_LOADS(sbr)
                    P_LOADS(sbr)
                    combine();
                }
            }
            TL(6)
            bar();
        }
        bar();
        __builtin_amdgcn_s_barrier();
    }
}

DI void signal_add(unsigned char* ws, unsigned off) {
    asm volatile("s_waitcnt vmcnt(0)" ::: "memory");
    __syncthreads();
    if (threadIdx.x == 0) __hip_atomic_fetch_add((unsigned*)(ws + CTRL_OFF + off), 1u, __ATOMIC_RELAXED, __HIP_MEMORY_SCOPE_AGENT);
}
DI void wg_wait(__amdgpu_buffer_rsrc_t rs, unsigned off_lane, unsigned need, int slp) {
    if (threadIdx.x < 64) wait_flags(rs, off_lane, need, slp);
    __syncthreads();
}

DI void gldsx16(const unsigned char* gsrc, unsigned lds_dst) {
    unsigned keep;
    asm volatile("s_mov_b32 %0, m0\n\ts_mov_b32 m0, %2\n\ts_nop 0\n\tglobal_load_lds_dwordx4 %1, off\n\ts_mov_b32 m0, %0" : "=&s"(keep) : "v"(gsrc), "s"(lds_dst) : "memory");
}
DI void gu_role(unsigned char* ws, unsigned char* lds, const int gw) {
    const int tid = threadIdx.x, lane = tid & 63, w8 = __builtin_amdgcn_readfirstlane(tid >> 6);
    const int w = w8 & 3, sp = w8 >> 2;
    const bool isG = gw < 56;
    const unsigned ldsb = __builtin_amdgcn_readfirstlane((unsigned)(size_t)(__attribute__((address_space(3))) unsigned char*)lds);
    const int nat0 = (((lane & 15) >> 2) & 1) * 14464 + ((4 * ((lane & 15) >> 3) + (lane & 3)) + 16 * (lane >> 4)) * 16;
    const unsigned char* xsrc = ws + XF_OFF + w8 * 1024 + lane * 16;
#define XDMA(pr_) { _Pragma("unroll") for (int i = 0; i < 7; ++i) gldsx16(xsrc + (size_t)(pr_) * 57344 + i * 8192, ldsb + ((pr_) & 1) * 57856 + (i * 8 + w8) * 1024 + ((i * 8 + w8) / 14) * 128); }
    XDMA(0)
    v4u wt[2][14];
    const unsigned char* wsrc = isG ? ws + AS_OFF + (size_t)(4 * gw + w) * 28 * 1024 : ws + BS_OFF + ((size_t)(gw - 56) * 8 + 2 * w) * 14 * 1024;
    unsigned char* xch = lds + 57856;
    v4u tmp[14];
#pragma unroll
    for (int ks = 0; ks < 14; ++ks) tmp[ks] = *(const v4u*)(wsrc + (sp * 14 + ks) * 1024 + lane * 16);
    SB0
#pragma unroll
    for (int half = 0; half < 2; ++half) {
        if (half) bar();
#pragma unroll
        for (int ks = 0; ks < 7; ++ks) *(v4u*)(xch + (w8 * 7 + ks) * 1024 + lane * 16) = tmp[half * 7 + ks];
        bar();
#pragma unroll
        for (int t = 0; t < 2; ++t)
#pragma unroll
            for (int ks = 0; ks < 7; ++ks) wt[t][half * 7 + ks] = *(const v4u*)(xch + ((w + 4 * t) * 7 + ks) * 1024 + lane * 16);
    }
    __amdgpu_buffer_rsrc_t rs = mkrs(ws, 0x7fffffff);
    asm volatile("s_waitcnt vmcnt(0)" ::: "memory");
    bar();
    const int bb = 4 * gw + w, hh = bb / 14, bq = bb % 14;
    const unsigned obase = isG ? (unsigned)(hh * 4 + (bq >> 3)) * 16384u + (bq & 7) * 1024 + lane * 16 : (unsigned)(64 + gw - 56) * 16384u + 2 * w * 1024 + lane * 16;
    const unsigned ostep = isG ? 32768u : 1024u;
    unsigned polo = 0;
    for (int pr = 0; pr < 32; ++pr) {
        const unsigned char* cur = lds + (pr & 1) * 57856 + sp * 28928;
        if (pr + 1 < 32) XDMA(pr + 1)
        f4 acc[2][2] = {{f4{0,0,0,0}, f4{0,0,0,0}}, {f4{0,0,0,0}, f4{0,0,0,0}}};
        {
            const unsigned char* p0 = cur + (isG ? nat0 : lane * 16);
            const unsigned char* p1 = p0 + (isG ? 128 : 14464);
            v4u xa[3], xb[3];
#pragma unroll
            for (int k = 0; k < 3; ++k) { xa[k] = *(const v4u*)(p0 + k * 1024); xb[k] = *(const v4u*)(p1 + k * 1024); }
            if (isG) {
#pragma unroll
                for (int ks = 0; ks < 14; ++ks) {
                    const v4u x0 = xa[ks % 3], x1 = xb[ks % 3];
                    acc[0][0] = mfma(wt[0][ks], x0, acc[0][0]); acc[1][0] = mfma(wt[1][ks], x0, acc[1][0]);
                    acc[0][1] = mfma(wt[0][ks], x1, acc[0][1]); acc[1][1] = mfma(wt[1][ks], x1, acc[1][1]);
                    if (ks + 3 < 14) { xa[ks % 3] = *(const v4u*)(p0 + (ks + 3) * 1024); xb[ks % 3] = *(const v4u*)(p1 + (ks + 3) * 1024); }
                    SB0
                }
            } else {
#pragma unroll
                for (int ks = 0; ks < 14; ++ks) {
                    const v4u x0 = xa[ks % 3], x1 = xb[ks % 3];
                    acc[0][0] = mfma(x0, wt[0][ks], acc[0][0]); acc[1][0] = mfma(x1, wt[0][ks], acc[1][0]);
                    acc[0][1] = mfma(x0, wt[1][ks], acc[0][1]); acc[1][1] = mfma(x1, wt[1][ks], acc[1][1]);
                    if (ks + 3 < 14) { xa[ks % 3] = *(const v4u*)(p0 + (ks + 3) * 1024); xb[ks % 3] = *(const v4u*)(p1 + (ks + 3) * 1024); }
                    SB0
                }
            }
        }
        if (pr == 0) polo = (wait_flags(rs, C_POL, 1, 1) - 1u) * 8192u;
        const unsigned so = segb(2 * pr + sp, polo);
        st_sc1(pack8(acc[0][0], acc[1][0]), rs, obase, so);
        st_sc1(pack8(acc[0][1], acc[1][1]), rs, obase + ostep, so);
        const bool drain = (pr < 4) || (pr == 31);
        if (drain) asm volatile("s_waitcnt vmcnt(0)" ::: "memory");
        else asm volatile("s_waitcnt vmcnt(2)" ::: "memory");
        bar();
        if (tid == 448) __builtin_amdgcn_raw_buffer_store_b32((unsigned)(drain ? 2 * pr + 2 : 2 * pr), rs, (unsigned)CTRL_OFF + C_GU + 128u * gw, 0, 16);
    }
}

DI void a0_prep(unsigned char* ws, const int u, const u16* __restrict__ wqkv, const u16* __restrict__ wphi, const u16* __restrict__ awo) {
    const int tid = threadIdx.x, lane = tid & 63, w = __builtin_amdgcn_readfirstlane(tid >> 6);
    const int rho = lane & 15, g = lane >> 4;
    __amdgpu_buffer_rsrc_t rs = mkrs(ws, 0x7fffffff);
    {
        const int which = w >> 2, h2 = u;
        v4u bf[2];
#pragma unroll
        for (int t = 0; t < 2; ++t) {
            const int f = perm32(t, rho);
            unsigned e[8];
#pragma unroll
            for (int jj = 0; jj < 8; ++jj) { const int d = 8 * g + jj; e[jj] = (d < 28 && f < 28) ? wphi[(h2 * 28 + d) * 28 + f] : 0u; }
            bf[t] = v4u{e[0] | (e[1] << 16), e[2] | (e[3] << 16), e[4] | (e[5] << 16), e[6] | (e[7] << 16)};
        }
        v4u af[4][2];
#pragma unroll
        for (int i = 0; i < 4; ++i) {
            const int ks = min((w & 3) + 4 * i, 13);
#pragma unroll
            for (int t = 0; t < 2; ++t) {
                const u16* p = wqkv + (size_t)(32 * ks + perm32(t, rho)) * 1344 + which * 448 + h2 * 28 + g * 8;
                v2u lo = *(const v2u*)p, hi = {0, 0};
                if (g < 3) hi = *(const v2u*)(p + 4);
                af[i][t] = v4u{lo.x, lo.y, hi.x, hi.y};
            }
        }
#pragma unroll
        for (int i = 0; i < 4; ++i) {
            const int ks = (w & 3) + 4 * i;
            if (ks < 14) {
#pragma unroll
                for (int t = 0; t < 2; ++t) {
                    f4 d0 = mfma(af[i][0], bf[t], f4{0, 0, 0, 0}), d1 = mfma(af[i][1], bf[t], f4{0, 0, 0, 0});
                    st_sc1(pack8(d0 * LOG2E, d1 * LOG2E), rs, (unsigned)(WQF_OFF + which * WQF_ONE + ((size_t)(h2 * 2 + t) * 14 + ks) * 1024) + lane * 16, 0);
                }
            }
        }
    }
#pragma unroll
    for (int i = 0; i < 4; ++i) {
        const int fi = w + 8 * i;
        if (fi < 28) {
            const int t = fi / 14, ks = fi % 14;
            const int d = 16 * t + rho;
            unsigned e[8], e2[8];
            const int cb = fi >> 1, t2 = fi & 1; const int c = 32 * cb + perm32(t2, rho);
#pragma unroll
            for (int jj = 0; jj < 8; ++jj) {
                e[jj] = d < 28 ? wqkv[(size_t)(32 * ks + 8 * g + jj) * 1344 + 896 + u * 28 + d] : 0u;
                const int sl = 8 * g + jj; e2[jj] = sl < 28 ? awo[(size_t)(u * 28 + sl) * 448 + c] : 0u;
            }
            st_sc1(v4u{e[0] | (e[1] << 16), e[2] | (e[3] << 16), e[4] | (e[5] << 16), e[6] | (e[7] << 16)}, rs, (unsigned)(WQF_OFF + 2 * WQF_ONE + (size_t)(u * 28 + fi) * 1024) + lane * 16, 0);
            st_sc1(v4u{e2[0] | (e2[1] << 16), e2[2] | (e2[3] << 16), e2[4] | (e2[5] << 16), e2[6] | (e2[7] << 16)}, rs, (unsigned)(WOF_OFF + (size_t)((cb * 2 + t2) * 16 + u) * 1024) + lane * 16, 0);
        }
    }
}
DI void phi_pair(f4 d0, f4 d1, int g, v4u& pos, v4u& neg) {
    float v[8] = {d0[0], d0[1], d0[2], d0[3], d1[0], d1[1], d1[2], d1[3]};
    const bool tail = (g == 3);
    float mx = fmaxf(fmaxf(v[0], v[1]), fmaxf(v[2], v[3])), mn = fminf(fminf(v[0], v[1]), fminf(v[2], v[3]));
    float mx2 = fmaxf(fmaxf(v[4], v[5]), fmaxf(v[6], v[7])), mn2 = fminf(fminf(v[4], v[5]), fminf(v[6], v[7]));
    mx = tail ? mx : fmaxf(mx, mx2); mn = tail ? mn : fminf(mn, mn2);
    mx = xmax32(xmax16(mx)); mn = -xmax32(xmax16(-mn));
    float ep[8], en[8], sp = 0.f, sn = 0.f;
#pragma unroll
    for (int k = 0; k < 8; ++k) {
        ep[k] = __builtin_amdgcn_exp2f(v[k] - mx); en[k] = __builtin_amdgcn_exp2f(mn - v[k]);
        if (k >= 4) { ep[k] = tail ? 0.f : ep[k]; en[k] = tail ? 0.f : en[k]; }
        sp += ep[k]; sn += en[k];
    }
    sp = xsum32(xsum16(sp)); sn = xsum32(xsum16(sn));
    const float rp = __builtin_amdgcn_rcpf(sp), rn = __builtin_amdgcn_rcpf(sn);
    pos = v4u{pk2(ep[0] * rp, ep[1] * rp), pk2(ep[2] * rp, ep[3] * rp), pk2(ep[4] * rp, ep[5] * rp), pk2(ep[6] * rp, ep[7] * rp)};
    neg = v4u{pk2(en[0] * rn, en[1] * rn), pk2(en[2] * rn, en[3] * rn), pk2(en[4] * rn, en[5] * rn), pk2(en[6] * rn, en[7] * rn)};
}
DI void stage_x64(const u16* __restrict__ ax, unsigned char* lds, int blk64) {
    int tid = threadIdx.x; asm volatile("" : "+v"(tid));
    const int lane = tid & 63, w = tid >> 6;
    v4u stg[7];
#pragma unroll
    for (int i = 0; i < 7; ++i) {
        const int f = i * 8 + w; const int q = f / 14, ks = f % 14;
        stg[i] = *(const v4u*)(ax + (size_t)(blk64 * 64 + 32 * (q >> 1) + perm32(q & 1, lane & 15)) * D + ks * 32 + (lane >> 4) * 8);
    }
#pragma unroll
    for (int i = 0; i < 7; ++i) *(v4u*)(lds + (i * 512 + tid) * 16) = stg[i];
}

DI void a1_unit(unsigned char* ws, unsigned char* lds, const int u, const u16* __restrict__ ax) {
    const int tid = threadIdx.x, lane = tid & 63, w = __builtin_amdgcn_readfirstlane(tid >> 6);
    const int rho = lane & 15, g = lane >> 4;
    __amdgpu_buffer_rsrc_t rs = mkrs(ws, 0x7fffffff);
    unsigned char* T = lds + 57344 + w * 9216;
    constexpr int TS = 144;
    f4 kv[2][4][2];
#pragma unroll
    for (int a = 0; a < 2; ++a)
#pragma unroll
        for (int b = 0; b < 4; ++b) { kv[a][b][0] = f4{0,0,0,0}; kv[a][b][1] = f4{0,0,0,0}; }
    for (int pass = 0; pass < 2; ++pass) {
        __syncthreads();
        stage_x64(ax, lds, 2 * u + pass);
        __syncthreads();
#pragma unroll
        for (int hi = 0; hi < 2; ++hi) {
            const int h = 2 * w + hi;
            const unsigned wk = (unsigned)(WQF_OFF + WQF_ONE + (size_t)h * 28672) + lane * 16;
            const unsigned wv = (unsigned)(WQF_OFF + 2 * WQF_ONE + (size_t)h * 28672) + lane * 16;
            f4 ak[2][4], av[2][4];
#pragma unroll
            for (int t = 0; t < 2; ++t)
#pragma unroll
                for (int q = 0; q < 4; ++q) { ak[t][q] = f4{0,0,0,0}; av[t][q] = f4{0,0,0,0}; }
#pragma unroll 2
            for (int ks = 0; ks < 14; ++ks) {
                v4u k0 = ld_sc1(rs, wk + ks * 1024, 0), k1 = ld_sc1(rs, wk + (14 + ks) * 1024, 0);
                v4u v0 = ld_sc1(rs, wv + ks * 1024, 0), v1 = ld_sc1(rs, wv + (14 + ks) * 1024, 0);
#pragma unroll
                for (int q = 0; q < 4; ++q) {
                    v4u x = *(const v4u*)(lds + (q * 14 + ks) * 1024 + lane * 16);
                    ak[0][q] = mfma(k0, x, ak[0][q]); ak[1][q] = mfma(k1, x, ak[1][q]);
                    av[0][q] = mfma(x, v0, av[0][q]); av[1][q] = mfma(x, v1, av[1][q]);
                }
            }
#pragma unroll
            for (int q = 0; q < 4; ++q) {
                v4u pos, neg; phi_pair(ak[0][q], ak[1][q], g, pos, neg);
                const int tok = 32 * (q >> 1) + perm32(q & 1, rho);
                *(v4u*)(T + tok * TS + g * 16) = pos;
                *(v4u*)(T + tok * TS + 64 + g * 16) = neg;
            }
            asm volatile("s_waitcnt lgkmcnt(0)" ::: "memory");
            v4u vf[2][2];
#pragma unroll
            for (int bk = 0; bk < 2; ++bk)
#pragma unroll
                for (int t = 0; t < 2; ++t) {
                    vf[bk][t] = pack8(av[t][2 * bk], av[t][2 * bk + 1]);
                    if (t == 1 && rho == 12) vf[bk][t] = v4u{0x3F803F80u, 0x3F803F80u, 0x3F803F80u, 0x3F803F80u};
                }
            const unsigned trb = (8 * g + (rho >> 2)) * TS + (lane & 3) * 8;
#pragma unroll
            for (int bk = 0; bk < 2; ++bk)
#pragma unroll
                for (int ft = 0; ft < 4; ++ft) {
                    v2u lo = trread(T + trb + (32 * bk) * TS + ft * 32), hi2 = trread(T + trb + (32 * bk + 4) * TS + ft * 32);
                    v4u pa = {lo.x, lo.y, hi2.x, hi2.y};
                    kv[hi][ft][0] = mfma(pa, vf[bk][0], kv[hi][ft][0]);
                    kv[hi][ft][1] = mfma(pa, vf[bk][1], kv[hi][ft][1]);
                }
            asm volatile("s_waitcnt lgkmcnt(0)" ::: "memory");
        }
    }
#pragma unroll
    for (int hi = 0; hi < 2; ++hi)
#pragma unroll
        for (int ft = 0; ft < 4; ++ft)
#pragma unroll
            for (int dt = 0; dt < 2; ++dt)
                st_sc1(__builtin_bit_cast(v4u, kv[hi][ft][dt]), rs, (unsigned)(KVP_OFF + ((size_t)(u * 16 + 2 * w + hi) * 8 + ft * 2 + dt) * 1024) + lane * 16, 0);
}
DI void a15_unit(unsigned char* ws, unsigned char* lds, const int h) {
    const int tid = threadIdx.x, lane = tid & 63, w = tid >> 6;
    __amdgpu_buffer_rsrc_t rs = mkrs(ws, 0x7fffffff);
    f4 s = {0, 0, 0, 0};
    v4u t[16];
#pragma unroll
    for (int u = 0; u < 16; ++u) t[u] = ld_sc1(rs, (unsigned)(KVP_OFF + ((size_t)(u * 16 + h) * 8) * 1024) + tid * 16, 0);
#pragma unroll
    for (int u = 0; u < 16; ++u) s += __builtin_bit_cast(f4, t[u]);
    const int ft = w >> 1, dt = w & 1, rho = lane & 15, g = lane >> 4;
    __syncthreads();
    v2u o; o.x = pk2(s[0], s[1]); o.y = pk2(s[2], s[3]);
    *(v2u*)(lds + (16 * dt + rho) * 128 + (16 * ft + 4 * g) * 2) = o;
    __syncthreads();
    if (tid < 256) {
        const int fr = tid >> 6;
        const int tt = fr >> 1, kb = fr & 1;
        v4u v = *(const v4u*)(lds + perm32(tt, rho) * 128 + (32 * kb + 8 * g) * 2);
        st_sc1(v, rs, (unsigned)(KVF_OFF + (size_t)(h * 4 + fr) * 1024) + lane * 16, 0);
    }
}
DI void a2_unit(unsigned char* ws, unsigned char* lds, const int u, u16* __restrict__ out, const u16* __restrict__ ax) {
    int tid = threadIdx.x; asm volatile("" : "+v"(tid));
    const int lane = tid & 63, w = __builtin_amdgcn_readfirstlane(tid >> 6);
    const int rho = lane & 15, g = lane >> 4;
    __amdgpu_buffer_rsrc_t rs = mkrs(ws, 0x7fffffff);
    unsigned char* O = lds + 57344;
    __syncthreads();
    stage_x64(ax, lds, u);
    wg_wait(rs, C_A0, 16, 4);
    v4u pos[2][4], neg[2][4];
#pragma unroll
    for (int hi = 0; hi < 2; ++hi) {
        const int h = 2 * w + hi;
        const unsigned wq = (unsigned)(WQF_OFF + (size_t)h * 28672) + lane * 16;
        f4 aq[2][4];
#pragma unroll
        for (int t = 0; t < 2; ++t)
#pragma unroll
            for (int q = 0; q < 4; ++q) aq[t][q] = f4{0,0,0,0};
#pragma unroll 2
        for (int ks = 0; ks < 14; ++ks) {
            v4u k0 = ld_sc1(rs, wq + ks * 1024, 0), k1 = ld_sc1(rs, wq + (14 + ks) * 1024, 0);
#pragma unroll
            for (int q = 0; q < 4; ++q) {
                v4u x = *(const v4u*)(lds + (q * 14 + ks) * 1024 + lane * 16);
                aq[0][q] = mfma(k0, x, aq[0][q]); aq[1][q] = mfma(k1, x, aq[1][q]);
            }
        }
#pragma unroll
        for (int q = 0; q < 4; ++q) phi_pair(aq[0][q], aq[1][q], g, pos[hi][q], neg[hi][q]);
    }
    wg_wait(rs, C_KV, 16, 4);
#pragma unroll
    for (int hi = 0; hi < 2; ++hi) {
        const int h = 2 * w + hi;
        v4u kf[4];
#pragma unroll
        for (int fr = 0; fr < 4; ++fr) kf[fr] = ld_sc1(rs, (unsigned)(KVF_OFF + (size_t)(h * 4 + fr) * 1024) + lane * 16, 0);
#pragma unroll
        for (int q = 0; q < 4; ++q) {
            f4 n0 = mfma(kf[0], pos[hi][q], f4{0,0,0,0}); n0 = mfma(kf[1], neg[hi][q], n0);
            f4 n1 = mfma(kf[2], pos[hi][q], f4{0,0,0,0}); n1 = mfma(kf[3], neg[hi][q], n1);
            const float den = asf((unsigned)__builtin_amdgcn_ds_bpermute((48 + rho) * 4, (int)asu(n1[0])));
            const float rd = 1.0f / (den + 1e-6f);
            if (g == 3) n1 = f4{0, 0, 0, 0};
            *(v4u*)(O + (h * 4 + q) * 1024 + lane * 16) = pack8(n0 * rd, n1 * rd);
        }
    }
    __syncthreads();
    for (int cb = w; cb < 14; cb += 8) {
        f4 acc[2][4];
#pragma unroll
        for (int t = 0; t < 2; ++t)
#pragma unroll
            for (int q = 0; q < 4; ++q) acc[t][q] = f4{0,0,0,0};
        const unsigned wo = (unsigned)(WOF_OFF + (size_t)cb * 32768) + lane * 16;
#pragma unroll 4
        for (int h = 0; h < 16; ++h) {
            v4u w0 = ld_sc1(rs, wo + h * 1024, 0), w1 = ld_sc1(rs, wo + (16 + h) * 1024, 0);
#pragma unroll
            for (int q = 0; q < 4; ++q) {
                v4u o = *(const v4u*)(O + (h * 4 + q) * 1024 + lane * 16);
                acc[0][q] = mfma(w0, o, acc[0][q]); acc[1][q] = mfma(w1, o, acc[1][q]);
            }
        }
#pragma unroll
        for (int q = 0; q < 4; ++q) {
            const int tok = u * 64 + 32 * (q >> 1) + perm32(q & 1, rho);
            *(v4u*)(out + (size_t)tok * OUTW + 32 * cb + 8 * g) = pack8(acc[0][q], acc[1][q]);
        }
    }
}

DI void p2_unit(unsigned char* ws, unsigned char* lds, const int unit, u16* __restrict__ out) {
    const int seg = unit >> 1, chalf = unit & 1;
    int tid = threadIdx.x; asm volatile("" : "+v"(tid));
    const int lane = tid & 63, w = __builtin_amdgcn_readfirstlane(tid >> 6);
    const int rho = lane & 15, g = lane >> 4;
    __amdgpu_buffer_rsrc_t rs = mkrs(ws, 0x7fffffff);
    unsigned char* xl = lds + 40960;
    __syncthreads();
    for (int i = tid; i < 1792; i += 512) *(v4u*)(xl + i * 16) = *(const v4u*)(ws + XF_OFF + (size_t)seg * 28672 + i * 16);
    if (tid < 64 && seg > 8) wait_flags(rs, C_REC, seg - 8, 16);
    if (tid < 128) wait_flags(rs, C_GU + 128u * (unsigned)min(tid, 111), seg + 1, 8);
    __syncthreads();
    const unsigned sgb = segb(seg, (__builtin_amdgcn_readfirstlane(ldflag(rs, C_POL)) - 1u) * 8192u);
    f4 sx[2][2][2];
#pragma unroll
    for (int hi = 0; hi < 2; ++hi) {
#pragma unroll
        for (int a = 0; a < 2; ++a) { sx[hi][a][0] = f4{0,0,0,0}; sx[hi][a][1] = f4{0,0,0,0}; }
        const unsigned gb = sgb + (unsigned)(2 * w + hi) * 65536u + lane * 16;
#pragma unroll
        for (int ks = 0; ks < 14; ++ks) {
            v4u g0 = ld_sc1(rs, gb + (ks >> 3) * 16384 + (ks & 7) * 1024, 0), g1 = ld_sc1(rs, gb + 32768 + (ks >> 3) * 16384 + (ks & 7) * 1024, 0);
            v4u x0 = *(const v4u*)(xl + ks * 1024 + lane * 16), x1 = *(const v4u*)(xl + (14 + ks) * 1024 + lane * 16);
            sx[hi][0][0] = mfma(g0, x0, sx[hi][0][0]); sx[hi][0][1] = mfma(g0, x1, sx[hi][0][1]);
            sx[hi][1][0] = mfma(g1, x0, sx[hi][1][0]); sx[hi][1][1] = mfma(g1, x1, sx[hi][1][1]);
        }
    }
    f4* sxl = (f4*)(lds + 69632) + tid;
#pragma unroll
    for (int hi = 0; hi < 2; ++hi)
#pragma unroll
        for (int a = 0; a < 2; ++a)
#pragma unroll
            for (int b2 = 0; b2 < 2; ++b2) sxl[((hi * 2 + a) * 2 + b2) * 512] = sx[hi][a][b2];
    SB0
    const int cb = 7 * chalf + (w < 7 ? w : 6);
    v4u uf[2][16];
    unsigned uo[2], vo[2];
#pragma unroll
    for (int t = 0; t < 2; ++t) {
        const int c = 32 * cb + perm32(t, rho); const int j = c >> 3, c8 = c & 7;
        uo[t] = sgb + (unsigned)(64 + j) * 16384u + (c8 + 16 * g) * 16;
        vo[t] = (unsigned)(VP_OFF + (size_t)(seg * 56 + j) * 2048) + g * 128 + c8 * 16;
#pragma unroll
        for (int h = 0; h < 16; ++h) uf[t][h] = ld_sc1(rs, uo[t] + (h >> 1) * 1024 + (h & 1) * 128, 0);
    }
    if (seg < 61) { if (tid < 64) wait_flags(rs, C_REC, seg + 1, 8); }
    else if (tid < 128) { wait_flags(rs, C_REC, 59, 8); wait_flags(rs, C_DN2 + 4u * (unsigned)min(tid, 111), 1, 1); }
    __syncthreads();
    f4 sm[2] = {f4{0,0,0,0}, f4{0,0,0,0}};
    v4u vf[2][4];
    {
        const unsigned gmb = (unsigned)(GM_OFF + (size_t)(seg * 16 + 2 * w + (rho >> 3)) * 7168) + g * 128 + (rho & 7) * 16;
        v4u gm[14];
#pragma unroll
        for (int ks = 0; ks < 14; ++ks) gm[ks] = ld_sc1(rs, gmb + ks * 512, 0);
#pragma unroll
        for (int t = 0; t < 2; ++t)
#pragma unroll
            for (int w4 = 0; w4 < 4; ++w4) vf[t][w4] = ld_sc1(rs, vo[t] + w4 * 512, 0);
        SB0
#pragma unroll
        for (int ks = 0; ks < 14; ++ks) {
            v4u x0 = *(const v4u*)(xl + ks * 1024 + lane * 16), x1 = *(const v4u*)(xl + (14 + ks) * 1024 + lane * 16);
            sm[0] = mfma(gm[ks], x0, sm[0]); sm[1] = mfma(gm[ks], x1, sm[1]);
            SB0
        }
    }
#pragma unroll
    for (int hi = 0; hi < 2; ++hi) {
        const int h = 2 * w + hi;
        const bool own = ((g >> 1) == hi);
#pragma unroll
        for (int tp = 0; tp < 2; ++tp) {
            const int tq = perm32(tp, rho);
            float v[8];
#pragma unroll
            for (int tau = 0; tau < 2; ++tau) {
                const f4 sv = sxl[((hi * 2 + tau) * 2 + tp) * 512];
#pragma unroll
                for (int r = 0; r < 4; ++r) { const int key = 16 * tau + 4 * g + r; v[4 * tau + r] = (key <= tq) ? sv[r] : -3.0e38f; }
            }
            float mx = fmaxf(fmaxf(fmaxf(v[0], v[1]), fmaxf(v[2], v[3])), fmaxf(fmaxf(v[4], v[5]), fmaxf(v[6], v[7])));
            const float mo = fmaxf(fmaxf(sm[tp][0], sm[tp][1]), fmaxf(sm[tp][2], sm[tp][3]));
            mx = own ? fmaxf(mx, mo) : mx;
            mx = xmax32(xmax16(mx));
            float sum = 0.f, em[4];
#pragma unroll
            for (int k = 0; k < 8; ++k) { v[k] = __builtin_amdgcn_exp2f(v[k] - mx); sum += v[k]; }
#pragma unroll
            for (int r = 0; r < 4; ++r) { em[r] = own ? __builtin_amdgcn_exp2f(sm[tp][r] - mx) : 0.f; sum += em[r]; }
            sum = xsum32(xsum16(sum));
            const float rz = __builtin_amdgcn_rcpf(sum);
            v2u p0; p0.x = pk2(v[0] * rz, v[1] * rz); p0.y = pk2(v[2] * rz, v[3] * rz);
            v2u p1; p1.x = pk2(v[4] * rz, v[5] * rz); p1.y = pk2(v[6] * rz, v[7] * rz);
            *(v2u*)(lds + (h * 2 + tp) * 1024 + (rho + 16 * (g >> 1)) * 16 + (g & 1) * 8) = p0;
            *(v2u*)(lds + (h * 2 + tp) * 1024 + (rho + 16 * (2 + (g >> 1))) * 16 + (g & 1) * 8) = p1;
            if (own) {
                v2u pm; pm.x = pk2(em[0] * rz, em[1] * rz); pm.y = pk2(em[2] * rz, em[3] * rz);
                *(v2u*)(lds + ((16 + (h >> 2)) * 2 + tp) * 1024 + (rho + 16 * (h & 3)) * 16 + (g & 1) * 8) = pm;
            }
        }
    }
    __syncthreads();
    if (w < 7) {
        f4 acc[2][2] = {{f4{0,0,0,0}, f4{0,0,0,0}}, {f4{0,0,0,0}, f4{0,0,0,0}}};
#pragma unroll
        for (int h = 0; h < 16; ++h) {
            v4u p0 = *(const v4u*)(lds + (h * 2) * 1024 + lane * 16), p1 = *(const v4u*)(lds + (h * 2 + 1) * 1024 + lane * 16);
#pragma unroll
            for (int t = 0; t < 2; ++t) { acc[t][0] = mfma(uf[t][h], p0, acc[t][0]); acc[t][1] = mfma(uf[t][h], p1, acc[t][1]); }
        }
#pragma unroll
        for (int w4 = 0; w4 < 4; ++w4) {
            v4u p0 = *(const v4u*)(lds + ((16 + w4) * 2) * 1024 + lane * 16), p1 = *(const v4u*)(lds + ((16 + w4) * 2 + 1) * 1024 + lane * 16);
#pragma unroll
            for (int t = 0; t < 2; ++t) { acc[t][0] = mfma(vf[t][w4], p0, acc[t][0]); acc[t][1] = mfma(vf[t][w4], p1, acc[t][1]); }
        }
#pragma unroll
        for (int tp = 0; tp < 2; ++tp) {
            const int tok = seg * 32 + perm32(tp, rho);
            *(v4u*)(out + (size_t)tok * OUTW + 448 + 32 * cb + 8 * g) = pack8(acc[0][tp], acc[1][tp]);
        }
    }
}

constexpr int LDS_MAIN = 135168 + 64;
__global__ void __launch_bounds__(512, 2) k_main(unsigned char* ws, u16* __restrict__ out, const u16* __restrict__ ax, const u16* __restrict__ wqkv, const u16* __restrict__ wphi, const u16* __restrict__ awo, int dS, int dV, int dVm, int pad_) {
    extern __shared__ __attribute__((aligned(16))) unsigned char lds[];
    const int bidx = blockIdx.x;
    const int idx = (bidx >> 3) * 4 + (bidx & 3);
#define PT(i)
    if ((bidx & 7) < 4) { rec_role(ws, lds, idx, dS, dV, dVm); return; }
    __amdgpu_buffer_rsrc_t rs = mkrs(ws, 0x7fffffff);
    if (idx < 112) gu_role(ws, lds, idx);
    else {
        const int u = idx - 112;
        a0_prep(ws, u, wqkv, wphi, awo);
        signal_add(ws, C_A0);
        wg_wait(rs, C_A0, 16, 2);
        a1_unit(ws, lds, u, ax);
        signal_add(ws, C_A1);
        wg_wait(rs, C_A1, 16, 4);
        a15_unit(ws, lds, u);
        signal_add(ws, C_KV);
    }
    PT(0)
    for (;;) {
        __syncthreads();
        if (threadIdx.x == 0) LDSV(LDS_MAIN - 16) = __hip_atomic_fetch_add((unsigned*)(ws + CTRL_OFF + C_TICKET), 1u, __ATOMIC_RELAXED, __HIP_MEMORY_SCOPE_AGENT);
        __syncthreads();
        const int t = __builtin_amdgcn_readfirstlane(LDSV(LDS_MAIN - 16));
        if (t >= 160) break;
        if (t < 32) a2_unit(ws, lds, t, out, ax);
        else p2_unit(ws, lds, t - 32, out);
        if (t < 32) { PT(1) } else { PT(2) }
    }
}

#define REC_DS (82 | (50 << 8))
#define REC_DV (75 | (1 << 8))
#define REC_DVM (6 | (16 << 8) | (0 << 16))
constexpr int g_dS = REC_DS, g_dV = REC_DV, g_dVm = REC_DVM;

extern "C" void kernel_launch(void* const* d_in, const int* in_sizes, int n_in, void* d_out, int out_size, void* d_ws, size_t ws_size, hipStream_t stream) {
    const u16* A_x = (const u16*)d_in[0]; const u16* A_wqkv = (const u16*)d_in[1]; const u16* A_wphi = (const u16*)d_in[2]; const u16* A_wo = (const u16*)d_in[3];
    const u16* B_x = (const u16*)d_in[4]; const u16* B_mem0 = (const u16*)d_in[5];
    const u16* Wq = (const u16*)d_in[6]; const u16* Wk = (const u16*)d_in[7]; const u16* Wv = (const u16*)d_in[8]; const u16* Wo = (const u16*)d_in[9];
    u16* out = (u16*)d_out;
    unsigned char* ws = (unsigned char*)(((uintptr_t)d_ws + 2 * MB - 1) & ~(uintptr_t)(2 * MB - 1));
    (void)hipFuncSetAttribute((const void*)k_pre, hipFuncAttributeMaxDynamicSharedMemorySize, 61440);
    (void)hipFuncSetAttribute((const void*)k_main, hipFuncAttributeMaxDynamicSharedMemorySize, LDS_MAIN);
    k_pre<<<256, 448, 61440, stream>>>(ws, Wq, Wk, Wv, Wo, B_x, B_mem0);
    k_main<<<256, 512, LDS_MAIN, stream>>>(ws, out, A_x, A_wqkv, A_wphi, A_wo, g_dS, g_dV, g_dVm, 0);
}
```

```cpp
#include <hip/hip_runtime.h>
#include <hip/hip_bf16.h>
#include <stdint.h>

typedef unsigned short u16;
typedef unsigned v4u __attribute__((ext_vector_type(4)));
typedef unsigned v2u __attribute__((ext_vector_type(2)));
typedef float f4 __attribute__((ext_vector_type(4)));
typedef float f2 __attribute__((ext_vector_type(2)));
typedef short s8 __attribute__((ext_vector_type(8)));
typedef __bf16 bf2 __attribute__((ext_vector_type(2)));
#define DI __device__ __forceinline__

constexpr int D = 448, H = 16, DH = 256, MEM = 8, SEG = 32, NSEG = 64, S_TOK = 2048;
constexpr int NKS = 14;
constexpr int OUTW = 896;
constexpr unsigned SENT = 0xFFFFFFFFu;
constexpr float LOG2E = 1.4426950408889634f;

constexpr size_t MB = 1024 * 1024;
constexpr int KCOPY = 4;
#define FARX 8192u
constexpr size_t COMM_OFF = 0;
constexpr size_t COMM_BYTES = 2 * MB;
constexpr size_t CTRL_OFF = COMM_BYTES;
constexpr size_t AS_OFF = CTRL_OFF + 262144;
constexpr size_t AS_BYTES = (size_t)16 * 14 * 2 * 14 * 1024;
constexpr size_t BS_OFF = AS_OFF + AS_BYTES;
constexpr size_t BS_BYTES = (size_t)56 * 8 * 14 * 1024;
constexpr size_t XF_OFF = BS_OFF + BS_BYTES;
constexpr size_t XF_BYTES = (size_t)64 * 28 * 1024;
constexpr size_t GU_BASE = 40 * MB;
constexpr size_t GM_OFF = XF_OFF + XF_BYTES;
constexpr size_t GM_BYTES = (size_t)64 * 16 * 14 * 512;
constexpr size_t VP_OFF = GM_OFF + GM_BYTES;
constexpr size_t VP_BYTES = (size_t)64 * 56 * 4 * 512;
constexpr size_t AXF_OFF = VP_OFF + VP_BYTES;
constexpr size_t WQF_OFF = AXF_OFF + XF_BYTES;
constexpr size_t WQF_ONE = (size_t)16 * 2 * 14 * 1024;
constexpr size_t WOF_OFF = WQF_OFF + 3 * WQF_ONE;
constexpr size_t KVP_OFF = WOF_OFF + WQF_ONE;
constexpr size_t KVF_OFF = KVP_OFF + (size_t)16 * 16 * 8192;
constexpr size_t WS_END = KVF_OFF + 65536;
static_assert(WS_END + MB <= GU_BASE && GU_BASE % (4 * MB) == 0 && GU_BASE + 128 * MB + 2 * MB <= 256 * MB, "workspace layout");
constexpr unsigned C_POL = 0, C_REC = 128, C_DONE = 256, C_A1 = 384, C_KV = 512, C_TICKET = 640, C_A0 = 768, C_GU = 1024;
constexpr unsigned C_DN2 = 16384;
constexpr unsigned C_DBG = 32768;

DI unsigned pk2(float a, float b) { f2 v = {a, b}; return __builtin_bit_cast(unsigned, __builtin_convertvector(v, bf2)); }
DI float asf(unsigned x) { return __builtin_bit_cast(float, x); }
DI unsigned asu(float x) { return __builtin_bit_cast(unsigned, x); }
DI float bflo(unsigned x) { return __builtin_bit_cast(float, x << 16); }
DI float bfhi(unsigned x) { return __builtin_bit_cast(float, x & 0xffff0000u); }
DI float bf2f(u16 x) { return __builtin_bit_cast(float, (unsigned)x << 16); }
DI f4 mfma(v4u a, v4u b, f4 c) { return __builtin_amdgcn_mfma_f32_16x16x32_bf16(__builtin_bit_cast(s8, a), __builtin_bit_cast(s8, b), c, 0, 0, 0); }
DI v4u pack8(f4 a, f4 b) { v4u r; r.x = pk2(a[0], a[1]); r.y = pk2(a[2], a[3]); r.z = pk2(b[0], b[1]); r.w = pk2(b[2], b[3]); return r; }
DI v4u nosent(v4u v) { v.x = min(v.x, 0xFFFFFFFEu); v.y = min(v.y, 0xFFFFFFFEu); v.z = min(v.z, 0xFFFFFFFEu); v.w = min(v.w, 0xFFFFFFFEu); return v; }
DI int perm32(int t, int rho) { return 8 * (rho >> 2) + 4 * t + (rho & 3); }
DI __amdgpu_buffer_rsrc_t mkrs(void* p, unsigned bytes) { return __builtin_amdgcn_make_buffer_rsrc(p, 0, bytes, 0x00020000); }
DI v4u ld_sc1(__amdgpu_buffer_rsrc_t rs, unsigned voff, unsigned soff) { return __builtin_amdgcn_raw_buffer_load_b128(rs, voff, soff, 16); }
DI v2u ld2_sc1(__amdgpu_buffer_rsrc_t rs, unsigned voff, unsigned soff) { return __builtin_amdgcn_raw_buffer_load_b64(rs, voff, soff, 16); }
DI void st_sc1(v4u v, __amdgpu_buffer_rsrc_t rs, unsigned voff, unsigned soff) { __builtin_amdgcn_raw_buffer_store_b128(v, rs, voff, soff, 16); }
DI void st2_sc1(v2u v, __amdgpu_buffer_rsrc_t rs, unsigned voff, unsigned soff) { __builtin_amdgcn_raw_buffer_store_b64(v, rs, voff, soff, 16); }
DI unsigned long long mtime() { unsigned long long t; asm volatile("s_memtime %0\n\ts_waitcnt lgkmcnt(0)" : "=s"(t) :: "memory"); return t; }

typedef short s4t __attribute__((ext_vector_type(4)));
DI v2u trread(const unsigned char* p) { return __builtin_bit_cast(v2u, __builtin_amdgcn_ds_read_tr16_b64_v4i16((__attribute__((address_space(3))) s4t*)p)); }
DI unsigned comm_step_base(int step, int copy) { return __builtin_amdgcn_readfirstlane((unsigned)(step & 7) * 262144u + (unsigned)copy * 65536u); }
DI unsigned segb(int sg, unsigned polo) { return __builtin_amdgcn_readfirstlane((unsigned)GU_BASE + (unsigned)sg * 2097152u + ((polo ^ FARX ^ ((unsigned)(sg & 1) << 13)) & 8192u)); }
DI unsigned comm_off(int chunk0, unsigned o, unsigned pol) { return (unsigned)(chunk0 + (o >> 13)) * 16384u + pol * 8192u + (o & 8191u); }

__global__ void __launch_bounds__(448) k_pre(unsigned char* ws, const u16* __restrict__ Wq, const u16* __restrict__ Wk, const u16* __restrict__ Wv, const u16* __restrict__ Wo,
                                             const u16* __restrict__ bx, const u16* __restrict__ mem0) {
    extern __shared__ __attribute__((aligned(16))) unsigned char lds[];
    const int tid = threadIdx.x, lane = tid & 63, w = __builtin_amdgcn_readfirstlane(tid >> 6);
    const int wg = blockIdx.x;
#define PP(i)
    const int h = (wg & 7) * 2 + ((wg >> 3) & 1), rest = wg >> 4;
    const int mat = rest >> 3, rh = (rest >> 2) & 1, q = rest & 3;
    const int rho = lane & 15, g = lane >> 4;
    const int rb = 7 * rh + w;
    __amdgpu_buffer_rsrc_t rs = mkrs(ws, 0x7fffffff);
    const int gt = wg * 448 + tid; constexpr int GN = 256 * 448;
    constexpr int WST = 240;
    v4u bt[8];
    if (mat == 0) {
        const int ct = 7 * q + w; const int d = 32 * (ct >> 1) + perm32(ct & 1, rho);
#pragma unroll
        for (int es = 0; es < 8; ++es) bt[es] = *(const v4u*)(Wq + ((size_t)d * H + h) * DH + es * 32 + g * 8);
    } else {
#pragma unroll
        for (int it = 0; it < 8; ++it) {
            const int idx = it * 448 + tid; const int e = idx / 14, c8 = idx % 14;
            bt[it] = *(const v4u*)(Wo + ((size_t)h * DH + e) * D + 112 * q + c8 * 8);
        }
    }
    const u16* Wr = mat ? Wv : Wk;
    v4u a[2][8];
#pragma unroll
    for (int t = 0; t < 2; ++t) {
        const int row = 32 * rb + perm32(t, rho);
#pragma unroll
        for (int es = 0; es < 8; ++es) a[t][es] = *(const v4u*)(Wr + ((size_t)row * H + h) * DH + es * 32 + g * 8);
    }
    v4u xfv;
    {
        const int f = gt >> 6; const int ks = f % 14; const int st = f / 14; const int tau = st & 1, seg = st >> 1;
        xfv = *(const v4u*)(bx + (size_t)(seg * 32 + perm32(tau, rho)) * D + ks * 32 + g * 8);
    }
    v4u m0v = {0, 0, 0, 0};
    if (wg < 8) m0v = *(const v4u*)(mem0 + (tid & 7) * D + (tid >> 3) * 8);
    __builtin_amdgcn_sched_barrier(0);
    PP(0)
    if (mat == 0) {
#pragma unroll
        for (int es = 0; es < 8; ++es) *(v4u*)(lds + (w * 8 + es) * 1024 + lane * 16) = bt[es];
    } else {
#pragma unroll
        for (int it = 0; it < 8; ++it) { const int idx = it * 448 + tid; const int e = idx / 14, c8 = idx % 14; *(v4u*)(lds + e * WST + c8 * 16) = bt[it]; }
    }
    PP(3)
    asm volatile("s_waitcnt lgkmcnt(0)" ::: "memory"); __builtin_amdgcn_s_barrier(); asm volatile("" ::: "memory");
    PP(4)
    f4 acc[2][7];
#pragma unroll
    for (int t = 0; t < 2; ++t)
#pragma unroll
        for (int c = 0; c < 7; ++c) acc[t][c] = f4{0, 0, 0, 0};
    if (mat == 0) {
#pragma unroll
        for (int es = 0; es < 8; ++es)
#pragma unroll
            for (int c = 0; c < 7; ++c) {
                v4u bfr = *(const v4u*)(lds + (c * 8 + es) * 1024 + lane * 16);
                acc[0][c] = mfma(a[0][es], bfr, acc[0][c]);
                acc[1][c] = mfma(a[1][es], bfr, acc[1][c]);
            }
        const float sc = LOG2E * 0.0625f;
#pragma unroll
        for (int c = 0; c < 7; ++c) {
            const int ct = 7 * q + c;
            st_sc1(pack8(acc[0][c] * sc, acc[1][c] * sc), rs, (unsigned)(AS_OFF + ((size_t)((h * 14 + (ct >> 1)) * 2 + (ct & 1)) * 14 + rb) * 1024) + lane * 16, 0);
        }
    } else {
        const unsigned trb = (8 * g + (rho >> 2)) * WST + (lane & 3) * 8;
#pragma unroll
        for (int es = 0; es < 8; ++es)
#pragma unroll
            for (int c = 0; c < 7; ++c) {
                v2u lo = trread(lds + trb + (32 * es) * WST + c * 32), hi = trread(lds + trb + (32 * es + 4) * WST + c * 32);
                v4u bfr = {lo.x, lo.y, hi.x, hi.y};
                acc[0][c] = mfma(a[0][es], bfr, acc[0][c]);
                acc[1][c] = mfma(a[1][es], bfr, acc[1][c]);
            }
#pragma unroll
        for (int c = 0; c < 7; ++c) {
            const int ct = 7 * q + c;
            const int j = 2 * ct + (rho >> 3);
            st_sc1(pack8(acc[0][c], acc[1][c]), rs, (unsigned)(BS_OFF + ((size_t)(j * 8 + (h >> 1)) * 14 + rb) * 1024) + (((h & 1) * 8 + (rho & 7)) + 16 * g) * 16, 0);
        }
    }
    if (wg == 1 && tid < 128) __builtin_amdgcn_raw_buffer_store_b32(0u, rs, (unsigned)CTRL_OFF + tid * 128, 0, 16);
    if (wg == 2 && tid < 128) __builtin_amdgcn_raw_buffer_store_b32(0u, rs, (unsigned)CTRL_OFF + C_DN2 + tid * 4, 0, 16);
    for (unsigned i = gt; i < COMM_BYTES / 16; i += GN) {
        unsigned off = i * 16;
        unsigned within = off & 65535u, slot = off >> 18;
        unsigned chunk = (within >> 14) & 3, o = within & 8191u;
        if (chunk == 0 && (o >= 7184 || (slot == 0 && o < 7168))) continue;
        st_sc1(v4u{SENT, SENT, SENT, SENT}, rs, off, 0);
    }
    st_sc1(xfv, rs, (unsigned)XF_OFF + gt * 16, 0);
    if (wg < 8) st_sc1(nosent(m0v), rs, (wg >> 1) * 65536u + (wg & 1) * 8192u + tid * 16, 0);
    PP(5)
}

DI unsigned umax3(unsigned a, unsigned b, unsigned c) { return max(max(a, b), c); }
DI unsigned vmax4(unsigned t, v4u v) { return umax3(umax3(t, v.x, v.y), v.z, v.w); }
DI float dpp_xor8(float v) { return asf((unsigned)__builtin_amdgcn_update_dpp(0, (int)asu(v), 0x128, 0xf, 0xf, false)); }
DI unsigned dpp_xor8u(unsigned v) { return (unsigned)__builtin_amdgcn_update_dpp(0, (int)v, 0x128, 0xf, 0xf, false); }
constexpr unsigned SPIN_MAX = 200000;
DI float dpp_x1(float v) { return asf((unsigned)__builtin_amdgcn_update_dpp(0, (int)asu(v), 0xB1, 0xf, 0xf, false)); }
DI float dpp_x2(float v) { return asf((unsigned)__builtin_amdgcn_update_dpp(0, (int)asu(v), 0x4E, 0xf, 0xf, false)); }
DI f4 quadsum(f4 v) {
#pragma unroll
    for (int r = 0; r < 4; ++r) v[r] += dpp_x1(v[r]);
#pragma unroll
    for (int r = 0; r < 4; ++r) v[r] += dpp_x2(v[r]);
    return v;
}
DI float xmax16(float x) { auto r = __builtin_amdgcn_permlane16_swap(asu(x), asu(x), false, false); unsigned a = r[0], b = r[1]; return fmaxf(asf(a), asf(b)); }
DI float xmax32(float x) { auto r = __builtin_amdgcn_permlane32_swap(asu(x), asu(x), false, false); unsigned a = r[0], b = r[1]; return fmaxf(asf(a), asf(b)); }
DI float xsum16(float x) { auto r = __builtin_amdgcn_permlane16_swap(asu(x), asu(x), false, false); unsigned a = r[0], b = r[1]; return asf(a) + asf(b); }
DI float xsum32(float x) { auto r = __builtin_amdgcn_permlane32_swap(asu(x), asu(x), false, false); unsigned a = r[0], b = r[1]; return asf(a) + asf(b); }
#define LOADM(mf) v4u mf[14]; _Pragma("unroll") for (int ks = 0; ks < 14; ++ks) mf[ks] = *(const v4u*)(ldsm + ks * 512 + mfrag_off); __builtin_amdgcn_sched_barrier(0);
DI void bar() { asm volatile("s_waitcnt lgkmcnt(0)" ::: "memory"); __builtin_amdgcn_s_barrier(); asm volatile("" ::: "memory"); }
DI unsigned now32() { return (unsigned)mtime(); }
DI void wait_until(unsigned target) {
    int rem = (int)(target - now32());
    while (rem > 80) { __builtin_amdgcn_s_sleep(1); rem -= 64; }
    while (rem > 0) { asm volatile("s_nop 7" ::: "memory"); rem -= 16; }
}

DI unsigned rt32() { unsigned long long t; asm volatile("s_memrealtime %0\n\ts_waitcnt lgkmcnt(0)" : "=s"(t) :: "memory"); return (unsigned)t; }
DI unsigned approach_rt(unsigned target, int lead) {
    int rem;
    for (;;) {
        rem = (int)(target - rt32());
        if (rem <= lead + 4) break;
        int n = (rem - lead) >> 2;
        while (n-- > 0) __builtin_amdgcn_s_sleep(1);
    }
    return now32() + (unsigned)(rem * 23 - 110);
}
DI void clock_ref(unsigned& re, unsigned& me) {
    unsigned long long m0, m1, r[12];
    asm volatile("s_memtime %0\n\t"
        "s_memrealtime %1\n\ts_nop 1\n\ts_memrealtime %2\n\ts_nop 1\n\ts_memrealtime %3\n\ts_nop 1\n\ts_memrealtime %4\n\ts_nop 1\n\t"
        "s_memrealtime %5\n\ts_nop 1\n\ts_memrealtime %6\n\ts_nop 1\n\ts_memrealtime %7\n\ts_nop 1\n\ts_memrealtime %8\n\ts_nop 1\n\t"
        "s_memrealtime %9\n\ts_nop 1\n\ts_memrealtime %10\n\ts_nop 1\n\ts_memrealtime %11\n\ts_nop 1\n\ts_memrealtime %12\n\ts_nop 1\n\t"
        "s_memtime %13\n\ts_waitcnt lgkmcnt(0)"
        : "=&s"(m0), "=&s"(r[0]), "=&s"(r[1]), "=&s"(r[2]), "=&s"(r[3]), "=&s"(r[4]), "=&s"(r[5]), "=&s"(r[6]), "=&s"(r[7]), "=&s"(r[8]), "=&s"(r[9]), "=&s"(r[10]), "=&s"(r[11]), "=&s"(m1) :: "memory");
    unsigned cnt = 0;
#pragma unroll
    for (int i = 1; i < 12; ++i) cnt += ((unsigned)r[i] == (unsigned)r[0]) ? 1u : 0u;
    re = (unsigned)r[0] + 4u;
    me = (unsigned)m0 + ((((unsigned)m1 - (unsigned)m0) * (2u * cnt + 1u) * 43u) >> 10);
}
DI void sleep_to(unsigned tc) {
    for (;;) {
        const int rem = (int)(tc - now32());
        if (rem <= 0) return;
        if (rem > 260) { int n = (rem - 160) / 96; while (n-- > 0) __builtin_amdgcn_s_sleep(1); }
    }
}
DI void spin_to(unsigned tc) { while ((int)(tc - now32()) > 0) {} }
DI void wait_rt(unsigned target) { spin_to(approach_rt(target, 10)); }
template <int NP, bool CTRL>
DI unsigned gather_m(__amdgpu_buffer_rsrc_t rs, unsigned sb, unsigned char* ldsm, int p0, int lane, int skipline = -1) {
    const unsigned o0 = p0 * 1024 + lane * 16;
    for (unsigned spin = 0;; ++spin) {
        asm volatile("" ::: "memory");
        v4u a[NP]; v2u c = {0, 0};
        bool sk[NP];
#pragma unroll
        for (int k = 0; k < NP; ++k) { sk[k] = (int)((o0 + k * 1024) >> 7) == skipline; a[k] = ld_sc1(rs, (o0 + k * 1024) ^ (sk[k] ? 128u : 0u), sb); }
        if (CTRL) c = ld2_sc1(rs, 7168u, sb);
#pragma unroll
        for (int k = 0; k < NP; ++k) *(v4u*)(ldsm + (sk[k] ? 7424u + (lane & 7) * 16 : o0 + k * 1024)) = a[k];
        if (CTRL && lane == 0) *(v2u*)(ldsm + 7168) = c;
        unsigned t;
        if (NP == 2) t = max(vmax4(0, a[0]), vmax4(0, a[1])); else t = vmax4(0, a[0]);
        if (CTRL) t = umax3(t, c.x, c.y);
        if (__all(t != SENT) || spin > SPIN_MAX) return spin;
        __builtin_amdgcn_s_sleep(1);
    }
}
template <int NP, bool CTRL>
DI unsigned gather_fast(__amdgpu_buffer_rsrc_t rs, unsigned sb, unsigned char* ldsm, const unsigned (&vo)[2], const unsigned (&lo)[2], int p0, int lane, int skipline) {
    asm volatile("; gather %0 %1" :: "i"(NP), "i"((int)CTRL) : "memory");
    v4u a0 = ld_sc1(rs, vo[0], sb), a1; v2u c;
    if (NP == 2) a1 = ld_sc1(rs, vo[1], sb);
    if (CTRL) c = ld2_sc1(rs, 7168u, sb);
    __builtin_amdgcn_sched_barrier(0);
    *(v4u*)(ldsm + lo[0]) = a0;
    unsigned t = vmax4(0, a0);
    if (NP == 2) { *(v4u*)(ldsm + lo[1]) = a1; t = vmax4(t, a1); }
    if (CTRL) { *(v2u*)(ldsm + lo[1]) = c; t = umax3(t, c.x, c.y); }
    if (__builtin_expect(__all(t != SENT), 1)) return 0;
    gather_m<NP, CTRL>(rs, sb, ldsm, p0, lane, skipline);
    return 1;
}
#define GADDR(vo, lo, p0, np, skl) { _Pragma("unroll") for (int k_ = 0; k_ < 2; ++k_) { const unsigned o_ = ((p0) + k_) * 1024 + lane * 16; const bool sk_ = (int)(o_ >> 7) == (skl); \
        vo[k_] = o_ ^ (sk_ ? 128u : 0u); lo[k_] = sk_ ? 7424u + (lane & 7) * 16 : o_; } if ((np) == 1) lo[1] = lane == 0 ? 7168u : 7552u + (lane & 7) * 8; }
#define LOADP(mf) v4u mf[7]; _Pragma("unroll") for (int q_ = 0; q_ < 7; ++q_) mf[q_] = *(const v4u*)(ldsm + q_ * 1024 + pfrag_off); __builtin_amdgcn_sched_barrier(0);
DI unsigned reduce8(const float* rd, int lane) {
    const float* q = rd + (lane >> 3) * 100 + (lane & 7) * 12;
    f4 a = *(const f4*)q, b = *(const f4*)(q + 4);
    a += b;
    float v = (a[0] + a[1]) + (a[2] + a[3]);
    float o = dpp_x1(v);
    unsigned r = (lane & 1) ? pk2(o, v) : pk2(v, o);
    return min(r, 0xFFFFFFFEu);
}
#define READCTRL() { SAVER1 v2u c_ = *(const v2u*)(ldsm + 7168); R2 = __builtin_amdgcn_readfirstlane(c_.x); R1n = __builtin_amdgcn_readfirstlane(c_.y); }

#define SAVER1
typedef short s4 __attribute__((ext_vector_type(4)));
DI f4 mfma16(v2u a, v2u b, f4 c) { return __builtin_amdgcn_mfma_f32_16x16x16bf16_1k(__builtin_bit_cast(s4, a), __builtin_bit_cast(s4, b), c, 0, 0, 0); }
DI float vmax(float a, float b) { float r; asm("s_nop 1\n\tv_max_f32 %0, %1, %2" : "=v"(r) : "v"(a), "v"(b)); return r; }
DI float vmax3(float a, float b, float c) { float r; asm("v_max3_f32 %0, %1, %2, %3" : "=v"(r) : "v"(a), "v"(b), "v"(c)); return r; }
DI float vmax3_mfma(float a, float b, float c) { float r; asm("s_nop 3\n\tv_max3_f32 %0, %1, %2, %3" : "=v"(r) : "v"(a), "v"(b), "v"(c)); return r; }
DI float rmax16(float x) { auto r = __builtin_amdgcn_permlane16_swap(asu(x), asu(x), false, false); unsigned a = r[0], b = r[1]; return vmax(asf(a), asf(b)); }
DI float rmax32(float x) { auto r = __builtin_amdgcn_permlane32_swap(asu(x), asu(x), false, false); unsigned a = r[0], b = r[1]; return vmax(asf(a), asf(b)); }
constexpr int HS = 1408;
constexpr int ST_OFF = 16 * HS;
constexpr int ZL_OFF = ST_OFF + 1024;
constexpr int L_M = 0, L_RED = 8192, L_RED2 = 12800, L_GM = 16128, L_VP = 17152, L_FL = 19200, L_UF = 20480;

DI unsigned gather_ctrl(__amdgpu_buffer_rsrc_t rs, unsigned sb, unsigned char* ldsm, int lane) {
    for (unsigned spin = 0;; ++spin) {
        asm volatile("" ::: "memory");
        v2u c = ld2_sc1(rs, 7168u, sb);
        if (lane == 0) *(v2u*)(ldsm + 7168) = c;
        if (__all(max(c.x, c.y) != SENT) || spin > SPIN_MAX) return spin;
        __builtin_amdgcn_s_sleep(1);
    }
}
#define REREF 1
#define RSLEEP 0
#define XCK 100
#define FIRSTN 2
#define FLK 10
#define CQMAX 500u
#define LATEREF 1
#ifndef NVX
#define NVX 30u
#endif
#define FASTN 0
#define FASTD 4u
#define ASLEEP 2
#define HSLEEP 2
#define SB0 __builtin_amdgcn_sched_barrier(0);

DI void glds16(const unsigned char* gsrc, unsigned lds_dst) {
    unsigned keep;
    asm volatile("s_mov_b32 %0, m0\n\ts_mov_b32 m0, %2\n\ts_nop 0\n\tglobal_load_lds_dwordx4 %1, off sc1\n\ts_mov_b32 m0, %0" : "=&s"(keep) : "v"(gsrc), "s"(lds_dst) : "memory");
}
DI void glds16x4(const unsigned char* gsrc, unsigned lds_dst) {
    unsigned keep;
    asm volatile("s_mov_b32 %0, m0\n\ts_mov_b32 m0, %2\n\ts_nop 0\n\t"
                 "global_load_lds_dwordx4 %1, off sc1\n\tglobal_load_lds_dwordx4 %1, off offset:1024 sc1\n\t"
                 "global_load_lds_dwordx4 %1, off offset:2048 sc1\n\tglobal_load_lds_dwordx4 %1, off offset:3072 sc1\n\t"
                 "s_mov_b32 m0, %0" : "=&s"(keep) : "v"(gsrc), "s"(lds_dst) : "memory");
}
DI void glds4(const unsigned char* gsrc, unsigned lds_dst) {
    unsigned keep;
    asm volatile("s_mov_b32 %0, m0\n\ts_mov_b32 m0, %2\n\ts_nop 0\n\tglobal_load_lds_dword %1, off sc1\n\ts_mov_b32 m0, %0" : "=&s"(keep) : "v"(gsrc), "s"(lds_dst) : "memory");
}
DI unsigned ldflag(__amdgpu_buffer_rsrc_t rs, unsigned off) { return __builtin_amdgcn_raw_buffer_load_b32(rs, (unsigned)CTRL_OFF + off, 0, 16); }
DI unsigned wait_flags(__amdgpu_buffer_rsrc_t rs, unsigned off, unsigned need, int slp) {
    unsigned v;
    for (unsigned spin = 0;; ++spin) {
        asm volatile("" ::: "memory");
        v = ldflag(rs, off);
        if (__all(v >= need) || spin > 4 * SPIN_MAX) break;
        for (int i = 0; i < slp; ++i) __builtin_amdgcn_s_sleep(4);
    }
#pragma unroll
    for (int o = 32; o >= 1; o >>= 1) v = min(v, (unsigned)__shfl_xor((int)v, o));
    return __builtin_amdgcn_readfirstlane(v);
}

#define LDSV(off) (*(volatile __attribute__((address_space(3))) unsigned*)((__attribute__((address_space(3))) unsigned char*)lds + (off)))
DI void rec_role(unsigned char* ws, unsigned char* lds, const int role, int dS, int dV, int dVm) {
    const int tid = threadIdx.x, lane = tid & 63, w = __builtin_amdgcn_readfirstlane(tid >> 6);
    const int rho = lane & 15, g = lane >> 4;
    __amdgpu_buffer_rsrc_t rs = mkrs(ws, 0x7fffffff);
    unsigned char* ldsm = lds + L_M;
    float* red = (float*)(lds + L_RED);
    float* red2 = (float*)(lds + L_RED2);
    if (role == 112) {
        unsigned best = 0xffffffffu;
        for (int r = 0; r < 10; ++r) {
            const v4u z = {SENT, SENT, SENT, SENT};
            const unsigned off = (unsigned)((w >> 1) + 4 * (r & 1)) * 262144u + ((r >> 1) & 3) * 65536u + 7680u + ((r >> 3) + 2 * (r & 1)) * 128u + (w & 1) * 8192u;
            const unsigned t0 = now32();
            if (lane < 8) st_sc1(z, rs, off + lane * 16, 0);
            asm volatile("s_waitcnt vmcnt(0)" ::: "memory");
            const unsigned dt = now32() - t0;
            if (r > 0) best = min(best, dt);
        }
        if (lane == 0) ((unsigned*)lds)[w] = best;
        __syncthreads();
        if (tid == 0) {
            const unsigned* bl = (const unsigned*)lds;
            const unsigned b0 = (bl[0] + bl[2]) + (bl[4] + bl[6]), b1 = (bl[1] + bl[3]) + (bl[5] + bl[7]);
            __builtin_amdgcn_raw_buffer_store_b32(b1 < b0 ? 2u : 1u, rs, (unsigned)CTRL_OFF + C_POL, 0, 16);
        }
        __syncthreads();
    }
#define POLINIT() const unsigned polo = (wait_flags(rs, C_POL, 1, 1) - 1u) * 8192u; const unsigned myc = (unsigned)(role & 3) * 65536u + polo;
    if (role < 112) { for (int i = 0; i < RSLEEP; ++i) __builtin_amdgcn_s_sleep(16); }
    unsigned nfail = 0, nfail2 = 0;
    unsigned long long* dbg = (unsigned long long*)(ws + CTRL_OFF + C_DBG);
#define STAMP(cond, idx)
#define TL(idx)
#define GT(idx, ref)
#define BT(i) {}
    constexpr unsigned TOFF = 70, TOFF2 = 30;
#define CLKUPD() { unsigned r1_, m1_; clock_ref(r1_, m1_); const unsigned dr = r1_ - r0, dm = m1_ - m0; \
        if (dr > 60u && dr < CQMAX) { const int err = (int)(dm * 256u) - (int)(cq * dr); cq = (unsigned)min(max((int)cq + (err >> (dr > 230u ? 9 : 8)), 4096), 6400); } r0 = r1_; m0 = m1_; }
#define CLKINIT() { for (int i_ = 0; i_ < 6; ++i_) __builtin_amdgcn_s_sleep(8); unsigned r1_, m1_; clock_ref(r1_, m1_); const unsigned dr = r1_ - r0, dm = m1_ - m0; \
        if (dr > 60u) cq = __builtin_amdgcn_readfirstlane(min(max((unsigned)((float)dm * 256.0f / (float)dr), 4096u), 6400u)); r0 = r1_; m0 = m1_; }
#define WAITR1() { unsigned tg_ = m0 - TOFF - NVX + (unsigned)(((int)(R1n - r0) * (int)cq) >> 8); \
        if (REREF && (int)(tg_ - now32()) > 1100) { sleep_to(tg_ - 900u); unsigned ra_, ma_; clock_ref(ra_, ma_); tg_ = ma_ - TOFF - NVX + (unsigned)(((int)(R1n - ra_) * (int)cq) >> 8); } \
        sleep_to(tg_); }
    const unsigned pfrag_off = (rho >> 3) * 512 + g * 128 + (rho & 7) * 16;
    const unsigned TA = dS & 255, TB = dV & 255, TE = dVm & 255, TLATE = (dS >> 8) & 255, XUP = (dV >> 8) & 255, XDN = (dVm >> 8) & 255, X0 = (dVm >> 16) & 255;
    unsigned R2 = 0, R1n = 0;
    const bool is_mx = role >= 112;
    const bool is_v = !is_mx && (((role >> 2) & 1) == 0);
    const int j = (role >> 3) * 4 + (role & 3);
    const int h = role / 7, p = role % 7;
    const int hp = (lane >> 3) & 1, i8 = lane & 7;
    const v4u SENT4 = {SENT, SENT, SENT, SENT};

    if (is_mx) {
        const int hx = role - 112;
        POLINIT()
        if (w >= 4) {
            const unsigned orst = comm_off(1, hx * HS + 896 + (lane & 31) * 16, 0) + (lane >> 5) * 65536u + polo;
            const unsigned orst2 = comm_off(1, ST_OFF + (hx >> 1) * 128 + (hx & 1) * 64 + (lane & 1) * 16, 0) + ((lane >> 1) & 3) * 65536u + polo;
            const unsigned gfl2 = C_GU + 128u * (unsigned)min((14 * hx) / 4 + (lane & 7), (14 * hx + 13) / 4);
            unsigned kn = 0, gvo[2], glo[2];
            GADDR(gvo, glo, 2 * (w - 4), (w < 7 ? 2 : 1), -1)
            unsigned r0 = 0, m0 = 0, cq = 5900u;
            if (w == 4) {
                for (int sl = 0; sl < 8; ++sl) {
                    const unsigned sbz = comm_step_base(sl, 0);
                    st_sc1(SENT4, rs, orst, sbz); st_sc1(SENT4, rs, orst + 131072u, sbz);
                    if (lane < 8) st_sc1(SENT4, rs, orst2, sbz);
                }
                asm volatile("s_waitcnt vmcnt(0)" ::: "memory");
            }
            if (w == 7) { clock_ref(r0, m0); CLKINIT() }
            if (w == 5 && lane == 0) LDSV(L_UF) = 0u;
            for (int step = 0; step < NSEG; ++step) {
                const unsigned sbr = comm_step_base(step, 0) + myc;
                if (step > 0 && w == 7) WAITR1()
                bar();
                if (w < 7) nfail += gather_fast<2, false>(rs, sbr, ldsm, gvo, glo, 2 * (w - 4), lane, -1);
                else nfail += gather_fast<1, true>(rs, sbr, ldsm, gvo, glo, 6, lane, -1);
                bar();
                READCTRL()
                bar();
                if (w == 4 && step >= 4) {
                    __builtin_amdgcn_s_sleep(4);
                    const unsigned sbz = comm_step_base(step + 4, 0);
                    st_sc1(SENT4, rs, orst, sbz); st_sc1(SENT4, rs, orst + 131072u, sbz);
                    if (lane < 8) st_sc1(SENT4, rs, orst2, sbz);
                }
                if (w == 7) CLKUPD()
                if (w == 5 && kn < 64u) {
                    __builtin_amdgcn_s_sleep(4);
                    kn = wait_flags(rs, gfl2, 0, 1);
                    if (lane == 0) LDSV(L_UF) = kn;
                }
            }
        } else {
            __builtin_amdgcn_s_setprio(2);
            const int tau = w & 1, kh = w >> 1;
            v4u wa[8], wb[8];
            const unsigned gbase = (unsigned)((hx * 2 + tau) * 2 + kh) * 16384u + lane * 16;
            const unsigned g67 = gbase + (kh ? 4096u : 6144u);
#define GLOAD(wt, sb_) { _Pragma("unroll") for (int ks = 0; ks < 6; ++ks) wt[ks] = ld_sc1(rs, gbase + ks * 1024, sb_); \
                     wt[6] = ld_sc1(rs, g67, sb_); wt[7] = ld_sc1(rs, g67 + 1024, sb_); }
            const unsigned gfl = C_GU + 128u * (unsigned)min((14 * hx) / 4 + (lane & 7), (14 * hx + 13) / 4);
            unsigned known = wait_flags(rs, gfl, 2, 1);
            GLOAD(wa, segb(0, polo))
            GLOAD(wb, segb(1, polo))
            known = wait_flags(rs, gfl, FIRSTN, 1);
            float* sc = red;
            const int tq = lane & 7, ir = lane >> 3;
            const unsigned oes = comm_off(1, hx * HS + 896 + lane * 8, 0) + w * 65536u + polo;
            const unsigned oss = comm_off(1, ST_OFF + (hx >> 1) * 128 + (hx & 1) * 64 + ir * 4, 0) + w * 65536u + polo;
#define MXGT
#define MXSTEP(wt, step) { \
                const bool last = ((step) == NSEG - 1); \
                const unsigned sbs = comm_step_base(step, 0); \
                __builtin_amdgcn_s_barrier(); \
                bar(); \
                f4 aE = {0, 0, 0, 0}, aO = {0, 0, 0, 0}; \
                v4u mf[4]; \
                _Pragma("unroll") for (int q = 0; q < 3; ++q) mf[q] = *(const v4u*)(ldsm + (4 * kh + q) * 1024 + pfrag_off); \
                mf[3] = *(const v4u*)(ldsm + 3 * 1024 + pfrag_off); \
                SB0 \
                _Pragma("unroll") for (int q = 0; q < 3; ++q) { aE = mfma(wt[2 * q], mf[q], aE); aO = mfma(wt[2 * q + 1], mf[q], aO); } \
                if (kh == 0) { aE = mfma(wt[6], mf[3], aE); aO = mfma(wt[7], mf[3], aO); } \
                f4 xs = hp ? aO : aE; \
                *(f4*)(sc + ((2 * kh + hp) * 8 + i8) * 36 + 16 * tau + 4 * g) = xs; \
                bar(); \
                const float* sp = sc + ir * 36 + 4 * tq; \
                f4 a0 = (*(const f4*)(sp) + *(const f4*)(sp + 288)) + (*(const f4*)(sp + 576) + *(const f4*)(sp + 864)); \
                float mx; \
                asm("v_max3_f32 %0, %1, %2, %3\n\tv_max_f32 %0, %0, %4\n\ts_nop 1\n\t" \
                    "v_max_f32_dpp %0, %0, %0 quad_perm:[1,0,3,2] row_mask:0xf bank_mask:0xf\n\ts_nop 1\n\t" \
                    "v_max_f32_dpp %0, %0, %0 quad_perm:[2,3,0,1] row_mask:0xf bank_mask:0xf\n\ts_nop 1\n\t" \
                    "v_max_f32_dpp %0, %0, %0 row_half_mirror row_mask:0xf bank_mask:0xf" \
                    : "=&v"(mx) : "v"(a0[0]), "v"(a0[1]), "v"(a0[2]), "v"(a0[3])); \
                if (!last) { \
                    unsigned e0 = pk2(__builtin_amdgcn_exp2f(a0[0] - mx), __builtin_amdgcn_exp2f(a0[1] - mx)); \
                    unsigned e1 = pk2(__builtin_amdgcn_exp2f(a0[2] - mx), __builtin_amdgcn_exp2f(a0[3] - mx)); \
                    v2u eo; eo.x = min(e0, 0xFFFFFFFEu); eo.y = min(e1, 0xFFFFFFFEu); \
                    st2_sc1(eo, rs, oes, sbs); \
                    if (tq == 1) __builtin_amdgcn_raw_buffer_store_b32(min(asu(mx), 0xFFFFFFFEu), rs, oss, sbs, 16); \
                    MXGT \
                } \
                { const int sn = min((step) + 2, NSEG - 1); \
                  known = max(known, (unsigned)__builtin_amdgcn_readfirstlane(LDSV(L_UF))); \
                  if (known < (unsigned)sn + 1) known = wait_flags(rs, gfl, sn + 1, 1); \
                  const unsigned sb = segb(sn, polo); \
                  GLOAD(wt, sb) } }
            for (int step = 0; step < NSEG; step += 2) {
                MXSTEP(wa, step)
                MXSTEP(wb, step + 1)
            }
        }
    } else if (w >= 4) {
        const int wv = w - 4;
        v4u wtv[2][14];
        if (is_v) {
            const unsigned char* src = ws + BS_OFF + ((size_t)j * 8 + 2 * wv) * 14 * 1024;
#pragma unroll
            for (int t = 0; t < 2; ++t)
#pragma unroll
                for (int f = 0; f < 14; ++f) wtv[t][f] = *(const v4u*)(src + (t * 14 + f) * 1024 + lane * 16);
        } else {
#pragma unroll
            for (int t = 0; t < 2; ++t)
#pragma unroll
                for (int f = 0; f < 14; ++f) wtv[t][f] = v4u{0, 0, 0, 0};
        }
        POLINIT()
        __builtin_amdgcn_s_waitcnt(0x0F70);
        const bool cond = is_v && j == 0;
        const bool slow = w >= 6;
        const unsigned ufl = C_GU + 128u * (56 + j);
        const unsigned char* usrc = ws + (size_t)(64 + j) * 16384 + (wv & 1) * 4096 + lane * 16;
        const unsigned udst = (unsigned)(size_t)(__attribute__((address_space(3))) unsigned char*)lds + L_UF + (wv & 1) * 4096;
#define UDMA(sg) { const unsigned char* p_ = usrc + segb(sg, polo); const unsigned d_ = __builtin_amdgcn_readfirstlane(udst + ((sg) & 3) * 8192); \
            glds16x4(p_, d_); }
#define VPRIME() { \
                f4 e0 = {0, 0, 0, 0}, o0 = {0, 0, 0, 0}, e1 = {0, 0, 0, 0}, o1 = {0, 0, 0, 0}; \
                _Pragma("unroll") for (int q = 0; q < 7; ++q) { \
                    e0 = mfma(mf[q], wtv[0][2 * q], e0); o0 = mfma(mf[q], wtv[0][2 * q + 1], o0); \
                    e1 = mfma(mf[q], wtv[1][2 * q], e1); o1 = mfma(mf[q], wtv[1][2 * q + 1], o1); } \
                f4 cs; \
                _Pragma("unroll") for (int r = 0; r < 4; ++r) { \
                    auto sw = __builtin_amdgcn_permlane32_swap(asu(o0[r]), asu(e1[r]), false, false); \
                    unsigned d0 = sw[0], d1 = sw[1]; \
                    cs[r] = g < 2 ? e0[r] + asf(d1) : o1[r] + asf(d0); } \
                v2u vp; vp.x = pk2(cs[0], cs[1]); vp.y = pk2(cs[2], cs[3]); \
                *(v2u*)(lds + L_VP + wv * 512 + (2 * (g >> 1) + hp) * 128 + i8 * 16 + (g & 1) * 8) = vp; }
#define BYPROD(st_) { \
            if (w == 7) { v4u gmv = *(const v4u*)(lds + L_GM + lane * 16); \
                st_sc1(gmv, rs, (unsigned)(GM_OFF + ((size_t)h * 14 + 2 * p) * 512) + lane * 16, __builtin_amdgcn_readfirstlane((st_) * 16 * 14 * 512)); } \
            if (w == 6 && is_v) { v4u v0 = *(const v4u*)(lds + L_VP + lane * 16), v1 = *(const v4u*)(lds + L_VP + 1024 + lane * 16); \
                const unsigned sbv = __builtin_amdgcn_readfirstlane((st_) * 56 * 4 * 512); \
                st_sc1(v0, rs, (unsigned)(VP_OFF + (size_t)j * 2048) + lane * 16, sbv); \
                st_sc1(v1, rs, (unsigned)(VP_OFF + (size_t)j * 2048 + 1024) + lane * 16, sbv); } }
#define RESETS() if (w == 7 && step >= 4) { \
                    if (dorst) st_sc1(SENT4, rs, orst, comm_step_base(step + 4, 0)); \
                    if (cond && lane < 4) st_sc1(SENT4, rs, polo + 7168u + lane * 65536u, comm_step_base(step + 4, 0)); }
        const unsigned orst = (lane < 32 ? comm_off(1, h * HS + p * 128 + (lane & 7) * 16, 0) : (unsigned)(j * 128 + (lane & 7) * 16)) + ((lane >> 3) & 3) * 65536u + polo;
        const bool dorst = lane < 32 || is_v;
#define CSTORES() { \
                    if (cond && lane < 4) st_sc1(SENT4, rs, polo + 7168u + lane * 65536u, comm_step_base(step + 4, 0)); \
                    if (dorst) st_sc1(SENT4, rs, orst, comm_step_base(step + 4, 0)); \
                    v4u gmv = *(const v4u*)(lds + L_GM + lane * 16); \
                    st_sc1(gmv, rs, (unsigned)(GM_OFF + ((size_t)h * 14 + 2 * p) * 512) + lane * 16, __builtin_amdgcn_readfirstlane(step * 16 * 14 * 512)); }
        if (w == 7) {
            unsigned known = 0;
            for (int sl = 0; sl < 8; ++sl) {
                if (dorst && (sl > 0 || lane < 32)) st_sc1(SENT4, rs, orst, comm_step_base(sl, 0));
                if (cond && lane < 4 && sl > 0) st_sc1(SENT4, rs, polo + 7168u + lane * 65536u, comm_step_base(sl, 0));
            }
            asm volatile("s_waitcnt vmcnt(0)" ::: "memory");
            unsigned r0, m0, cq = 5900u; clock_ref(r0, m0);
            CLKINIT()
            if (is_v) { known = wait_flags(rs, ufl, 2, 1); UDMA(0) UDMA(1) asm volatile("s_waitcnt vmcnt(0)" ::: "memory"); }
            for (int step = 0; step < NSEG; ++step) {
                if (step > 0) {
                    if (LATEREF && is_v) {
                        const v2u lr = *(const v2u*)(lds + L_FL + 528);
                        const unsigned ra_ = __builtin_amdgcn_readfirstlane(lr.x), ma_ = __builtin_amdgcn_readfirstlane(lr.y);
                        sleep_to(ma_ - TOFF + (unsigned)(((int)(R1n - ra_) * (int)cq) >> 8));
                    } else WAITR1()
                }
                bar();
                BT(0)
                CLKUPD()
                if (lane == 0) *(v4u*)(lds + L_FL + 512) = v4u{r0, m0 - TOFF2, cq, 0u};
                bar();
                __builtin_amdgcn_s_sleep(HSLEEP);
                READCTRL()
                LOADP(mf)
                bar();
                if (is_v) VPRIME()
                bar();
                asm volatile("s_waitcnt vmcnt(0)" ::: "memory");
                if (is_v && step + 3 < NSEG) UDMA(step + 2)
                if (is_v) bar();
                else CSTORES()
            }
            bar();
            asm volatile("s_waitcnt vmcnt(0)" ::: "memory");
            __builtin_amdgcn_s_barrier();
        } else if (w == 6) {
            unsigned known = 0;
            if (is_v) { known = wait_flags(rs, ufl, 2, 1); UDMA(0) UDMA(1) asm volatile("s_waitcnt vmcnt(0)" ::: "memory"); known = wait_flags(rs, ufl, FIRSTN, 1); }
            const bool reporter = (role == 4);
            const unsigned char* fsrc = ws + CTRL_OFF + ufl;
            const unsigned fdst = __builtin_amdgcn_readfirstlane((unsigned)(size_t)(__attribute__((address_space(3))) unsigned char*)lds + L_FL);
            if (lane == 0) LDSV(L_FL) = 0u;
#define VPSTORE(st_) { v4u v0 = *(const v4u*)(lds + L_VP + lane * 16), v1 = *(const v4u*)(lds + L_VP + 1024 + lane * 16); \
                const unsigned sbv = __builtin_amdgcn_readfirstlane((st_) * 56 * 4 * 512); \
                st_sc1(v0, rs, (unsigned)(VP_OFF + (size_t)j * 2048) + lane * 16, sbv); \
                st_sc1(v1, rs, (unsigned)(VP_OFF + (size_t)j * 2048 + 1024) + lane * 16, sbv); }
            for (int step = 0; step < NSEG; ++step) {
                bar();
                if (is_v) {
                    asm volatile("s_waitcnt vmcnt(2)" ::: "memory");
                    __builtin_amdgcn_s_sleep(ASLEEP);
                    if (step + 3 < NSEG) {
                        if (known < 64u) known = max(known, (unsigned)__builtin_amdgcn_readfirstlane(LDSV(L_FL)));
                        if (known < (unsigned)step + 3) known = wait_flags(rs, ufl, step + 3, 1);
                        UDMA(step + 2)
                    }
                    if (step > 0) VPSTORE(step - 1)
                    if (known < (unsigned)step + FLK && known < 64u && lane == 0) glds4(fsrc, fdst);
                }
                bar();
                __builtin_amdgcn_s_sleep(HSLEEP);
                LOADP(mf)
                bar();
                if (is_v) VPRIME()
                bar();
                if (reporter && lane == 0 && step >= 3) __builtin_amdgcn_raw_buffer_store_b32((unsigned)(step - 2), rs, (unsigned)CTRL_OFF + C_REC, 0, 16);
                if (is_v) { CSTORES() bar(); }
            }
            bar();
            if (is_v) VPSTORE(NSEG - 1)
            asm volatile("s_waitcnt vmcnt(0)" ::: "memory");
            __builtin_amdgcn_s_barrier();
        } else {
            unsigned extra = X0, calm = 0, ont = 0, gvo[2] = {0, 0}, glo[2] = {0, 0};
            if (cond && w == 5) {
                wait_flags(rs, ufl, FIRSTN, 1);
                unsigned t = rt32() + 120u;
                v2u c; c.x = min(t, 0xFFFFFFFEu); c.y = min(t + TB, 0xFFFFFFFEu);
                if (lane < KCOPY) st2_sc1(c, rs, polo + 7168u + lane * 65536u, 0);
                if (lane == 0) *(v2u*)(ldsm + 7168) = c;
            }
            const unsigned opub = polo + j * 128 + (lane >> 1) * 4 + (lane & 1) * 65536u;
            const unsigned opubS = comm_off(1, h * HS + p * 128 + (lane >> 1) * 4, 0) + (lane & 1) * 65536u + polo;
            for (int step = 0; step < NSEG; ++step) {
                const bool last = (step == NSEG - 1);
                const unsigned sbs = comm_step_base(step, 0);
                const unsigned sbr = sbs + myc;
                const unsigned sbn = comm_step_base(step + 1, 0);
                const int skl = (is_v && step > 0) ? j : -1;
                if (step < 2) GADDR(gvo, glo, w + 1, 1, skl)
                bar();
                if (w == 4 || cond) nfail += gather_fast<1, false>(rs, sbr, ldsm, gvo, glo, w + 1, lane, skl);
                else nfail += gather_fast<1, true>(rs, sbr, ldsm, gvo, glo, 6, lane, skl);
                bar();
                if (w == 5) BT(1)
                READCTRL()
                __builtin_amdgcn_s_sleep(HSLEEP);
                LOADP(mf)
                if (w == 4) __builtin_amdgcn_s_setprio(3);
                bar();
                if (w == 5) BT(2)
                if (w == 4) {
                    if (!last) {
                        unsigned o = reduce8(red, lane);
                        __builtin_amdgcn_raw_buffer_store_b32(o, rs, opubS, sbs, 16);
                        __builtin_amdgcn_raw_buffer_store_b32(o, rs, opubS + 131072u, sbs, 16);
                        BT(5)
                        if ((lane & 1) == 0) *(unsigned*)(lds + L_FL + 640 + (lane >> 1) * 4) = o;
                    }
                    __builtin_amdgcn_s_setprio(0);
                }
                if (is_v) {
                    VPRIME()
                    bar();
                    if (w == 5) BT(3)
                    unsigned ra_ = 0, ma_ = 0;
                    if (w == 5) { clock_ref(ra_, ma_); if (lane == 0) *(v2u*)(lds + L_FL + 528) = v2u{ra_, ma_}; }
                    unsigned xclk = 0;
                    if (cond && w == 5) {
                        const int cqv = (int)__builtin_amdgcn_readfirstlane(LDSV(L_FL + 520));
                        xclk = (unsigned)max((5950 - cqv) * XCK, 0) >> 13;
                    }
                    if (w == 4) __builtin_amdgcn_s_setprio(3);
                    bar();
                    if (w == 5 && !cond) BT(4)
                    if (!last) {
                        if (w == 4) {
                            unsigned o = reduce8(red2, lane);
                            __builtin_amdgcn_raw_buffer_store_b32(o, rs, opub, sbn, 16);
                            __builtin_amdgcn_raw_buffer_store_b32(o, rs, opub + 131072u, sbn, 16);
                            BT(6)
                            if ((lane & 1) == 0) *(unsigned*)(ldsm + j * 128 + (lane >> 1) * 4) = o;
                            __builtin_amdgcn_s_setprio(0);
                        }
                        if (cond && w == 5) {
                            const unsigned tp = ra_ + (((now32() - ma_) * 11u) >> 8) + 2u;
                            unsigned base = R1n;
                            if ((int)(tp + 4u - R1n) > 0) { base = tp + TLATE; if (ont) extra = min(extra + XUP, 10u); ont = 0; calm = 0; }
                            else { ont = 1; if (++calm >= ((step < FASTN && extra > 1u) ? FASTD : XDN)) { calm = 0; extra = extra ? extra - 1u : 0u; } }
                            const unsigned ex = extra + xclk;
                            v2u c; c.x = min(base + TA + ex, 0xFFFFFFFEu); c.y = min(base + TA + TB + 2u * ex, 0xFFFFFFFEu);
                            if (lane < KCOPY) st2_sc1(c, rs, polo + 7168u + lane * 65536u, sbn);
                            if (lane == 0) *(v2u*)(ldsm + 7168) = c;
                        }
                    }
                } else bar();
            }
            bar();
            __builtin_amdgcn_s_barrier();
            if (w == 4 && lane == 0) __builtin_amdgcn_raw_buffer_store_b32(1u, rs, (unsigned)CTRL_OFF + C_DN2 + role * 4, 0, 16);
        }
    } else {
        __builtin_amdgcn_s_setprio(2);
        const int b = 2 * p + (w >> 1), t = w & 1;
        v4u wt[14];
        {
            const unsigned char* src = ws + AS_OFF + ((size_t)(h * 14 + b) * 2 + t) * 14 * 1024;
#pragma unroll
            for (int ks = 0; ks < 14; ++ks) wt[ks] = *(const v4u*)(src + ks * 1024 + lane * 16);
        }
        POLINIT()
        __builtin_amdgcn_s_waitcnt(0x0F70);
        unsigned op[2][2], oe[2], ost, gvo[2] = {0, 0}, glo[2] = {0, 0};
        v4u pf[2][2], ef[2]; unsigned stt;
#pragma unroll
        for (int a = 0; a < 2; ++a) {
            const int hd = 4 * w + 2 * a + hp;
#pragma unroll
            for (int u = 0; u < 2; ++u) {
                const int q = 4 * u + g;
                const bool z = (q == 7) || (hd == h && q == p);
                op[a][u] = z ? 0x80000000u : comm_off(1, hd * HS + q * 128 + i8 * 16, 0);
            }
            oe[a] = comm_off(1, hd * HS + 896 + i8 * 64 + g * 16, 0);
        }
        const int hl = 4 * w + 2 * (g >> 1) + hp;
        ost = comm_off(1, ST_OFF + (hl >> 1) * 128 + (hl & 1) * 64 + i8 * 4, 0);
        const bool ownhead = (hl == h);
        v4u selLo = {0, 0, 0, 0}, selHi = {0, 0, 0, 0}, oneLo, oneHi;
        {
            const unsigned one = 0x3F80u << (16 * (rho & 1));
            const int wd = (rho & 7) >> 1;
            v4u sv; sv.x = wd == 0 ? one : 0; sv.y = wd == 1 ? one : 0; sv.z = wd == 2 ? one : 0; sv.w = wd == 3 ? one : 0;
            if (rho < 8) selLo = sv; else selHi = sv;
            const unsigned o2 = 0x3F803F80u;
            oneLo = rho < 8 ? v4u{o2, o2, o2, o2} : v4u{0, 0, 0, 0};
            oneHi = rho < 8 ? v4u{0, 0, 0, 0} : v4u{o2, o2, o2, o2};
        }
        for (int step = 0; step < NSEG; ++step) {
            const bool last = (step == NSEG - 1);
            const unsigned sbs = comm_step_base(step, 0);
            const unsigned sbr = sbs + myc;
            STAMP(step == 1, 0); STAMP(step == 63, 1);
            if (step < 2) GADDR(gvo, glo, (w == 0 ? 0 : w + 1), 2, ((is_v && step > 0) ? j : -1))
            bar();
            TL(0)
            const int skl = (is_v && step > 0) ? j : -1;
            if (w == 0) nfail += gather_fast<2, false>(rs, sbr, ldsm, gvo, glo, 0, lane, skl);
            else nfail += gather_fast<1, false>(rs, sbr, ldsm, gvo, glo, w + 1, lane, skl);
            bar();
            TL(1)
            f4 aE = {0, 0, 0, 0}, aO = {0, 0, 0, 0};
            v4u mf[7];
#pragma unroll
            for (int q_ = 0; q_ < 7; ++q_) mf[q_] = *(const v4u*)(ldsm + q_ * 1024 + pfrag_off);
            v2u mb = *(const v2u*)(ldsm + b * 512 + g * 128 + i8 * 16 + t * 8);
            const unsigned cwx = *(const unsigned*)(ldsm + 7168);
            const v4u ckr = *(const v4u*)(lds + L_FL + 512);
            SB0
#pragma unroll
            for (int q = 0; q < 7; ++q) { aE = mfma(wt[2 * q], mf[q], aE); aO = mfma(wt[2 * q + 1], mf[q], aO); }
            f4 xs = hp ? aO : aE;
            v2u gh; gh.x = pk2(xs[0], xs[1]); gh.y = pk2(xs[2], xs[3]);
            f4 sp = mfma16(gh, mb, f4{0, 0, 0, 0});
            if (rho < 8) {
                float* rq = red + rho * 100 + (4 * (g & 1)) * 12 + 2 * w + (g >> 1);
                rq[0] = sp[0]; rq[12] = sp[1]; rq[24] = sp[2]; rq[36] = sp[3];
            }
            SB0
            TL(7)
            bar();
#define GMSTAGE() { f4 fs; _Pragma("unroll") for (int r = 0; r < 4; ++r) fs[r] = xs[r] + dpp_xor8(xs[r]); \
                v2u gm; gm.x = pk2(fs[0], fs[1]); gm.y = pk2(fs[2], fs[3]); \
                if (rho < 8) *(v2u*)(lds + L_GM + (w >> 1) * 512 + g * 128 + rho * 16 + t * 8) = gm; }
            if (!is_v) { GMSTAGE() bar(); continue; }
            if (last) { GMSTAGE() bar(); bar(); continue; }
#define E_LOADS(sb_) { stt = __builtin_amdgcn_raw_buffer_load_b32(rs, ost, sb_, 16); ef[0] = ld_sc1(rs, oe[0], sb_); ef[1] = ld_sc1(rs, oe[1], sb_); SB0 }
#define P_LOADS(sb_) { pf[0][0] = ld_sc1(rs, op[0][0], sb_); SB0 pf[1][0] = ld_sc1(rs, op[1][0], sb_); SB0 pf[0][1] = ld_sc1(rs, op[0][1], sb_); SB0 pf[1][1] = ld_sc1(rs, op[1][1], sb_); SB0 }
            const unsigned tc2 = (unsigned)__builtin_amdgcn_readfirstlane(ckr.y) + (unsigned)(((int)(__builtin_amdgcn_readfirstlane(cwx) - __builtin_amdgcn_readfirstlane(ckr.x)) * (int)__builtin_amdgcn_readfirstlane(ckr.z)) >> 8);
            spin_to(tc2 - TE * 23u);
            E_LOADS(sbr)
            asm volatile("" ::: "memory");
            spin_to(tc2);
            TL(3)
            P_LOADS(sbr)
            asm volatile("" ::: "memory");
            SB0
            if (w == 1) BT(7)
            GMSTAGE()
            v4u uf[2];
            uf[0] = *(const v4u*)(lds + L_UF + (step & 3) * 8192 + w * 2048 + lane * 16); uf[1] = *(const v4u*)(lds + L_UF + (step & 3) * 8192 + w * 2048 + 1024 + lane * 16);
            bar();
            v2u vpA, vpB;
            f4 sini = {0, 0, 0, 0};
            {
                v2u vp = *(const v2u*)(lds + L_VP + w * 512 + (2 * (g >> 1) + hp) * 128 + i8 * 16 + (g & 1) * 8);
                v2u ow = *(const v2u*)(lds + L_FL + 640 + i8 * 16 + (g & 1) * 8);
                vpA = g < 2 ? vp : v2u{0, 0};
                vpB = g < 2 ? v2u{0, 0} : vp;
                if (ownhead) { sini[0] = bflo(ow.x); sini[1] = bfhi(ow.x); sini[2] = bflo(ow.y); sini[3] = bfhi(ow.y); }
            }
            asm volatile("" ::: "memory");
            SB0
            TL(4)
            bool okall;
            auto combine = [&]() __attribute__((always_inline)) {
                f4 ls = mfma(oneLo, ef[0], f4{0, 0, 0, 0});
                ls = mfma(oneHi, ef[1], ls);
                f4 x0 = mfma(uf[0], ef[0], f4{0, 0, 0, 0});
                f4 x1 = mfma(uf[1], ef[1], f4{0, 0, 0, 0});
                unsigned t2 = vmax4(vmax4(stt, ef[0]), ef[1]);
                float lsv = ls[0];
                asm volatile("" : "+v"(t2), "+v"(lsv));
                SB0
                f4 sacc = mfma(selLo, pf[0][0], sini);
                sacc = mfma(selHi, pf[1][0], sacc);
                sacc = mfma(selLo, pf[0][1], sacc);
                sacc = mfma(selHi, pf[1][1], sacc);
                SB0
                unsigned t0 = vmax4(vmax4(t2, pf[0][0]), pf[0][1]), t1 = vmax4(vmax4(0, pf[1][0]), pf[1][1]);
                asm volatile("" : "+v"(t0), "+v"(t1));
                SB0
                const float mloc = asf(stt);
                float mx = vmax3(vmax3_mfma(sacc[0], sacc[1], sacc[2]), sacc[3], mloc);
                mx = rmax16(mx);
                float p0 = __builtin_amdgcn_exp2f(sacc[0] - mx), p1 = __builtin_amdgcn_exp2f(sacc[1] - mx), p2 = __builtin_amdgcn_exp2f(sacc[2] - mx), p3 = __builtin_amdgcn_exp2f(sacc[3] - mx);
                float f = __builtin_amdgcn_exp2f(mloc - mx);
                v2u pb; pb.x = pk2(p0, p1); pb.y = pk2(p2, p3);
                float zq = (p0 + p1) + (p2 + p3);
                SB0
                f4 cmA = mfma16(vpA, pb, f4{0, 0, 0, 0});
                f4 cmB = mfma16(vpB, pb, f4{0, 0, 0, 0});
                SB0
                float z = xsum16(zq);
                auto fs = __builtin_amdgcn_permlane32_swap(asu(f), asu(f), false, false);
                unsigned fa = fs[0], fb = fs[1];
                z += f * lsv;
                float rz = __builtin_amdgcn_rcpf(z);
                okall = __all(max(t0, t1) != SENT);
                f4 ya = x0 * asf(fa) + cmA, yb = x1 * asf(fb) + cmB;
                auto rs2 = __builtin_amdgcn_permlane32_swap(asu(rz), asu(rz), false, false);
                unsigned ra = rs2[0], rb = rs2[1];
                f4 cx = ya * asf(ra) + yb * asf(rb);
                if (hp == (g >> 1)) {
                    float* rq = red2 + i8 * 100 + (4 * (g & 1)) * 12 + 2 * w + hp;
                    rq[0] = cx[0]; rq[12] = cx[1]; rq[24] = cx[2]; rq[36] = cx[3];
                }
            };
            combine();
            if (!okall) {
                nfail2++;
                for (unsigned spin = 0; spin < SPIN_MAX && !okall; ++spin) {
                    __builtin_amdgcn_s_sleep(1);
                    asm volatile("" ::: "memory");
                    E_LOADS(sbr)
                    P_LOADS(sbr)
                    combine();
                }
            }
            TL(6)
            bar();
        }
        bar();
        __builtin_amdgcn_s_barrier();
    }
}

DI void signal_add(unsigned char* ws, unsigned off) {
    asm volatile("s_waitcnt vmcnt(0)" ::: "memory");
    __syncthreads();
    if (threadIdx.x == 0) __hip_atomic_fetch_add((unsigned*)(ws + CTRL_OFF + off), 1u, __ATOMIC_RELAXED, __HIP_MEMORY_SCOPE_AGENT);
}
DI void wg_wait(__amdgpu_buffer_rsrc_t rs, unsigned off_lane, unsigned need, int slp) {
    if (threadIdx.x < 64) wait_flags(rs, off_lane, need, slp);
    __syncthreads();
}

DI void gldsx16(const unsigned char* gsrc, unsigned lds_dst) {
    unsigned keep;
    asm volatile("s_mov_b32 %0, m0\n\ts_mov_b32 m0, %2\n\ts_nop 0\n\tglobal_load_lds_dwordx4 %1, off\n\ts_mov_b32 m0, %0" : "=&s"(keep) : "v"(gsrc), "s"(lds_dst) : "memory");
}
DI void gu_role(unsigned char* ws, unsigned char* lds, const int gw) {
    const int tid = threadIdx.x, lane = tid & 63, w8 = __builtin_amdgcn_readfirstlane(tid >> 6);
    const int w = w8 & 3, sp = w8 >> 2;
    const bool isG = gw < 56;
    const unsigned ldsb = __builtin_amdgcn_readfirstlane((unsigned)(size_t)(__attribute__((address_space(3))) unsigned char*)lds);
    const int nat0 = (((lane & 15) >> 2) & 1) * 14464 + ((4 * ((lane & 15) >> 3) + (lane & 3)) + 16 * (lane >> 4)) * 16;
    const unsigned char* xsrc = ws + XF_OFF + w8 * 1024 + lane * 16;
#define XDMA(pr_) { _Pragma("unroll") for (int i = 0; i < 7; ++i) gldsx16(xsrc + (size_t)(pr_) * 57344 + i * 8192, ldsb + ((pr_) & 1) * 57856 + (i * 8 + w8) * 1024 + ((i * 8 + w8) / 14) * 128); }
    XDMA(0)
    v4u wt[2][14];
    const unsigned char* wsrc = isG ? ws + AS_OFF + (size_t)(4 * gw + w) * 28 * 1024 : ws + BS_OFF + ((size_t)(gw - 56) * 8 + 2 * w) * 14 * 1024;
    unsigned char* xch = lds + 57856;
    v4u tmp[14];
#pragma unroll
    for (int ks = 0; ks < 14; ++ks) tmp[ks] = *(const v4u*)(wsrc + (sp * 14 + ks) * 1024 + lane * 16);
    SB0
#pragma unroll
    for (int half = 0; half < 2; ++half) {
        if (half) bar();
#pragma unroll
        for (int ks = 0; ks < 7; ++ks) *(v4u*)(xch + (w8 * 7 + ks) * 1024 + lane * 16) = tmp[half * 7 + ks];
        bar();
#pragma unroll
        for (int t = 0; t < 2; ++t)
#pragma unroll
            for (int ks = 0; ks < 7; ++ks) wt[t][half * 7 + ks] = *(const v4u*)(xch + ((w + 4 * t) * 7 + ks) * 1024 + lane * 16);
    }
    __amdgpu_buffer_rsrc_t rs = mkrs(ws, 0x7fffffff);
    asm volatile("s_waitcnt vmcnt(0)" ::: "memory");
    bar();
    const int bb = 4 * gw + w, hh = bb / 14, bq = bb % 14;
    const unsigned obase = isG ? (unsigned)(hh * 4 + (bq >> 3)) * 16384u + (bq & 7) * 1024 + lane * 16 : (unsigned)(64 + gw - 56) * 16384u + 2 * w * 1024 + lane * 16;
    const unsigned ostep = isG ? 32768u : 1024u;
    unsigned polo = 0;
    for (int pr = 0; pr < 32; ++pr) {
        const unsigned char* cur = lds + (pr & 1) * 57856 + sp * 28928;
        if (pr + 1 < 32) XDMA(pr + 1)
        f4 acc[2][2] = {{f4{0,0,0,0}, f4{0,0,0,0}}, {f4{0,0,0,0}, f4{0,0,0,0}}};
        {
            const unsigned char* p0 = cur + (isG ? nat0 : lane * 16);
            const unsigned char* p1 = p0 + (isG ? 128 : 14464);
            v4u xa[3], xb[3];
#pragma unroll
            for (int k = 0; k < 3; ++k) { xa[k] = *(const v4u*)(p0 + k * 1024); xb[k] = *(const v4u*)(p1 + k * 1024); }
            if (isG) {
#pragma unroll
                for (int ks = 0; ks < 14; ++ks) {
                    const v4u x0 = xa[ks % 3], x1 = xb[ks % 3];
                    acc[0][0] = mfma(wt[0][ks], x0, acc[0][0]); acc[1][0] = mfma(wt[1][ks], x0, acc[1][0]);
                    acc[0][1] = mfma(wt[0][ks], x1, acc[0][1]); acc[1][1] = mfma(wt[1][ks], x1, acc[1][1]);
                    if (ks + 3 < 14) { xa[ks % 3] = *(const v4u*)(p0 + (ks + 3) * 1024); xb[ks % 3] = *(const v4u*)(p1 + (ks + 3) * 1024); }
                    SB0
                }
            } else {
#pragma unroll
                for (int ks = 0; ks < 14; ++ks) {
                    const v4u x0 = xa[ks % 3], x1 = xb[ks % 3];
                    acc[0][0] = mfma(x0, wt[0][ks], acc[0][0]); acc[1][0] = mfma(x1, wt[0][ks], acc[1][0]);
                    acc[0][1] = mfma(x0, wt[1][ks], acc[0][1]); acc[1][1] = mfma(x1, wt[1][ks], acc[1][1]);
                    if (ks + 3 < 14) { xa[ks % 3] = *(const v4u*)(p0 + (ks + 3) * 1024); xb[ks % 3] = *(const v4u*)(p1 + (ks + 3) * 1024); }
                    SB0
                }
            }
        }
        if (pr == 0) polo = (wait_flags(rs, C_POL, 1, 1) - 1u) * 8192u;
        const unsigned so = segb(2 * pr + sp, polo);
        st_sc1(pack8(acc[0][0], acc[1][0]), rs, obase, so);
        st_sc1(pack8(acc[0][1], acc[1][1]), rs, obase + ostep, so);
        const bool drain = (pr < 4) || (pr == 31);
        if (drain) asm volatile("s_waitcnt vmcnt(0)" ::: "memory");
        else asm volatile("s_waitcnt vmcnt(2)" ::: "memory");
        bar();
        if (tid == 448) __builtin_amdgcn_raw_buffer_store_b32((unsigned)(drain ? 2 * pr + 2 : 2 * pr), rs, (unsigned)CTRL_OFF + C_GU + 128u * gw, 0, 16);
    }
}

DI void a0_prep(unsigned char* ws, const int u, const u16* __restrict__ wqkv, const u16* __restrict__ wphi, const u16* __restrict__ awo) {
    const int tid = threadIdx.x, lane = tid & 63, w = __builtin_amdgcn_readfirstlane(tid >> 6);
    const int rho = lane & 15, g = lane >> 4;
    __amdgpu_buffer_rsrc_t rs = mkrs(ws, 0x7fffffff);
    {
        const int which = w >> 2, h2 = u;
        v4u bf[2];
#pragma unroll
        for (int t = 0; t < 2; ++t) {
            const int f = perm32(t, rho);
            unsigned e[8];
#pragma unroll
            for (int jj = 0; jj < 8; ++jj) { const int d = 8 * g + jj; e[jj] = (d < 28 && f < 28) ? wphi[(h2 * 28 + d) * 28 + f] : 0u; }
            bf[t] = v4u{e[0] | (e[1] << 16), e[2] | (e[3] << 16), e[4] | (e[5] << 16), e[6] | (e[7] << 16)};
        }
        v4u af[4][2];
#pragma unroll
        for (int i = 0; i < 4; ++i) {
            const int ks = min((w & 3) + 4 * i, 13);
#pragma unroll
            for (int t = 0; t < 2; ++t) {
                const u16* p = wqkv + (size_t)(32 * ks + perm32(t, rho)) * 1344 + which * 448 + h2 * 28 + g * 8;
                v2u lo = *(const v2u*)p, hi = {0, 0};
                if (g < 3) hi = *(const v2u*)(p + 4);
                af[i][t] = v4u{lo.x, lo.y, hi.x, hi.y};
            }
        }
#pragma unroll
        for (int i = 0; i < 4; ++i) {
            const int ks = (w & 3) + 4 * i;
            if (ks < 14) {
#pragma unroll
                for (int t = 0; t < 2; ++t) {
                    f4 d0 = mfma(af[i][0], bf[t], f4{0, 0, 0, 0}), d1 = mfma(af[i][1], bf[t], f4{0, 0, 0, 0});
                    st_sc1(pack8(d0 * LOG2E, d1 * LOG2E), rs, (unsigned)(WQF_OFF + which * WQF_ONE + ((size_t)(h2 * 2 + t) * 14 + ks) * 1024) + lane * 16, 0);
                }
            }
        }
    }
#pragma unroll
    for (int i = 0; i < 4; ++i) {
        const int fi = w + 8 * i;
        if (fi < 28) {
            const int t = fi / 14, ks = fi % 14;
            const int d = 16 * t + rho;
            unsigned e[8], e2[8];
            const int cb = fi >> 1, t2 = fi & 1; const int c = 32 * cb + perm32(t2, rho);
#pragma unroll
            for (int jj = 0; jj < 8; ++jj) {
                e[jj] = d < 28 ? wqkv[(size_t)(32 * ks + 8 * g + jj) * 1344 + 896 + u * 28 + d] : 0u;
                const int sl = 8 * g + jj; e2[jj] = sl < 28 ? awo[(size_t)(u * 28 + sl) * 448 + c] : 0u;
            }
            st_sc1(v4u{e[0] | (e[1] << 16), e[2] | (e[3] << 16), e[4] | (e[5] << 16), e[6] | (e[7] << 16)}, rs, (unsigned)(WQF_OFF + 2 * WQF_ONE + (size_t)(u * 28 + fi) * 1024) + lane * 16, 0);
            st_sc1(v4u{e2[0] | (e2[1] << 16), e2[2] | (e2[3] << 16), e2[4] | (e2[5] << 16), e2[6] | (e2[7] << 16)}, rs, (unsigned)(WOF_OFF + (size_t)((cb * 2 + t2) * 16 + u) * 1024) + lane * 16, 0);
        }
    }
}
DI void phi_pair(f4 d0, f4 d1, int g, v4u& pos, v4u& neg) {
    float v[8] = {d0[0], d0[1], d0[2], d0[3], d1[0], d1[1], d1[2], d1[3]};
    const bool tail = (g == 3);
    float mx = fmaxf(fmaxf(v[0], v[1]), fmaxf(v[2], v[3])), mn = fminf(fminf(v[0], v[1]), fminf(v[2], v[3]));
    float mx2 = fmaxf(fmaxf(v[4], v[5]), fmaxf(v[6], v[7])), mn2 = fminf(fminf(v[4], v[5]), fminf(v[6], v[7]));
    mx = tail ? mx : fmaxf(mx, mx2); mn = tail ? mn : fminf(mn, mn2);
    mx = xmax32(xmax16(mx)); mn = -xmax32(xmax16(-mn));
    float ep[8], en[8], sp = 0.f, sn = 0.f;
#pragma unroll
    for (int k = 0; k < 8; ++k) {
        ep[k] = __builtin_amdgcn_exp2f(v[k] - mx); en[k] = __builtin_amdgcn_exp2f(mn - v[k]);
        if (k >= 4) { ep[k] = tail ? 0.f : ep[k]; en[k] = tail ? 0.f : en[k]; }
        sp += ep[k]; sn += en[k];
    }
    sp = xsum32(xsum16(sp)); sn = xsum32(xsum16(sn));
    const float rp = __builtin_amdgcn_rcpf(sp), rn = __builtin_amdgcn_rcpf(sn);
    pos = v4u{pk2(ep[0] * rp, ep[1] * rp), pk2(ep[2] * rp, ep[3] * rp), pk2(ep[4] * rp, ep[5] * rp), pk2(ep[6] * rp, ep[7] * rp)};
    neg = v4u{pk2(en[0] * rn, en[1] * rn), pk2(en[2] * rn, en[3] * rn), pk2(en[4] * rn, en[5] * rn), pk2(en[6] * rn, en[7] * rn)};
}
DI void stage_x64(const u16* __restrict__ ax, unsigned char* lds, int blk64) {
    int tid = threadIdx.x; asm volatile("" : "+v"(tid));
    const int lane = tid & 63, w = tid >> 6;
    v4u stg[7];
#pragma unroll
    for (int i = 0; i < 7; ++i) {
        const int f = i * 8 + w; const int q = f / 14, ks = f % 14;
        stg[i] = *(const v4u*)(ax + (size_t)(blk64 * 64 + 32 * (q >> 1) + perm32(q & 1, lane & 15)) * D + ks * 32 + (lane >> 4) * 8);
    }
#pragma unroll
    for (int i = 0; i < 7; ++i) *(v4u*)(lds + (i * 512 + tid) * 16) = stg[i];
}

DI void a1_unit(unsigned char* ws, unsigned char* lds, const int u, const u16* __restrict__ ax) {
    const int tid = threadIdx.x, lane = tid & 63, w = __builtin_amdgcn_readfirstlane(tid >> 6);
    const int rho = lane & 15, g = lane >> 4;
    __amdgpu_buffer_rsrc_t rs = mkrs(ws, 0x7fffffff);
    unsigned char* T = lds + 57344 + w * 9216;
    constexpr int TS = 144;
    f4 kv[2][4][2];
#pragma unroll
    for (int a = 0; a < 2; ++a)
#pragma unroll
        for (int b = 0; b < 4; ++b) { kv[a][b][0] = f4{0,0,0,0}; kv[a][b][1] = f4{0,0,0,0}; }
    for (int pass = 0; pass < 2; ++pass) {
        __syncthreads();
        stage_x64(ax, lds, 2 * u + pass);
        __syncthreads();
#pragma unroll
        for (int hi = 0; hi < 2; ++hi) {
            const int h = 2 * w + hi;
            const unsigned wk = (unsigned)(WQF_OFF + WQF_ONE + (size_t)h * 28672) + lane * 16;
            const unsigned wv = (unsigned)(WQF_OFF + 2 * WQF_ONE + (size_t)h * 28672) + lane * 16;
            f4 ak[2][4], av[2][4];
#pragma unroll
            for (int t = 0; t < 2; ++t)
#pragma unroll
                for (int q = 0; q < 4; ++q) { ak[t][q] = f4{0,0,0,0}; av[t][q] = f4{0,0,0,0}; }
#pragma unroll 2
            for (int ks = 0; ks < 14; ++ks) {
                v4u k0 = ld_sc1(rs, wk + ks * 1024, 0), k1 = ld_sc1(rs, wk + (14 + ks) * 1024, 0);
                v4u v0 = ld_sc1(rs, wv + ks * 1024, 0), v1 = ld_sc1(rs, wv + (14 + ks) * 1024, 0);
#pragma unroll
                for (int q = 0; q < 4; ++q) {
                    v4u x = *(const v4u*)(lds + (q * 14 + ks) * 1024 + lane * 16);
                    ak[0][q] = mfma(k0, x, ak[0][q]); ak[1][q] = mfma(k1, x, ak[1][q]);
                    av[0][q] = mfma(x, v0, av[0][q]); av[1][q] = mfma(x, v1, av[1][q]);
                }
            }
#pragma unroll
            for (int q = 0; q < 4; ++q) {
                v4u pos, neg; phi_pair(ak[0][q], ak[1][q], g, pos, neg);
                const int tok = 32 * (q >> 1) + perm32(q & 1, rho);
                *(v4u*)(T + tok * TS + g * 16) = pos;
                *(v4u*)(T + tok * TS + 64 + g * 16) = neg;
            }
            asm volatile("s_waitcnt lgkmcnt(0)" ::: "memory");
            v4u vf[2][2];
#pragma unroll
            for (int bk = 0; bk < 2; ++bk)
#pragma unroll
                for (int t = 0; t < 2; ++t) {
                    vf[bk][t] = pack8(av[t][2 * bk], av[t][2 * bk + 1]);
                    if (t == 1 && rho == 12) vf[bk][t] = v4u{0x3F803F80u, 0x3F803F80u, 0x3F803F80u, 0x3F803F80u};
                }
            const unsigned trb = (8 * g + (rho >> 2)) * TS + (lane & 3) * 8;
#pragma unroll
            for (int bk = 0; bk < 2; ++bk)
#pragma unroll
                for (int ft = 0; ft < 4; ++ft) {
                    v2u lo = trread(T + trb + (32 * bk) * TS + ft * 32), hi2 = trread(T + trb + (32 * bk + 4) * TS + ft * 32);
                    v4u pa = {lo.x, lo.y, hi2.x, hi2.y};
                    kv[hi][ft][0] = mfma(pa, vf[bk][0], kv[hi][ft][0]);
                    kv[hi][ft][1] = mfma(pa, vf[bk][1], kv[hi][ft][1]);
                }
            asm volatile("s_waitcnt lgkmcnt(0)" ::: "memory");
        }
    }
#pragma unroll
    for (int hi = 0; hi < 2; ++hi)
#pragma unroll
        for (int ft = 0; ft < 4; ++ft)
#pragma unroll
            for (int dt = 0; dt < 2; ++dt)
                st_sc1(__builtin_bit_cast(v4u, kv[hi][ft][dt]), rs, (unsigned)(KVP_OFF + ((size_t)(u * 16 + 2 * w + hi) * 8 + ft * 2 + dt) * 1024) + lane * 16, 0);
}
DI void a15_unit(unsigned char* ws, unsigned char* lds, const int h) {
    const int tid = threadIdx.x, lane = tid & 63, w = tid >> 6;
    __amdgpu_buffer_rsrc_t rs = mkrs(ws, 0x7fffffff);
    f4 s = {0, 0, 0, 0};
    v4u t[16];
#pragma unroll
    for (int u = 0; u < 16; ++u) t[u] = ld_sc1(rs, (unsigned)(KVP_OFF + ((size_t)(u * 16 + h) * 8) * 1024) + tid * 16, 0);
#pragma unroll
    for (int u = 0; u < 16; ++u) s += __builtin_bit_cast(f4, t[u]);
    const int ft = w >> 1, dt = w & 1, rho = lane & 15, g = lane >> 4;
    __syncthreads();
    v2u o; o.x = pk2(s[0], s[1]); o.y = pk2(s[2], s[3]);
    *(v2u*)(lds + (16 * dt + rho) * 128 + (16 * ft + 4 * g) * 2) = o;
    __syncthreads();
    if (tid < 256) {
        const int fr = tid >> 6;
        const int tt = fr >> 1, kb = fr & 1;
        v4u v = *(const v4u*)(lds + perm32(tt, rho) * 128 + (32 * kb + 8 * g) * 2);
        st_sc1(v, rs, (unsigned)(KVF_OFF + (size_t)(h * 4 + fr) * 1024) + lane * 16, 0);
    }
}
DI void a2_unit(unsigned char* ws, unsigned char* lds, const int u, u16* __restrict__ out, const u16* __restrict__ ax) {
    int tid = threadIdx.x; asm volatile("" : "+v"(tid));
    const int lane = tid & 63, w = __builtin_amdgcn_readfirstlane(tid >> 6);
    const int rho = lane & 15, g = lane >> 4;
    __amdgpu_buffer_rsrc_t rs = mkrs(ws, 0x7fffffff);
    unsigned char* O = lds + 57344;
    __syncthreads();
    stage_x64(ax, lds, u);
    wg_wait(rs, C_A0, 16, 4);
    v4u pos[2][4], neg[2][4];
#pragma unroll
    for (int hi = 0; hi < 2; ++hi) {
        const int h = 2 * w + hi;
        const unsigned wq = (unsigned)(WQF_OFF + (size_t)h * 28672) + lane * 16;
        f4 aq[2][4];
#pragma unroll
        for (int t = 0; t < 2; ++t)
#pragma unroll
            for (int q = 0; q < 4; ++q) aq[t][q] = f4{0,0,0,0};
#pragma unroll 2
        for (int ks = 0; ks < 14; ++ks) {
            v4u k0 = ld_sc1(rs, wq + ks * 1024, 0), k1 = ld_sc1(rs, wq + (14 + ks) * 1024, 0);
#pragma unroll
            for (int q = 0; q < 4; ++q) {
                v4u x = *(const v4u*)(lds + (q * 14 + ks) * 1024 + lane * 16);
                aq[0][q] = mfma(k0, x, aq[0][q]); aq[1][q] = mfma(k1, x, aq[1][q]);
            }
        }
#pragma unroll
        for (int q = 0; q < 4; ++q) phi_pair(aq[0][q], aq[1][q], g, pos[hi][q], neg[hi][q]);
    }
    wg_wait(rs, C_KV, 16, 4);
#pragma unroll
    for (int hi = 0; hi < 2; ++hi) {
        const int h = 2 * w + hi;
        v4u kf[4];
#pragma unroll
        for (int fr = 0; fr < 4; ++fr) kf[fr] = ld_sc1(rs, (unsigned)(KVF_OFF + (size_t)(h * 4 + fr) * 1024) + lane * 16, 0);
#pragma unroll
        for (int q = 0; q < 4; ++q) {
            f4 n0 = mfma(kf[0], pos[hi][q], f4{0,0,0,0}); n0 = mfma(kf[1], neg[hi][q], n0);
            f4 n1 = mfma(kf[2], pos[hi][q], f4{0,0,0,0}); n1 = mfma(kf[3], neg[hi][q], n1);
            const float den = asf((unsigned)__builtin_amdgcn_ds_bpermute((48 + rho) * 4, (int)asu(n1[0])));
            const float rd = 1.0f / (den + 1e-6f);
            if (g == 3) n1 = f4{0, 0, 0, 0};
            *(v4u*)(O + (h * 4 + q) * 1024 + lane * 16) = pack8(n0 * rd, n1 * rd);
        }
    }
    __syncthreads();
    for (int cb = w; cb < 14; cb += 8) {
        f4 acc[2][4];
#pragma unroll
        for (int t = 0; t < 2; ++t)
#pragma unroll
            for (int q = 0; q < 4; ++q) acc[t][q] = f4{0,0,0,0};
        const unsigned wo = (unsigned)(WOF_OFF + (size_t)cb * 32768) + lane * 16;
#pragma unroll 4
        for (int h = 0; h < 16; ++h) {
            v4u w0 = ld_sc1(rs, wo + h * 1024, 0), w1 = ld_sc1(rs, wo + (16 + h) * 1024, 0);
#pragma unroll
            for (int q = 0; q < 4; ++q) {
                v4u o = *(const v4u*)(O + (h * 4 + q) * 1024 + lane * 16);
                acc[0][q] = mfma(w0, o, acc[0][q]); acc[1][q] = mfma(w1, o, acc[1][q]);
            }
        }
#pragma unroll
        for (int q = 0; q < 4; ++q) {
            const int tok = u * 64 + 32 * (q >> 1) + perm32(q & 1, rho);
            *(v4u*)(out + (size_t)tok * OUTW + 32 * cb + 8 * g) = pack8(acc[0][q], acc[1][q]);
        }
    }
}

DI void p2_unit(unsigned char* ws, unsigned char* lds, const int unit, u16* __restrict__ out) {
    const int seg = unit >> 1, chalf = unit & 1;
    int tid = threadIdx.x; asm volatile("" : "+v"(tid));
    const int lane = tid & 63, w = __builtin_amdgcn_readfirstlane(tid >> 6);
    const int rho = lane & 15, g = lane >> 4;
    __amdgpu_buffer_rsrc_t rs = mkrs(ws, 0x7fffffff);
    unsigned char* xl = lds + 40960;
    __syncthreads();
    for (int i = tid; i < 1792; i += 512) *(v4u*)(xl + i * 16) = *(const v4u*)(ws + XF_OFF + (size_t)seg * 28672 + i * 16);
    if (tid < 64 && seg > 8) wait_flags(rs, C_REC, seg - 8, 16);
    if (tid < 128) wait_flags(rs, C_GU + 128u * (unsigned)min(tid, 111), seg + 1, 8);
    __syncthreads();
    const unsigned sgb = segb(seg, (__builtin_amdgcn_readfirstlane(ldflag(rs, C_POL)) - 1u) * 8192u);
    f4 sx[2][2][2];
#pragma unroll
    for (int hi = 0; hi < 2; ++hi) {
#pragma unroll
        for (int a = 0; a < 2; ++a) { sx[hi][a][0] = f4{0,0,0,0}; sx[hi][a][1] = f4{0,0,0,0}; }
        const unsigned gb = sgb + (unsigned)(2 * w + hi) * 65536u + lane * 16;
#pragma unroll
        for (int ks = 0; ks < 14; ++ks) {
            v4u g0 = ld_sc1(rs, gb + (ks >> 3) * 16384 + (ks & 7) * 1024, 0), g1 = ld_sc1(rs, gb + 32768 + (ks >> 3) * 16384 + (ks & 7) * 1024, 0);
            v4u x0 = *(const v4u*)(xl + ks * 1024 + lane * 16), x1 = *(const v4u*)(xl + (14 + ks) * 1024 + lane * 16);
            sx[hi][0][0] = mfma(g0, x0, sx[hi][0][0]); sx[hi][0][1] = mfma(g0, x1, sx[hi][0][1]);
            sx[hi][1][0] = mfma(g1, x0, sx[hi][1][0]); sx[hi][1][1] = mfma(g1, x1, sx[hi][1][1]);
        }
    }
    f4* sxl = (f4*)(lds + 69632) + tid;
#pragma unroll
    for (int hi = 0; hi < 2; ++hi)
#pragma unroll
        for (int a = 0; a < 2; ++a)
#pragma unroll
            for (int b2 = 0; b2 < 2; ++b2) sxl[((hi * 2 + a) * 2 + b2) * 512] = sx[hi][a][b2];
    SB0
    const int cb = 7 * chalf + (w < 7 ? w : 6);
    v4u uf[2][16];
    unsigned uo[2], vo[2];
#pragma unroll
    for (int t = 0; t < 2; ++t) {
        const int c = 32 * cb + perm32(t, rho); const int j = c >> 3, c8 = c & 7;
        uo[t] = sgb + (unsigned)(64 + j) * 16384u + (c8 + 16 * g) * 16;
        vo[t] = (unsigned)(VP_OFF + (size_t)(seg * 56 + j) * 2048) + g * 128 + c8 * 16;
#pragma unroll
        for (int h = 0; h < 16; ++h) uf[t][h] = ld_sc1(rs, uo[t] + (h >> 1) * 1024 + (h & 1) * 128, 0);
    }
    if (seg < 61) { if (tid < 64) wait_flags(rs, C_REC, seg + 1, 8); }
    else if (tid < 128) { wait_flags(rs, C_REC, 59, 8); wait_flags(rs, C_DN2 + 4u * (unsigned)min(tid, 111), 1, 1); }
    __syncthreads();
    f4 sm[2] = {f4{0,0,0,0}, f4{0,0,0,0}};
    v4u vf[2][4];
    {
        const unsigned gmb = (unsigned)(GM_OFF + (size_t)(seg * 16 + 2 * w + (rho >> 3)) * 7168) + g * 128 + (rho & 7) * 16;
        v4u gm[14];
#pragma unroll
        for (int ks = 0; ks < 14; ++ks) gm[ks] = ld_sc1(rs, gmb + ks * 512, 0);
#pragma unroll
        for (int t = 0; t < 2; ++t)
#pragma unroll
            for (int w4 = 0; w4 < 4; ++w4) vf[t][w4] = ld_sc1(rs, vo[t] + w4 * 512, 0);
        SB0
#pragma unroll
        for (int ks = 0; ks < 14; ++ks) {
            v4u x0 = *(const v4u*)(xl + ks * 1024 + lane * 16), x1 = *(const v4u*)(xl + (14 + ks) * 1024 + lane * 16);
            sm[0] = mfma(gm[ks], x0, sm[0]); sm[1] = mfma(gm[ks], x1, sm[1]);
            SB0
        }
    }
#pragma unroll
    for (int hi = 0; hi < 2; ++hi) {
        const int h = 2 * w + hi;
        const bool own = ((g >> 1) == hi);
#pragma unroll
        for (int tp = 0; tp < 2; ++tp) {
            const int tq = perm32(tp, rho);
            float v[8];
#pragma unroll
            for (int tau = 0; tau < 2; ++tau) {
                const f4 sv = sxl[((hi * 2 + tau) * 2 + tp) * 512];
#pragma unroll
                for (int r = 0; r < 4; ++r) { const int key = 16 * tau + 4 * g + r; v[4 * tau + r] = (key <= tq) ? sv[r] : -3.0e38f; }
            }
            float mx = fmaxf(fmaxf(fmaxf(v[0], v[1]), fmaxf(v[2], v[3])), fmaxf(fmaxf(v[4], v[5]), fmaxf(v[6], v[7])));
            const float mo = fmaxf(fmaxf(sm[tp][0], sm[tp][1]), fmaxf(sm[tp][2], sm[tp][3]));
            mx = own ? fmaxf(mx, mo) : mx;
            mx = xmax32(xmax16(mx));
            float sum = 0.f, em[4];
#pragma unroll
            for (int k = 0; k < 8; ++k) { v[k] = __builtin_amdgcn_exp2f(v[k] - mx); sum += v[k]; }
#pragma unroll
            for (int r = 0; r < 4; ++r) { em[r] = own ? __builtin_amdgcn_exp2f(sm[tp][r] - mx) : 0.f; sum += em[r]; }
            sum = xsum32(xsum16(sum));
            const float rz = __builtin_amdgcn_rcpf(sum);
            v2u p0; p0.x = pk2(v[0] * rz, v[1] * rz); p0.y = pk2(v[2] * rz, v[3] * rz);
            v2u p1; p1.x = pk2(v[4] * rz, v[5] * rz); p1.y = pk2(v[6] * rz, v[7] * rz);
            *(v2u*)(lds + (h * 2 + tp) * 1024 + (rho + 16 * (g >> 1)) * 16 + (g & 1) * 8) = p0;
            *(v2u*)(lds + (h * 2 + tp) * 1024 + (rho + 16 * (2 + (g >> 1))) * 16 + (g & 1) * 8) = p1;
            if (own) {
                v2u pm; pm.x = pk2(em[0] * rz, em[1] * rz); pm.y = pk2(em[2] * rz, em[3] * rz);
                *(v2u*)(lds + ((16 + (h >> 2)) * 2 + tp) * 1024 + (rho + 16 * (h & 3)) * 16 + (g & 1) * 8) = pm;
            }
        }
    }
    __syncthreads();
    if (w < 7) {
        f4 acc[2][2] = {{f4{0,0,0,0}, f4{0,0,0,0}}, {f4{0,0,0,0}, f4{0,0,0,0}}};
#pragma unroll
        for (int h = 0; h < 16; ++h) {
            v4u p0 = *(const v4u*)(lds + (h * 2) * 1024 + lane * 16), p1 = *(const v4u*)(lds + (h * 2 + 1) * 1024 + lane * 16);
#pragma unroll
            for (int t = 0; t < 2; ++t) { acc[t][0] = mfma(uf[t][h], p0, acc[t][0]); acc[t][1] = mfma(uf[t][h], p1, acc[t][1]); }
        }
#pragma unroll
        for (int w4 = 0; w4 < 4; ++w4) {
            v4u p0 = *(const v4u*)(lds + ((16 + w4) * 2) * 1024 + lane * 16), p1 = *(const v4u*)(lds + ((16 + w4) * 2 + 1) * 1024 + lane * 16);
#pragma unroll
            for (int t = 0; t < 2; ++t) { acc[t][0] = mfma(vf[t][w4], p0, acc[t][0]); acc[t][1] = mfma(vf[t][w4], p1, acc[t][1]); }
        }
#pragma unroll
        for (int tp = 0; tp < 2; ++tp) {
            const int tok = seg * 32 + perm32(tp, rho);
            *(v4u*)(out + (size_t)tok * OUTW + 448 + 32 * cb + 8 * g) = pack8(acc[0][tp], acc[1][tp]);
        }
    }
}

constexpr int LDS_MAIN = 135168 + 64;
__global__ void __launch_bounds__(512, 2) k_main(unsigned char* ws, u16* __restrict__ out, const u16* __restrict__ ax, const u16* __restrict__ wqkv, const u16* __restrict__ wphi, const u16* __restrict__ awo, int dS, int dV, int dVm, int pad_) {
    extern __shared__ __attribute__((aligned(16))) unsigned char lds[];
    const int bidx = blockIdx.x;
    const int idx = (bidx >> 3) * 4 + (bidx & 3);
#define PT(i)
    if ((bidx & 7) < 4) { rec_role(ws, lds, idx, dS, dV, dVm); return; }
    __amdgpu_buffer_rsrc_t rs = mkrs(ws, 0x7fffffff);
    if (idx < 112) gu_role(ws, lds, idx);
    else {
        const int u = idx - 112;
        a0_prep(ws, u, wqkv, wphi, awo);
        signal_add(ws, C_A0);
        wg_wait(rs, C_A0, 16, 2);
        a1_unit(ws, lds, u, ax);
        signal_add(ws, C_A1);
        wg_wait(rs, C_A1, 16, 4);
        a15_unit(ws, lds, u);
        signal_add(ws, C_KV);
    }
    PT(0)
    for (;;) {
        __syncthreads();
        if (threadIdx.x == 0) LDSV(LDS_MAIN - 16) = __hip_atomic_fetch_add((unsigned*)(ws + CTRL_OFF + C_TICKET), 1u, __ATOMIC_RELAXED, __HIP_MEMORY_SCOPE_AGENT);
        __syncthreads();
        const int t = __builtin_amdgcn_readfirstlane(LDSV(LDS_MAIN - 16));
        if (t >= 160) break;
        if (t < 32) a2_unit(ws, lds, t, out, ax);
        else p2_unit(ws, lds, t - 32, out);
        if (t < 32) { PT(1) } else { PT(2) }
    }
}

#define REC_DS (81 | (50 << 8))
#define REC_DV (74 | (1 << 8))
#define REC_DVM (6 | (12 << 8) | (1 << 16))
constexpr int g_dS = REC_DS, g_dV = REC_DV, g_dVm = REC_DVM;

extern "C" void kernel_launch(void* const* d_in, const int* in_sizes, int n_in, void* d_out, int out_size, void* d_ws, size_t ws_size, hipStream_t stream) {
    const u16* A_x = (const u16*)d_in[0]; const u16* A_wqkv = (const u16*)d_in[1]; const u16* A_wphi = (const u16*)d_in[2]; const u16* A_wo = (const u16*)d_in[3];
    const u16* B_x = (const u16*)d_in[4]; const u16* B_mem0 = (const u16*)d_in[5];
    const u16* Wq = (const u16*)d_in[6]; const u16* Wk = (const u16*)d_in[7]; const u16* Wv = (const u16*)d_in[8]; const u16* Wo = (const u16*)d_in[9];
    u16* out = (u16*)d_out;
    unsigned char* ws = (unsigned char*)(((uintptr_t)d_ws + 2 * MB - 1) & ~(uintptr_t)(2 * MB - 1));
    (void)hipFuncSetAttribute((const void*)k_pre, hipFuncAttributeMaxDynamicSharedMemorySize, 61440);
    (void)hipFuncSetAttribute((const void*)k_main, hipFuncAttributeMaxDynamicSharedMemorySize, LDS_MAIN);
    k_pre<<<256, 448, 61440, stream>>>(ws, Wq, Wk, Wv, Wo, B_x, B_mem0);
    k_main<<<256, 512, LDS_MAIN, stream>>>(ws, out, A_x, A_wqkv, A_wphi, A_wo, g_dS, g_dV, g_dVm, 0);
}
```
